```python
import torch

def _softmax(x, axis=-1):
    return torch.softmax(x, dim=axis)
def _gelu(x):
    return torch.nn.functional.gelu(x, approximate="tanh")
def _silu(x):
    return torch.nn.functional.silu(x)
def _sigmoid(x):
    return torch.sigmoid(x)
def _layernorm(x, axis=-1, eps=1e-5):
    m = x.mean(dim=axis, keepdim=True)
    v = x.var(dim=axis, keepdim=True, unbiased=False)
    return (x - m) / torch.sqrt(v + eps)
def _scatter_add(tgt, idx, src, axis=0):
    return tgt.clone().index_add_(axis, idx, src)
def _segment_sum(src, seg, n):
    out = torch.zeros((n,) + src.shape[1:], dtype=src.dtype)
    return out.index_add_(0, seg, src)
def _pad(x, pw):
    flat = tuple(v for pair in reversed(pw) for v in pair)
    return torch.nn.functional.pad(x, flat)

kernel_name = 'interleave_layers__linear_attn_block__softmax_full_mha__prompt'

def setup_inputs():

    _w = (lambda a: (lambda t: t.to(torch.bfloat16) if t.is_floating_point() else t)(torch.as_tensor(a)))
    def _A_setup():
        B, S, D = 1, 2048, 448
        return {
            "x": _w((lambda *s: torch.randn(*s, dtype=torch.bfloat16) * 0.05)(B, S, D)),
            "wqkv": _w((lambda *s: torch.randn(*s, dtype=torch.bfloat16) * 0.1)(D, 3 * D)),
            "wo": _w((lambda *s: torch.randn(*s, dtype=torch.bfloat16) * 0.05)(D, D)),
        }
    _A_in = _A_setup()

    def _B_setup():
        B, S, D = 1, 2048, 448
        return {
            "x": _w((lambda *s: torch.randn(*s, dtype=torch.bfloat16) * 0.05)(B, S, D)),
            "wqkv": _w((lambda *s: torch.randn(*s, dtype=torch.bfloat16) * 0.1)(D, 3 * D)),
            "wo": _w((lambda *s: torch.randn(*s, dtype=torch.bfloat16) * 0.05)(D, D)),
        }
    _B_in = _B_setup()

    return {
        "A_x": _A_in["x"],
        "A_wqkv": _A_in["wqkv"],
        "A_wo": _A_in["wo"],
        "B_x": _B_in["x"],
        "B_wqkv": _B_in["wqkv"],
        "B_wo": _B_in["wo"],
    }

def reference(A_x, A_wqkv, A_wo, B_x, B_wqkv, B_wo):
    def _A_ref(x, wqkv, wo):
        B, S, D = x.shape
        H = 16; Dh = D // H
        qkv = (lambda x, *d: x.permute(*d))((x @ wqkv).reshape(B, S, 3, H, Dh), 2, 0, 3, 1, 4)
        q, k, v = qkv[0], qkv[1], qkv[2]
        phi = lambda t: torch.where(t > 0, t + 1, torch.exp(t)) + 1e-6
        pq, pk = phi(q), phi(k)
        kv = torch.einsum("bhsd,bhse->bhde", pk, v)
        z = torch.einsum("bhsd->bhd", pk)
        num = torch.einsum("bhqd,bhde->bhqe", pq, kv)
        den = torch.einsum("bhqd,bhd->bhq", pq, z)[..., None] + 1e-6
        o = num / den
        return (lambda x, *d: x.permute(*d))(o, 0, 2, 1, 3).reshape(B, S, D) @ wo

    def _B_ref(x, wqkv, wo):
        B, S, D = x.shape
        H = 16; Dh = D // H
        qkv = (lambda x, *d: x.permute(*d))((x @ wqkv).reshape(B, S, 3, H, Dh), 2, 0, 3, 1, 4)
        q, k, v = qkv[0], qkv[1], qkv[2]
        scale = Dh ** -0.5
        scores = torch.einsum("bhqd,bhkd->bhqk", q, k) * scale
        p = _softmax(scores, axis=-1)
        o = torch.einsum("bhqk,bhkd->bhqd", p, v)
        return (lambda x, *d: x.permute(*d))(o, 0, 2, 1, 3).reshape(B, S, D) @ wo

    x = A_x
    x = x + _A_ref(_layernorm(x, axis=-1), A_wqkv, A_wo)
    x = x + _B_ref(_layernorm(x, axis=-1), B_wqkv, B_wo)
    x = x + _A_ref(_layernorm(x, axis=-1), A_wqkv, A_wo)
    x = x + _B_ref(_layernorm(x, axis=-1), B_wqkv, B_wo)
    return x
```

```cpp
#include <hip/hip_runtime.h>
#include <hip/hip_bf16.h>
typedef unsigned short u16;
constexpr int S=2048, D=448, H=16, DH=28;
__device__ __forceinline__ float bf2f(u16 b){ return __uint_as_float(((unsigned)b)<<16); }
__device__ __forceinline__ u16 f2bf(float f){ unsigned u=__float_as_uint(f); u += 0x7fff + ((u>>16)&1); return (u16)(u>>16); }

__global__ void k_ln(const u16* x, u16* y){
  int r=blockIdx.x; __shared__ float sh[448]; __shared__ float st[2];
  for(int i=threadIdx.x;i<D;i+=blockDim.x) sh[i]=bf2f(x[r*D+i]);
  __syncthreads();
  if(threadIdx.x==0){ float m=0; for(int i=0;i<D;i++) m+=sh[i]; m/=D; float v=0; for(int i=0;i<D;i++){float d=sh[i]-m; v+=d*d;} v/=D; st[0]=m; st[1]=1.0f/sqrtf(v+1e-5f);}
  __syncthreads();
  for(int i=threadIdx.x;i<D;i+=blockDim.x) y[r*D+i]=f2bf((sh[i]-st[0])*st[1]);
}
__global__ void k_gemm(const u16* A, const u16* B, const u16* res, u16* C, int N, int K){
  int r=blockIdx.x; int c=blockIdx.y*blockDim.x+threadIdx.x; if(c>=N) return;
  float acc=0; for(int k=0;k<K;k++) acc+=bf2f(A[r*K+k])*bf2f(B[k*N+c]);
  if(res) acc = bf2f(f2bf(acc)) + bf2f(res[r*N+c]);
  C[r*N+c]=f2bf(acc);
}
__device__ __forceinline__ float phi(float t){ return (t>0? t+1.0f : __expf(t)) + 1e-6f; }
__global__ void k_kv(const u16* qkv, float* kv){
  int h=blockIdx.x; int d=blockIdx.y; int e=threadIdx.x; if(e>DH) return;
  float acc=0; for(int s=0;s<S;s++){ float pk=phi(bf2f(qkv[s*3*D + D + h*DH + d])); float v = e<DH? bf2f(qkv[s*3*D+2*D+h*DH+e]) : 1.0f; acc+=pk*v; }
  kv[(h*DH+d)*32+e]=acc;
}
__global__ void k_lin(const u16* qkv, const float* kv, u16* o){
  int s=blockIdx.x; int c=threadIdx.x; if(c>=D) return; int h=c/DH, e=c%DH;
  float num=0, den=0; for(int d=0;d<DH;d++){ float pq=phi(bf2f(qkv[s*3*D+h*DH+d])); num+=pq*kv[(h*DH+d)*32+e]; den+=pq*kv[(h*DH+d)*32+DH]; }
  o[s*D+c]=f2bf(num/(den+1e-6f));
}
__global__ void k_attn(const u16* qkv, u16* o){
  int s=blockIdx.x*64+threadIdx.x; int h=blockIdx.y;
  float q[DH]; for(int d=0;d<DH;d++) q[d]=bf2f(qkv[s*3*D+h*DH+d])*0.18898223650461362f;
  float m=-1e30f,l=0; float acc[DH]; for(int d=0;d<DH;d++) acc[d]=0;
  for(int j=0;j<S;j++){ float sc=0;
    #pragma unroll
    for(int d=0;d<DH;d++) sc+=q[d]*bf2f(qkv[j*3*D+D+h*DH+d]);
    float mn=fmaxf(m,sc); float a=__expf(m-mn), p=__expf(sc-mn); l=l*a+p;
    #pragma unroll
    for(int d=0;d<DH;d++) acc[d]=acc[d]*a+p*bf2f(qkv[j*3*D+2*D+h*DH+d]); m=mn; }
  for(int d=0;d<DH;d++) o[s*D+h*DH+d]=f2bf(acc[d]/l);
}
extern "C" void kernel_launch(void* const* d_in, const int* in_sizes, int n_in, void* d_out, int out_size, void* d_ws, size_t ws_size, hipStream_t stream){
  const u16* x0=(const u16*)d_in[0]; const u16* Aw=(const u16*)d_in[1]; const u16* Ao=(const u16*)d_in[2]; const u16* Bw=(const u16*)d_in[4]; const u16* Bo=(const u16*)d_in[5];
  char* ws=(char*)d_ws; u16* ln=(u16*)ws; u16* qkv=(u16*)(ws+(4<<20)); u16* o=(u16*)(ws+(12<<20)); float* kv=(float*)(ws+(16<<20)); u16* xa=(u16*)(ws+(20<<20)); u16* xb=(u16*)(ws+(24<<20));
  const u16* x=x0; u16* outs[4]={xa,xb,xa,(u16*)d_out};
  for(int l=0;l<4;l++){
    k_ln<<<S,64,0,stream>>>(x,ln);
    k_gemm<<<dim3(S,21),64,0,stream>>>(ln,(l&1)?Bw:Aw,nullptr,qkv,3*D,D);
    if(!(l&1)){ k_kv<<<dim3(H,DH),64,0,stream>>>(qkv,kv); k_lin<<<S,448,0,stream>>>(qkv,kv,o);} else { k_attn<<<dim3(S/64,H),64,0,stream>>>(qkv,o);}
    k_gemm<<<dim3(S,7),64,0,stream>>>(o,(l&1)?Bo:Ao,x,outs[l],D,D);
    x=outs[l];
  }
}
```

```cpp
#include <hip/hip_runtime.h>

typedef unsigned short u16;
typedef unsigned long long u64;
typedef __bf16 bf16x2 __attribute__((ext_vector_type(2)));
typedef __bf16 bf16x8 __attribute__((ext_vector_type(8)));
typedef __bf16 bf16x16 __attribute__((ext_vector_type(16)));
typedef float f32x2 __attribute__((ext_vector_type(2)));
typedef float f32x4 __attribute__((ext_vector_type(4)));
typedef float f32x8 __attribute__((ext_vector_type(8)));
typedef float f32x16 __attribute__((ext_vector_type(16)));
typedef unsigned u32x2 __attribute__((ext_vector_type(2)));
typedef unsigned u32x4 __attribute__((ext_vector_type(4)));
typedef short s16x4 __attribute__((ext_vector_type(4)));
#define LAS __attribute__((address_space(3)))
#define GAS __attribute__((address_space(1)))
#define DI __device__ __forceinline__
#define RLX_AGENT __ATOMIC_RELAXED, __HIP_MEMORY_SCOPE_AGENT

constexpr int SEQ = 2048, DM = 448, NH = 16, DH = 28, NKS = 28;
constexpr int WS_FLAG = 0;
constexpr int WS_EPOCH = 131072;
constexpr int TILE32 = NKS * 1024;
constexpr int ACT = 64 * TILE32;
constexpr int WS_X = 1 << 18;
constexpr int WS_O = WS_X + 3 * ACT;
constexpr int WS_KV = WS_O + 4 * ACT;
constexpr int KVSZ = 16 * 64 * 4096;
constexpr int WS_KP = WS_KV + 2 * KVSZ;
constexpr int KPSZ = 16 * 16 * 4096;
constexpr int WS_ST = WS_KP + 2 * KPSZ;
constexpr int STSZ = 64 * 16 * 256;
constexpr int WS_END = WS_ST + 3 * STSZ;
constexpr int IMG = 448 * 56;
constexpr int L_Q = 0, L_K = IMG, L_V = 2 * IMG, L_W = 3 * IMG;
constexpr int L_S = 4 * IMG;
constexpr int L_QX = L_S + 49152;
constexpr int L_CS = L_QX + 8192;
constexpr int L_RS = L_CS + 3072;
constexpr int L_MISC = L_RS + 1024;
constexpr int LDS_BYTES = L_MISC + 256;
static_assert(LDS_BYTES <= 163840, "lds");
constexpr int AUX_SC1 = 16;

struct Params { const u16* x0; const u16* wa; const u16* woa; const u16* wb; const u16* wob; u16* out; char* ws; };

DI f32x16 mfma(u32x4 a, u32x4 b, f32x16 c) { return __builtin_amdgcn_mfma_f32_32x32x16_bf16(__builtin_bit_cast(bf16x8, a), __builtin_bit_cast(bf16x8, b), c, 0, 0, 0); }
DI unsigned pk(float lo, float hi) { f32x2 v = {lo, hi}; return __builtin_bit_cast(unsigned, __builtin_convertvector(v, bf16x2)); }
DI float rbf(float x) { return __uint_as_float(pk(x, x) & 0xffff0000u); }
DI float bflo(unsigned u) { return __uint_as_float(u << 16); }
DI float bfhi(unsigned u) { return __uint_as_float(u & 0xffff0000u); }
DI u32x4 pack8(const f32x16& v, int o) { u32x4 r = {pk(v[o], v[o + 1]), pk(v[o + 2], v[o + 3]), pk(v[o + 4], v[o + 5]), pk(v[o + 6], v[o + 7])}; return r; }
DI float xsum(float v) { auto r = __builtin_amdgcn_permlane32_swap(__float_as_uint(v), __float_as_uint(v), false, false); return __uint_as_float(r[0]) + __uint_as_float(r[1]); }
DI float xmax(float v) { auto r = __builtin_amdgcn_permlane32_swap(__float_as_uint(v), __float_as_uint(v), false, false); return fmaxf(__uint_as_float(r[0]), __uint_as_float(r[1])); }
DI float xhi(float v) { auto r = __builtin_amdgcn_permlane32_swap(__float_as_uint(v), __float_as_uint(v), false, false); return __uint_as_float(r[1]); }
DI u32x4 wfrag(LAS char* p, int s) {
  s16x4 a = __builtin_amdgcn_ds_read_tr16_b64_v4i16((LAS s16x4*)(p + s * 896));
  s16x4 b = __builtin_amdgcn_ds_read_tr16_b64_v4i16((LAS s16x4*)(p + s * 896 + 224));
  u32x2 x = __builtin_bit_cast(u32x2, a), y = __builtin_bit_cast(u32x2, b);
  u32x4 r = {x[0], x[1], y[0], y[1]}; return r;
}
DI void bar() { asm volatile("s_waitcnt lgkmcnt(0)\n\ts_barrier" ::: "memory"); }
DI void drain() { asm volatile("s_waitcnt vmcnt(0)" ::: "memory"); }

typedef __amdgpu_buffer_rsrc_t rsrc_t;
template <int AUX> DI u32x4 ld16(rsrc_t rs, int voff, int soff, int k) { return __builtin_amdgcn_raw_buffer_load_b128(rs, voff + (k & 4095), soff + (k & ~4095), AUX); }
template <int AUX> DI u32x2 ld8(rsrc_t rs, int voff, int soff, int k) { return __builtin_amdgcn_raw_buffer_load_b64(rs, voff + (k & 4095), soff + (k & ~4095), AUX); }
DI rsrc_t mkrs(const void* p, int bytes) { return __builtin_amdgcn_make_buffer_rsrc((void*)p, 0, bytes, 0x00020000); }
DI int opaque(int v) { asm volatile("" : "+v"(v)); return v; }
DI int opaque_s(int v) { asm volatile("" : "+s"(v)); return v; }
#define SB() __builtin_amdgcn_sched_barrier(0)
struct Ctx {
  int rb, h, w, lane, r, hh;
  char* ws; __amdgpu_buffer_rsrc_t rs; u64 tag; LAS char* L;
};

DI void signal(const Ctx& c, int kind, int seq) {
  drain(); bar();
  if (threadIdx.x < 16) {
    const int g = kind ? c.h : c.rb, me = kind ? c.rb : c.h;
    GAS u64* f = (GAS u64*)(c.ws + WS_FLAG + ((kind * 16 + g) * 16 + c.lane) * 256) + me;
    (void)__hip_atomic_exchange(f, c.tag + seq, RLX_AGENT);
  }
}
DI void await(const Ctx& c, int kind, int seq) {
  if (c.w == 3) {
    const int g = kind ? c.h : c.rb, me = kind ? c.rb : c.h;
    GAS u64* f = (GAS u64*)(c.ws + WS_FLAG + ((kind * 16 + g) * 16 + me) * 256) + (c.lane & 15);
    const u64 want = c.tag + seq;
    for (unsigned spins = 0; spins < (1u << 18); ++spins) {
      const u64 v = __hip_atomic_load(f, RLX_AGENT);
      if (__all((v - want) < 64ull)) break;
    }
  }
  __builtin_amdgcn_fence(__ATOMIC_ACQUIRE, "wavefront");
  bar();
}

DI void load_img(const Ctx& c, const u16* W, int ld, int col0, int img) {
  const int lane = opaque(c.lane);
  if (lane < 56) {
    const int rr = lane / 7, g = lane - rr * 7;
    const rsrc_t wr = mkrs(W, 448 * ld * 2);
    const int voff = (rr * ld + 4 * g) * 2, soff = opaque_s((8 * c.w * ld + col0) * 2);
    LAS char* dst = c.L + img + 8 * c.w * 56 + lane * 8;
    u32x2 v[14];
#pragma unroll
    for (int i = 0; i < 14; ++i) v[i] = __builtin_amdgcn_raw_buffer_load_b64(wr, voff, soff + i * 32 * ld * 2, 0);
#pragma unroll
    for (int i = 0; i < 14; ++i) *(LAS u32x2*)(dst + i * 32 * 56) = v[i];
  }
}

#define STAMP(i)

DI float phi(float t) { return (t > 0.f ? t + 1.0f : __expf(t)) + 1e-6f; }

DI void store_slice(const Ctx& c, int base, const f32x16& v) {
  const int p0 = (7 * c.h + (c.h & 1)) >> 1;
  const int rowoff = base + (c.rb * 4 + c.w) * TILE32 + c.r * 16;
  __builtin_amdgcn_raw_buffer_store_b128(pack8(v, 0), c.rs, rowoff + (p0 + c.hh) * 512, 0, AUX_SC1);
  const u32x4 hi = pack8(v, 8);
  if (c.hh == 0) __builtin_amdgcn_raw_buffer_store_b128(hi, c.rs, rowoff + (p0 + 2) * 512, 0, AUX_SC1);
  else { u32x2 t = {hi[0], hi[1]}; __builtin_amdgcn_raw_buffer_store_b64(t, c.rs, rowoff + ((c.h & 1) ? (p0 - 1) * 512 + 8 : (p0 + 3) * 512), 0, AUX_SC1); }
}

template <bool SOFTMAX>
DI void layer(Ctx c, const Params& p, int it, u32x4& xa, u32x4& xb2) {
  const int L = it * 2 + (SOFTMAX ? 1 : 0);
  c.lane = opaque(c.lane); c.r = c.lane & 31; c.hh = c.lane >> 5;
  c.w = opaque_s(c.w); c.h = opaque_s(c.h); c.rb = opaque_s(c.rb);
  const int lane = c.lane, r = c.r, hh = c.hh, w = c.w;
  const int G = ((lane >> 4) & 1) * 4 + (lane & 3), q4 = (lane & 15) >> 2;
  const int rowb = (8 * hh + q4) * 56;
  const int natg = (c.h & 1) ? (G == 5 ? 0 : (G == 0 ? 1 : G == 2 ? 2 : G == 1 ? 3 : G == 3 ? 4 : G == 4 ? 5 : 6))
                             : (G == 5 ? 6 : (G == 0 ? 0 : G == 2 ? 1 : G == 1 ? 2 : G == 3 ? 3 : G == 4 ? 4 : 5));
  const int padoff = 8 * hh * 56 + 224 + 8 * q4;
  LAS char* pid = c.L + (G == 7 ? padoff : rowb + G * 8);
  LAS char* pnat = c.L + (G == 7 ? padoff : rowb + natg * 8);

  u32x4 xf[NKS];
  float s1 = 0.f, s2 = 0.f;
  if (L == 0) {
    const rsrc_t xr = mkrs(p.x0, SEQ * DM * 2);
#pragma unroll
    for (int s = 0; s < NKS; ++s) xf[s] = ld16<0>(xr, r * 896 + hh * 16, (c.rb * 128 + w * 32) * 896, s * 32);
    load_img(c, p.wa, 3 * DM, DM + c.h * DH, L_K);
    load_img(c, p.wa, 3 * DM, 2 * DM + c.h * DH, L_V);
    load_img(c, p.wa, 3 * DM, c.h * DH, L_Q);
    load_img(c, p.woa, DM, c.h * DH, L_W);
    const bf16x2 one = __builtin_bit_cast(bf16x2, opaque(0x3f803f80));
#pragma unroll
    for (int s = 0; s < NKS; ++s)
#pragma unroll
      for (int j = 0; j < 4; ++j) { const unsigned e = xf[s][j]; const bf16x2 v = __builtin_bit_cast(bf16x2, e); s1 = __builtin_amdgcn_fdot2_f32_bf16(v, one, s1, false); s2 = __builtin_amdgcn_fdot2_f32_bf16(v, v, s2, false); }
    bar();
  } else {
    await(c, 0, 3 * L);
    STAMP(L * 12 + 0);
    const int xb = WS_X + (L - 1) * ACT + (c.rb * 4 + w) * TILE32;
    const int sb = WS_ST + (L - 1) * STSZ + (c.rb * 4 + w) * 4096;
    u32x2 st[8];
#pragma unroll
    for (int j = 0; j < 8; ++j) st[j] = ld8<AUX_SC1>(c.rs, hh * 2048 + r * 8, sb, j * 256);
#pragma unroll
    for (int s = 0; s < NKS; ++s) xf[s] = ld16<AUX_SC1>(c.rs, lane * 16, xb, s * 1024);
#pragma unroll
    for (int j = 0; j < 8; ++j) { s1 += __uint_as_float(st[j][0]); s2 += __uint_as_float(st[j][1]); }
  }
  s1 = xsum(s1); s2 = xsum(s2);
  const float mu = s1 * (1.0f / DM);
  const float rstd = __builtin_amdgcn_rsqf(fmaxf(s2 * (1.0f / DM) - mu * mu, 0.f) + 1e-5f);
  const float mr = mu * rstd;
  LAS float* rsl = (LAS float*)(c.L + L_RS) + w * 64;
  if (hh == 0) rsl[r] = rstd; else rsl[32 + r] = mr;

  LAS float* csl = (LAS float*)(c.L + L_CS) + (SOFTMAX ? 384 : 0);
  if (it == 0) {
    const u32x4 ones = {0x3f803f80u, 0x3f803f80u, 0x3f803f80u, 0x3f803f80u};
    f32x16 a0 = {}, a1 = {}, a2 = {};
#pragma unroll
    for (int s = 0; s < 7; ++s) {
      a0 = mfma(ones, wfrag(pid + L_Q + w * 7 * 896, s), a0);
      a1 = mfma(ones, wfrag(pid + L_K + w * 7 * 896, s), a1);
      a2 = mfma(ones, wfrag(pnat + L_V + w * 7 * 896, s), a2);
    }
    if (hh == 0) { csl[w * 96 + r] = a0[0]; csl[w * 96 + 32 + r] = a1[0]; csl[w * 96 + 64 + r] = a2[0]; }
    bar();
  }

  f32x16 ak = {}, av = {};
#pragma unroll
  for (int s = 0; s < NKS; ++s) {
    const u32x4 kf = wfrag(pid + L_K, s), vf = wfrag(pnat + L_V, s);
    if (SOFTMAX) ak = mfma(kf, xf[s], ak); else ak = mfma(xf[s], kf, ak);
    av = mfma(xf[s], vf, av);
  }
  STAMP(L * 12 + 1);
  f32x16 rr, rm;
#pragma unroll
  for (int g = 0; g < 4; ++g) {
    const f32x4 a = *(LAS f32x4*)(rsl + 8 * g + 4 * hh), b = *(LAS f32x4*)(rsl + 32 + 8 * g + 4 * hh);
#pragma unroll
    for (int j = 0; j < 4; ++j) { rr[4 * g + j] = a[j]; rm[4 * g + j] = b[j]; }
  }
  const float csv = (csl[64 + r] + csl[96 + 64 + r]) + (csl[192 + 64 + r] + csl[288 + 64 + r]);
#pragma unroll
  for (int i = 0; i < 16; ++i) { const float v = av[i] * rr[i] - rm[i] * csv; av[i] = r < 28 ? v : (r == 28 ? 1.0f : 0.f); }
  const u32x4 vf0 = pack8(av, 0), vf1 = pack8(av, 8);
  u32x4 kf0, kf1;
  if (SOFTMAX) {
#pragma unroll
    for (int g = 0; g < 4; ++g) {
      const LAS float* cp = csl + 32 + 8 * g + 4 * hh;
      const f32x4 cs = (*(LAS f32x4*)cp + *(LAS f32x4*)(cp + 96)) + (*(LAS f32x4*)(cp + 192) + *(LAS f32x4*)(cp + 288));
#pragma unroll
      for (int j = 0; j < 4; ++j) ak[4 * g + j] = ak[4 * g + j] * rstd - mr * cs[j];
    }
    if (hh) { ak[12] = 1.0f; ak[13] = 1.0f; ak[14] = 1.0f; ak[15] = 0.f; }
    kf0 = pack8(ak, 0); kf1 = pack8(ak, 8);
    const int tb = WS_KV + it * KVSZ + (c.h * 64 + c.rb * 4 + w) * 4096;
    const int rs23 = (r & 19) | ((r & 4) << 1) | ((r & 8) >> 1);
    __builtin_amdgcn_raw_buffer_store_b128(kf0, c.rs, tb + (hh * 32 + rs23) * 16, 0, AUX_SC1);
    __builtin_amdgcn_raw_buffer_store_b128(kf1, c.rs, tb + 1024 + (hh * 32 + rs23) * 16, 0, AUX_SC1);
    __builtin_amdgcn_raw_buffer_store_b128(vf0, c.rs, tb + 2048 + lane * 16, 0, AUX_SC1);
    __builtin_amdgcn_raw_buffer_store_b128(vf1, c.rs, tb + 3072 + lane * 16, 0, AUX_SC1);
  } else {
    const float csk = (csl[32 + r] + csl[96 + 32 + r]) + (csl[192 + 32 + r] + csl[288 + 32 + r]);
#pragma unroll
    for (int i = 0; i < 16; ++i) { const float v = phi(rbf(ak[i] * rr[i] - rm[i] * csk)); ak[i] = r < 28 ? v : 0.f; }
    f32x16 kv = mfma(pack8(ak, 0), vf0, f32x16{});
    kv = mfma(pack8(ak, 8), vf1, kv);
    LAS f32x4* ex = (LAS f32x4*)(c.L + L_S);
#pragma unroll
    for (int g = 0; g < 4; ++g) { const f32x4 t = {kv[4 * g], kv[4 * g + 1], kv[4 * g + 2], kv[4 * g + 3]}; ex[(g * 4 + w) * 64 + lane] = t; }
    bar();
    const f32x4 t = (ex[(w * 4 + 0) * 64 + lane] + ex[(w * 4 + 1) * 64 + lane]) + (ex[(w * 4 + 2) * 64 + lane] + ex[(w * 4 + 3) * 64 + lane]);
    __builtin_amdgcn_raw_buffer_store_b128(__builtin_bit_cast(u32x4, t), c.rs, WS_KP + it * KPSZ + (c.h * 16 + c.rb) * 4096 + w * 1024 + lane * 16, 0, AUX_SC1);
  }
  STAMP(L * 12 + 2);
  signal(c, 1, 3 * L + 1);
  STAMP(L * 12 + 3);

  f32x16 aq = {};
#pragma unroll
  for (int s = 0; s < NKS; ++s) aq = mfma(wfrag(pid + L_Q, s), xf[s], aq);
#pragma unroll
  for (int g = 0; g < 4; ++g) {
    const LAS float* cp = csl + 8 * g + 4 * hh;
    const f32x4 cs = (*(LAS f32x4*)cp + *(LAS f32x4*)(cp + 96)) + (*(LAS f32x4*)(cp + 192) + *(LAS f32x4*)(cp + 288));
#pragma unroll
    for (int j = 0; j < 4; ++j) aq[4 * g + j] = aq[4 * g + j] * rstd - mr * cs[j];
  }
  f32x16 o;
  if (!SOFTMAX) {
#pragma unroll
    for (int i = 0; i < 16; ++i) aq[i] = phi(rbf(aq[i]));
    if (hh) { aq[12] = 0.f; aq[13] = 0.f; aq[14] = 0.f; aq[15] = 0.f; }
    const u32x4 q0 = pack8(aq, 0), q1 = pack8(aq, 8);
    STAMP(L * 12 + 4);
    await(c, 1, 3 * L + 1);
    STAMP(L * 12 + 5);
    u32x4 pt[16];
    const int pb = WS_KP + it * KPSZ + c.h * 16 * 4096 + w * 1024;
#pragma unroll
    for (int j = 0; j < 16; ++j) pt[j] = ld16<AUX_SC1>(c.rs, lane * 16, pb, j * 4096);
    f32x4 t = {};
#pragma unroll
    for (int j = 0; j < 16; ++j) t += __builtin_bit_cast(f32x4, pt[j]);
    LAS u32x2* ex = (LAS u32x2*)(c.L + L_QX);
    { const u32x2 v = {pk(t[0], t[1]), pk(t[2], t[3])}; ex[((w >> 1) * 64 + lane) * 2 + (w & 1)] = v; }
    bar();
    const u32x4 a0 = *(LAS u32x4*)(c.L + L_QX + lane * 16), a1 = *(LAS u32x4*)(c.L + L_QX + 1024 + lane * 16);
    o = mfma(a0, q0, f32x16{});
    o = mfma(a1, q1, o);
    const float rd = 1.0f / (xhi(o[12]) + 1e-6f);
#pragma unroll
    for (int i = 0; i < 16; ++i) o[i] *= rd;
    STAMP(L * 12 + 6);
    store_slice(c, WS_O + L * ACT, o);
  } else {
    const float qs = 0.18898223650461362f * 1.4426950408889634f;
#pragma unroll
    for (int i = 0; i < 16; ++i) aq[i] *= qs;
    if (hh) { aq[12] = 0.f; aq[13] = 0.f; aq[14] = 0.f; aq[15] = 0.f; }
    const u32x4 q0 = pack8(aq, 0); u32x4 q1 = pack8(aq, 8);
    f32x16 s0 = mfma(kf0, q0, f32x16{});
    s0 = mfma(kf1, q1, s0);
    float m = s0[0];
#pragma unroll
    for (int i = 1; i < 16; ++i) m = fmaxf(m, s0[i]);
    m = xmax(m);
    LAS u32x4* qx = (LAS u32x4*)(c.L + L_QX);
    LAS unsigned* badw = (LAS unsigned*)(c.L + L_MISC);
    STAMP(L * 12 + 4);
    const int kb = WS_KV + it * KVSZ + (c.h * 64 + w * 16) * 4096;
    for (int pass = 0;; ++pass) {
      { const float m0 = rbf(m), m1 = rbf(m - m0), m2 = rbf((m - m0) - m1);
        if (hh) { q1[2] = pk(-m0, -m1); q1[3] = pk(-m2, 0.f); } }
      qx[(w * 2) * 64 + lane] = q0; qx[(w * 2 + 1) * 64 + lane] = q1;
      if (threadIdx.x == 0) *badw = 0u;
      if (pass == 0) await(c, 1, 3 * L + 1); else bar();
      STAMP(L * 12 + 5);
      u32x4 qf[8];
#pragma unroll
      for (int j = 0; j < 8; ++j) qf[j] = qx[j * 64 + lane];
      f32x16 oa[4] = {};
      u32x4 kv[4], kn[4];
#pragma unroll
      for (int j = 0; j < 4; ++j) kv[j] = ld16<AUX_SC1>(c.rs, lane * 16, kb, j * 1024);
      for (int t = 0; t < 16; ++t) {
        const int tn = t < 15 ? t + 1 : t;
#pragma unroll
        for (int j = 0; j < 4; ++j) kn[j] = ld16<AUX_SC1>(c.rs, lane * 16, kb + tn * 4096, j * 1024);
#pragma unroll
        for (int b = 0; b < 4; ++b) {
          f32x16 s = mfma(kv[0], qf[2 * b], f32x16{});
          s = mfma(kv[1], qf[2 * b + 1], s);
          f32x8 p0, p1;
#pragma unroll
          for (int i = 0; i < 8; ++i) { p0[i] = __builtin_amdgcn_exp2f(s[i]); p1[i] = __builtin_amdgcn_exp2f(s[8 + i]); }
          oa[b] = __builtin_amdgcn_smfmac_f32_32x32x32_bf16(__builtin_bit_cast(bf16x8, kv[2]), __builtin_bit_cast(bf16x16, p0), oa[b], 0xdddd, 0, 0);
          oa[b] = __builtin_amdgcn_smfmac_f32_32x32x32_bf16(__builtin_bit_cast(bf16x8, kv[3]), __builtin_bit_cast(bf16x16, p1), oa[b], 0xdddd, 0, 0);
        }
#pragma unroll
        for (int j = 0; j < 4; ++j) kv[j] = kn[j];
      }
      STAMP(L * 12 + 6);
      LAS f32x4* ex = (LAS f32x4*)(c.L + L_S);
#pragma unroll
      for (int b = 0; b < 4; ++b) {
        if (b != w) {
          const int slot = b * 3 + (w > b ? w - 1 : w);
#pragma unroll
          for (int g = 0; g < 4; ++g) { const f32x4 t = {oa[b][4 * g], oa[b][4 * g + 1], oa[b][4 * g + 2], oa[b][4 * g + 3]}; ex[(slot * 4 + g) * 64 + lane] = t; }
        } else o = oa[b];
      }
      bar();
#pragma unroll
      for (int k = 0; k < 3; ++k)
#pragma unroll
        for (int g = 0; g < 4; ++g) { const f32x4 t = ex[((w * 3 + k) * 4 + g) * 64 + lane];
#pragma unroll
          for (int j = 0; j < 4; ++j) o[4 * g + j] += t[j]; }
      const float l = xhi(o[12]);
      const float rl = 1.0f / l;
      float chk = 0.f;
#pragma unroll
      for (int i = 0; i < 16; ++i) { chk = __builtin_fmaf(o[i], 0.f, chk); o[i] *= rl; }
      const bool bad = !(l < 1.0e30f) || !(chk == 0.f);
      store_slice(c, WS_O + L * ACT, o);
      if (pass) break;
      if (__any(bad) && lane == 0) *badw = 1u;
      bar();
      if (*badw == 0u) break;
      float mm[4] = {-3.0e38f, -3.0e38f, -3.0e38f, -3.0e38f};
#pragma unroll
      for (int b = 0; b < 4; ++b) if (hh) { qf[2 * b + 1][2] = 0u; qf[2 * b + 1][3] = 0u; }
      for (int t = 0; t < 16; ++t) {
        const u32x4 k0 = ld16<AUX_SC1>(c.rs, lane * 16, kb + t * 4096, 0), k1 = ld16<AUX_SC1>(c.rs, lane * 16, kb + t * 4096, 1024);
#pragma unroll
        for (int b = 0; b < 4; ++b) {
          f32x16 s = mfma(k0, qf[2 * b], f32x16{});
          s = mfma(k1, qf[2 * b + 1], s);
#pragma unroll
          for (int i = 0; i < 16; ++i) mm[b] = fmaxf(mm[b], s[i]);
        }
      }
      LAS float* mx = (LAS float*)(c.L + L_S);
#pragma unroll
      for (int b = 0; b < 4; ++b) mx[(b * 4 + w) * 64 + lane] = mm[b];
      bar();
      m = fmaxf(fmaxf(mx[(w * 4) * 64 + lane], mx[(w * 4 + 1) * 64 + lane]), fmaxf(mx[(w * 4 + 2) * 64 + lane], mx[(w * 4 + 3) * 64 + lane]));
      m = xmax(m);
      bar();
    }
  }
  STAMP(L * 12 + 7);
  signal(c, 0, 3 * L + 2);
  if (L < 3) {
    const u16* wn = SOFTMAX ? p.wa : p.wb;
    load_img(c, wn, 3 * DM, DM + c.h * DH, L_K);
    load_img(c, wn, 3 * DM, 2 * DM + c.h * DH, L_V);
    load_img(c, wn, 3 * DM, c.h * DH, L_Q);
  }
  const int p0 = (7 * c.h + (c.h & 1)) >> 1;
  if (L == 0) {
    const GAS char* src = (const GAS char*)p.x0 + (c.rb * 128 + w * 32 + r) * 896;
    xa = *(const GAS u32x4*)(src + (p0 + hh) * 16);
    if (hh == 0) xb2 = *(const GAS u32x4*)(src + (p0 + 2) * 16);
    else { const u32x2 t = *(const GAS u32x2*)(src + ((c.h & 1) ? (p0 - 1) * 16 + 8 : (p0 + 3) * 16)); xb2[0] = t[0]; xb2[1] = t[1]; xb2[2] = 0u; xb2[3] = 0u; }
  }
  STAMP(L * 12 + 8);
  await(c, 0, 3 * L + 2);
  STAMP(L * 12 + 9);

  {
    const int ob = WS_O + L * ACT + (c.rb * 4 + w) * TILE32;
    u32x4 of[NKS];
#pragma unroll
    for (int s = 0; s < NKS; ++s) of[s] = ld16<AUX_SC1>(c.rs, lane * 16, ob, s * 1024);
    f32x16 y = {};
#pragma unroll
    for (int s = 0; s < NKS; ++s) y = mfma(wfrag(pnat + L_W, s), of[s], y);
    float t1 = 0.f, t2 = 0.f;
    f32x16 xs;
#pragma unroll
    for (int j = 0; j < 4; ++j) { xs[2 * j] = bflo(xa[j]); xs[2 * j + 1] = bfhi(xa[j]); xs[8 + 2 * j] = bflo(xb2[j]); xs[8 + 2 * j + 1] = bfhi(xb2[j]); }
#pragma unroll
    for (int i = 0; i < 16; ++i) { float v = rbf(xs[i] + rbf(y[i])); if (i >= 12 && hh) v = 0.f; xs[i] = v; t1 += v; t2 = __builtin_fmaf(v, v, t2); }
    xa = pack8(xs, 0); xb2 = pack8(xs, 8);
    STAMP(L * 12 + 10);
    if (L < 3) {
      store_slice(c, WS_X + L * ACT, xs);
      t1 = xsum(t1); t2 = xsum(t2);
      if (hh == 0) { const u32x2 t = {__float_as_uint(t1), __float_as_uint(t2)}; __builtin_amdgcn_raw_buffer_store_b64(t, c.rs, WS_ST + L * STSZ + (c.rb * 4 + w) * 4096 + c.h * 256 + r * 8, 0, AUX_SC1); }
      signal(c, 0, 3 * L + 3);
      load_img(c, SOFTMAX ? p.woa : p.wob, DM, c.h * DH, L_W);
    } else {
      GAS char* dst = (GAS char*)p.out + (c.rb * 128 + w * 32 + r) * 896;
      *(GAS u32x4*)(dst + (p0 + hh) * 16) = pack8(xs, 0);
      const u32x4 hi = pack8(xs, 8);
      if (hh == 0) *(GAS u32x4*)(dst + (p0 + 2) * 16) = hi;
      else { const u32x2 t = {hi[0], hi[1]}; *(GAS u32x2*)(dst + ((c.h & 1) ? (p0 - 1) * 16 + 8 : (p0 + 3) * 16)) = t; }
    }
    STAMP(L * 12 + 11);
  }
}

__global__ void __launch_bounds__(256, 1) fused_kernel(Params p) {
  extern __shared__ __attribute__((aligned(16))) char smem[];
  Ctx c;
  c.L = (LAS char*)smem; c.ws = p.ws;
  c.lane = threadIdx.x & 63; c.r = c.lane & 31; c.hh = c.lane >> 5; c.w = __builtin_amdgcn_readfirstlane(threadIdx.x >> 6);
  { const int x = blockIdx.x & 7, j = blockIdx.x >> 3; c.rb = (x & 3) * 4 + (j & 3); c.h = (x >> 2) * 8 + (j >> 2); }
  c.rs = __builtin_amdgcn_make_buffer_rsrc(p.ws, 0, WS_END, 0x00020000);
  STAMP(0);
  const u64 ev = __hip_atomic_load((GAS u64*)(p.ws + WS_EPOCH), RLX_AGENT);
  const u64 epoch = ((u64)(unsigned)__builtin_amdgcn_readfirstlane((int)(ev >> 32)) << 32) | (unsigned)__builtin_amdgcn_readfirstlane((int)ev);
  c.tag = epoch * 0x9E3779B97F4A7C15ull + 0x632BE59BD9B4E019ull;
  u32x4 xa = {}, xb = {};
  for (int it = 0; it < 2; ++it) {
    layer<false>(c, p, it, xa, xb);
    layer<true>(c, p, it, xa, xb);
  }
  if (blockIdx.x == 0 && threadIdx.x == 0) __hip_atomic_store((GAS u64*)(p.ws + WS_EPOCH), epoch + 1, RLX_AGENT);
}

extern "C" void kernel_launch(void* const* d_in, const int* in_sizes, int n_in, void* d_out, int out_size, void* d_ws, size_t ws_size, hipStream_t stream) {
  Params p;
  p.x0 = (const u16*)d_in[0]; p.wa = (const u16*)d_in[1]; p.woa = (const u16*)d_in[2];
  p.wb = (const u16*)d_in[4]; p.wob = (const u16*)d_in[5]; p.out = (u16*)d_out; p.ws = (char*)d_ws;
  (void)hipFuncSetAttribute((const void*)fused_kernel, hipFuncAttributeMaxDynamicSharedMemorySize, LDS_BYTES);
  hipLaunchKernelGGL(fused_kernel, dim3(256), dim3(256), LDS_BYTES, stream, p);
}
```

```cpp
#include <hip/hip_runtime.h>
#define CODEPF 1

typedef unsigned short u16;
typedef unsigned long long u64;
typedef __bf16 bf16x2 __attribute__((ext_vector_type(2)));
typedef __bf16 bf16x8 __attribute__((ext_vector_type(8)));
typedef __bf16 bf16x16 __attribute__((ext_vector_type(16)));
typedef float f32x2 __attribute__((ext_vector_type(2)));
typedef float f32x4 __attribute__((ext_vector_type(4)));
typedef float f32x8 __attribute__((ext_vector_type(8)));
typedef float f32x16 __attribute__((ext_vector_type(16)));
typedef unsigned u32x2 __attribute__((ext_vector_type(2)));
typedef unsigned u32x4 __attribute__((ext_vector_type(4)));
typedef short s16x4 __attribute__((ext_vector_type(4)));
#define LAS __attribute__((address_space(3)))
#define GAS __attribute__((address_space(1)))
#define DI __device__ __forceinline__
#define RLX_AGENT __ATOMIC_RELAXED, __HIP_MEMORY_SCOPE_AGENT

constexpr int SEQ = 2048, DM = 448, NH = 16, DH = 28, NKS = 28;
constexpr int WS_FLAG = 0;
constexpr int WS_EPOCH = 131072;
constexpr int TILE32 = NKS * 1024;
constexpr int ACT = 64 * TILE32;
constexpr int WS_X = 1 << 18;
constexpr int WS_O = WS_X + 3 * ACT;
constexpr int WS_KV = WS_O + 4 * ACT;
constexpr int KVSZ = 16 * 64 * 4096;
constexpr int WS_KP = WS_KV + 2 * KVSZ;
constexpr int KPSZ = 16 * 16 * 4096;
constexpr int WS_ST = WS_KP + 2 * KPSZ;
constexpr int STSZ = 64 * 16 * 256;
constexpr int WS_END = WS_ST + 3 * STSZ;
constexpr int IMG = 448 * 56;
constexpr int L_Q = 0, L_K = IMG, L_V = 2 * IMG, L_W = 3 * IMG;
constexpr int L_S = 4 * IMG;
constexpr int L_QX = L_S + 49152;
constexpr int L_CS = L_QX + 8192;
constexpr int L_RS = L_CS + 3072;
constexpr int L_MISC = L_RS + 1024;
constexpr int LDS_BYTES = L_MISC + 256;
static_assert(LDS_BYTES <= 163840, "lds");
constexpr int AUX_SC1 = 16;

struct Params { const u16* x0; const u16* wa; const u16* woa; const u16* wb; const u16* wob; u16* out; char* ws; };

DI f32x16 mfma(u32x4 a, u32x4 b, f32x16 c) { return __builtin_amdgcn_mfma_f32_32x32x16_bf16(__builtin_bit_cast(bf16x8, a), __builtin_bit_cast(bf16x8, b), c, 0, 0, 0); }
DI unsigned pk(float lo, float hi) { f32x2 v = {lo, hi}; return __builtin_bit_cast(unsigned, __builtin_convertvector(v, bf16x2)); }
DI float rbf(float x) { return __uint_as_float(pk(x, x) & 0xffff0000u); }
DI float bflo(unsigned u) { return __uint_as_float(u << 16); }
DI float bfhi(unsigned u) { return __uint_as_float(u & 0xffff0000u); }
DI u32x4 pack8(const f32x16& v, int o) { u32x4 r = {pk(v[o], v[o + 1]), pk(v[o + 2], v[o + 3]), pk(v[o + 4], v[o + 5]), pk(v[o + 6], v[o + 7])}; return r; }
DI float xsum(float v) { auto r = __builtin_amdgcn_permlane32_swap(__float_as_uint(v), __float_as_uint(v), false, false); return __uint_as_float(r[0]) + __uint_as_float(r[1]); }
DI float xmax(float v) { auto r = __builtin_amdgcn_permlane32_swap(__float_as_uint(v), __float_as_uint(v), false, false); return fmaxf(__uint_as_float(r[0]), __uint_as_float(r[1])); }
DI float xhi(float v) { auto r = __builtin_amdgcn_permlane32_swap(__float_as_uint(v), __float_as_uint(v), false, false); return __uint_as_float(r[1]); }
DI u32x4 wfrag(LAS char* p, int s) {
  s16x4 a = __builtin_amdgcn_ds_read_tr16_b64_v4i16((LAS s16x4*)(p + s * 896));
  s16x4 b = __builtin_amdgcn_ds_read_tr16_b64_v4i16((LAS s16x4*)(p + s * 896 + 224));
  u32x2 x = __builtin_bit_cast(u32x2, a), y = __builtin_bit_cast(u32x2, b);
  u32x4 r = {x[0], x[1], y[0], y[1]}; return r;
}
DI void bar() { asm volatile("s_waitcnt lgkmcnt(0)\n\ts_barrier" ::: "memory"); }
DI void drain() { asm volatile("s_waitcnt vmcnt(0)" ::: "memory"); }

typedef __amdgpu_buffer_rsrc_t rsrc_t;
template <int AUX> DI u32x4 ld16(rsrc_t rs, int voff, int soff, int k) { return __builtin_amdgcn_raw_buffer_load_b128(rs, voff + (k & 4095), soff + (k & ~4095), AUX); }
template <int AUX> DI u32x2 ld8(rsrc_t rs, int voff, int soff, int k) { return __builtin_amdgcn_raw_buffer_load_b64(rs, voff + (k & 4095), soff + (k & ~4095), AUX); }
DI rsrc_t mkrs(const void* p, int bytes) { return __builtin_amdgcn_make_buffer_rsrc((void*)p, 0, bytes, 0x00020000); }
DI int opaque(int v) { asm volatile("" : "+v"(v)); return v; }
DI int opaque_s(int v) { asm volatile("" : "+s"(v)); return v; }
#define SB() __builtin_amdgcn_sched_barrier(0)
struct Ctx {
  int rb, h, w, lane, r, hh;
  char* ws; __amdgpu_buffer_rsrc_t rs; u64 tag; LAS char* L;
};

DI void signal(const Ctx& c, int kind, int seq) {
  drain(); bar();
  if (threadIdx.x < 16) {
    const int g = kind ? c.h : c.rb, me = kind ? c.rb : c.h;
    GAS u64* f = (GAS u64*)(c.ws + WS_FLAG + ((kind * 16 + g) * 16 + c.lane) * 256) + me;
    (void)__hip_atomic_exchange(f, c.tag + seq, RLX_AGENT);
  }
}
DI void await(const Ctx& c, int kind, int seq) {
  if (c.w == 3) {
    const int g = kind ? c.h : c.rb, me = kind ? c.rb : c.h;
    GAS u64* f = (GAS u64*)(c.ws + WS_FLAG + ((kind * 16 + g) * 16 + me) * 256) + (c.lane & 15);
    const u64 want = c.tag + seq;
    for (unsigned spins = 0; spins < (1u << 18); ++spins) {
      const u64 v = __hip_atomic_load(f, RLX_AGENT);
      if (__all((v - want) < 64ull)) break;
    }
  }
  __builtin_amdgcn_fence(__ATOMIC_ACQUIRE, "wavefront");
  bar();
}

DI void load_img(const Ctx& c, const u16* W, int ld, int col0, int img) {
  const int lane = opaque(c.lane);
  if (lane < 56) {
    const int rr = lane / 7, g = lane - rr * 7;
    const rsrc_t wr = mkrs(W, 448 * ld * 2);
    const int voff = (rr * ld + 4 * g) * 2, soff = opaque_s((8 * c.w * ld + col0) * 2);
    LAS char* dst = c.L + img + 8 * c.w * 56 + lane * 8;
    u32x2 v[14];
#pragma unroll
    for (int i = 0; i < 14; ++i) v[i] = __builtin_amdgcn_raw_buffer_load_b64(wr, voff, soff + i * 32 * ld * 2, 0);
#pragma unroll
    for (int i = 0; i < 14; ++i) *(LAS u32x2*)(dst + i * 32 * 56) = v[i];
  }
}

#define STAMP(i)

DI float phi(float t) { return (t > 0.f ? t + 1.0f : __expf(t)) + 1e-6f; }

DI void store_slice(const Ctx& c, int base, const f32x16& v) {
  const int p0 = (7 * c.h + (c.h & 1)) >> 1;
  const int rowoff = base + (c.rb * 4 + c.w) * TILE32 + c.r * 16;
  __builtin_amdgcn_raw_buffer_store_b128(pack8(v, 0), c.rs, rowoff + (p0 + c.hh) * 512, 0, AUX_SC1);
  const u32x4 hi = pack8(v, 8);
  if (c.hh == 0) __builtin_amdgcn_raw_buffer_store_b128(hi, c.rs, rowoff + (p0 + 2) * 512, 0, AUX_SC1);
  else { u32x2 t = {hi[0], hi[1]}; __builtin_amdgcn_raw_buffer_store_b64(t, c.rs, rowoff + ((c.h & 1) ? (p0 - 1) * 512 + 8 : (p0 + 3) * 512), 0, AUX_SC1); }
}

template <bool SOFTMAX>
DI void layer(Ctx c, const Params& p, int it, u32x4& xa, u32x4& xb2) {
  const int L = it * 2 + (SOFTMAX ? 1 : 0);
  c.lane = opaque(c.lane); c.r = c.lane & 31; c.hh = c.lane >> 5;
  c.w = opaque_s(c.w); c.h = opaque_s(c.h); c.rb = opaque_s(c.rb);
  const int lane = c.lane, r = c.r, hh = c.hh, w = c.w;
  const int G = ((lane >> 4) & 1) * 4 + (lane & 3), q4 = (lane & 15) >> 2;
  const int rowb = (8 * hh + q4) * 56;
  const int natg = (c.h & 1) ? (G == 5 ? 0 : (G == 0 ? 1 : G == 2 ? 2 : G == 1 ? 3 : G == 3 ? 4 : G == 4 ? 5 : 6))
                             : (G == 5 ? 6 : (G == 0 ? 0 : G == 2 ? 1 : G == 1 ? 2 : G == 3 ? 3 : G == 4 ? 4 : 5));
  const int padoff = 8 * hh * 56 + 224 + 8 * q4;
  LAS char* pid = c.L + (G == 7 ? padoff : rowb + G * 8);
  LAS char* pnat = c.L + (G == 7 ? padoff : rowb + natg * 8);

  u32x4 xf[NKS];
  float s1 = 0.f, s2 = 0.f;
  if (L == 0) {
    const rsrc_t xr = mkrs(p.x0, SEQ * DM * 2);
#pragma unroll
    for (int s = 0; s < NKS; ++s) xf[s] = ld16<0>(xr, r * 896 + hh * 16, (c.rb * 128 + w * 32) * 896, s * 32);
    load_img(c, p.wa, 3 * DM, DM + c.h * DH, L_K);
    load_img(c, p.wa, 3 * DM, 2 * DM + c.h * DH, L_V);
    load_img(c, p.wa, 3 * DM, c.h * DH, L_Q);
    load_img(c, p.woa, DM, c.h * DH, L_W);
    const bf16x2 one = __builtin_bit_cast(bf16x2, opaque(0x3f803f80));
#pragma unroll
    for (int s = 0; s < NKS; ++s)
#pragma unroll
      for (int j = 0; j < 4; ++j) { const unsigned e = xf[s][j]; const bf16x2 v = __builtin_bit_cast(bf16x2, e); s1 = __builtin_amdgcn_fdot2_f32_bf16(v, one, s1, false); s2 = __builtin_amdgcn_fdot2_f32_bf16(v, v, s2, false); }
    bar();
  } else {
    await(c, 0, 3 * L);
    STAMP(L * 12 + 0);
    const int xb = WS_X + (L - 1) * ACT + (c.rb * 4 + w) * TILE32;
    const int sb = WS_ST + (L - 1) * STSZ + (c.rb * 4 + w) * 4096;
    u32x2 st[8];
#pragma unroll
    for (int j = 0; j < 8; ++j) st[j] = ld8<AUX_SC1>(c.rs, hh * 2048 + r * 8, sb, j * 256);
#pragma unroll
    for (int s = 0; s < NKS; ++s) xf[s] = ld16<AUX_SC1>(c.rs, lane * 16, xb, s * 1024);
#pragma unroll
    for (int j = 0; j < 8; ++j) { s1 += __uint_as_float(st[j][0]); s2 += __uint_as_float(st[j][1]); }
  }
  s1 = xsum(s1); s2 = xsum(s2);
  const float mu = s1 * (1.0f / DM);
  const float rstd = __builtin_amdgcn_rsqf(fmaxf(s2 * (1.0f / DM) - mu * mu, 0.f) + 1e-5f);
  const float mr = mu * rstd;
  LAS float* rsl = (LAS float*)(c.L + L_RS) + w * 64;
  if (hh == 0) rsl[r] = rstd; else rsl[32 + r] = mr;

  LAS float* csl = (LAS float*)(c.L + L_CS) + (SOFTMAX ? 384 : 0);
  if (it == 0) {
    const u32x4 ones = {0x3f803f80u, 0x3f803f80u, 0x3f803f80u, 0x3f803f80u};
    f32x16 a0 = {}, a1 = {}, a2 = {};
#pragma unroll
    for (int s = 0; s < 7; ++s) {
      a0 = mfma(ones, wfrag(pid + L_Q + w * 7 * 896, s), a0);
      a1 = mfma(ones, wfrag(pid + L_K + w * 7 * 896, s), a1);
      a2 = mfma(ones, wfrag(pnat + L_V + w * 7 * 896, s), a2);
    }
    if (hh == 0) { csl[w * 96 + r] = a0[0]; csl[w * 96 + 32 + r] = a1[0]; csl[w * 96 + 64 + r] = a2[0]; }
    bar();
  }

  f32x16 ak = {}, av = {};
#pragma unroll
  for (int s = 0; s < NKS; ++s) {
    const u32x4 kf = wfrag(pid + L_K, s), vf = wfrag(pnat + L_V, s);
    if (SOFTMAX) ak = mfma(kf, xf[s], ak); else ak = mfma(xf[s], kf, ak);
    av = mfma(xf[s], vf, av);
  }
  STAMP(L * 12 + 1);
  f32x16 rr, rm;
#pragma unroll
  for (int g = 0; g < 4; ++g) {
    const f32x4 a = *(LAS f32x4*)(rsl + 8 * g + 4 * hh), b = *(LAS f32x4*)(rsl + 32 + 8 * g + 4 * hh);
#pragma unroll
    for (int j = 0; j < 4; ++j) { rr[4 * g + j] = a[j]; rm[4 * g + j] = b[j]; }
  }
  const float csv = (csl[64 + r] + csl[96 + 64 + r]) + (csl[192 + 64 + r] + csl[288 + 64 + r]);
#pragma unroll
  for (int i = 0; i < 16; ++i) { const float v = av[i] * rr[i] - rm[i] * csv; av[i] = r < 28 ? v : (r == 28 ? 1.0f : 0.f); }
  const u32x4 vf0 = pack8(av, 0), vf1 = pack8(av, 8);
  u32x4 kf0, kf1;
  if (SOFTMAX) {
#pragma unroll
    for (int g = 0; g < 4; ++g) {
      const LAS float* cp = csl + 32 + 8 * g + 4 * hh;
      const f32x4 cs = (*(LAS f32x4*)cp + *(LAS f32x4*)(cp + 96)) + (*(LAS f32x4*)(cp + 192) + *(LAS f32x4*)(cp + 288));
#pragma unroll
      for (int j = 0; j < 4; ++j) ak[4 * g + j] = ak[4 * g + j] * rstd - mr * cs[j];
    }
    if (hh) { ak[12] = 1.0f; ak[13] = 1.0f; ak[14] = 1.0f; ak[15] = 0.f; }
    kf0 = pack8(ak, 0); kf1 = pack8(ak, 8);
    const int tb = WS_KV + it * KVSZ + (c.h * 64 + c.rb * 4 + w) * 4096;
    const int rs23 = (r & 19) | ((r & 4) << 1) | ((r & 8) >> 1);
    __builtin_amdgcn_raw_buffer_store_b128(kf0, c.rs, tb + (hh * 32 + rs23) * 16, 0, AUX_SC1);
    __builtin_amdgcn_raw_buffer_store_b128(kf1, c.rs, tb + 1024 + (hh * 32 + rs23) * 16, 0, AUX_SC1);
    __builtin_amdgcn_raw_buffer_store_b128(vf0, c.rs, tb + 2048 + lane * 16, 0, AUX_SC1);
    __builtin_amdgcn_raw_buffer_store_b128(vf1, c.rs, tb + 3072 + lane * 16, 0, AUX_SC1);
  } else {
    const float csk = (csl[32 + r] + csl[96 + 32 + r]) + (csl[192 + 32 + r] + csl[288 + 32 + r]);
#pragma unroll
    for (int i = 0; i < 16; ++i) { const float v = phi(rbf(ak[i] * rr[i] - rm[i] * csk)); ak[i] = r < 28 ? v : 0.f; }
    f32x16 kv = mfma(pack8(ak, 0), vf0, f32x16{});
    kv = mfma(pack8(ak, 8), vf1, kv);
    LAS f32x4* ex = (LAS f32x4*)(c.L + L_S);
#pragma unroll
    for (int g = 0; g < 4; ++g) { const f32x4 t = {kv[4 * g], kv[4 * g + 1], kv[4 * g + 2], kv[4 * g + 3]}; ex[(g * 4 + w) * 64 + lane] = t; }
    bar();
    const f32x4 t = (ex[(w * 4 + 0) * 64 + lane] + ex[(w * 4 + 1) * 64 + lane]) + (ex[(w * 4 + 2) * 64 + lane] + ex[(w * 4 + 3) * 64 + lane]);
    __builtin_amdgcn_raw_buffer_store_b128(__builtin_bit_cast(u32x4, t), c.rs, WS_KP + it * KPSZ + (c.h * 16 + c.rb) * 4096 + w * 1024 + lane * 16, 0, AUX_SC1);
  }
  STAMP(L * 12 + 2);
  signal(c, 1, 3 * L + 1);
  STAMP(L * 12 + 3);

  f32x16 aq = {};
#pragma unroll
  for (int s = 0; s < NKS; ++s) aq = mfma(wfrag(pid + L_Q, s), xf[s], aq);
#pragma unroll
  for (int g = 0; g < 4; ++g) {
    const LAS float* cp = csl + 8 * g + 4 * hh;
    const f32x4 cs = (*(LAS f32x4*)cp + *(LAS f32x4*)(cp + 96)) + (*(LAS f32x4*)(cp + 192) + *(LAS f32x4*)(cp + 288));
#pragma unroll
    for (int j = 0; j < 4; ++j) aq[4 * g + j] = aq[4 * g + j] * rstd - mr * cs[j];
  }
  f32x16 o;
  if (!SOFTMAX) {
#pragma unroll
    for (int i = 0; i < 16; ++i) aq[i] = phi(rbf(aq[i]));
    if (hh) { aq[12] = 0.f; aq[13] = 0.f; aq[14] = 0.f; aq[15] = 0.f; }
    const u32x4 q0 = pack8(aq, 0), q1 = pack8(aq, 8);
    STAMP(L * 12 + 4);
    await(c, 1, 3 * L + 1);
    STAMP(L * 12 + 5);
    u32x4 pt[16];
    const int pb = WS_KP + it * KPSZ + c.h * 16 * 4096 + w * 1024;
#pragma unroll
    for (int j = 0; j < 16; ++j) pt[j] = ld16<AUX_SC1>(c.rs, lane * 16, pb, j * 4096);
    f32x4 t = {};
#pragma unroll
    for (int j = 0; j < 16; ++j) t += __builtin_bit_cast(f32x4, pt[j]);
    LAS u32x2* ex = (LAS u32x2*)(c.L + L_QX);
    { const u32x2 v = {pk(t[0], t[1]), pk(t[2], t[3])}; ex[((w >> 1) * 64 + lane) * 2 + (w & 1)] = v; }
    bar();
    const u32x4 a0 = *(LAS u32x4*)(c.L + L_QX + lane * 16), a1 = *(LAS u32x4*)(c.L + L_QX + 1024 + lane * 16);
    o = mfma(a0, q0, f32x16{});
    o = mfma(a1, q1, o);
    const float rd = 1.0f / (xhi(o[12]) + 1e-6f);
#pragma unroll
    for (int i = 0; i < 16; ++i) o[i] *= rd;
    STAMP(L * 12 + 6);
    store_slice(c, WS_O + L * ACT, o);
  } else {
    const float qs = 0.18898223650461362f * 1.4426950408889634f;
#pragma unroll
    for (int i = 0; i < 16; ++i) aq[i] *= qs;
    if (hh) { aq[12] = 0.f; aq[13] = 0.f; aq[14] = 0.f; aq[15] = 0.f; }
    const u32x4 q0 = pack8(aq, 0); u32x4 q1 = pack8(aq, 8);
    f32x16 s0 = mfma(kf0, q0, f32x16{});
    s0 = mfma(kf1, q1, s0);
    float m = s0[0];
#pragma unroll
    for (int i = 1; i < 16; ++i) m = fmaxf(m, s0[i]);
    m = xmax(m);
    LAS u32x4* qx = (LAS u32x4*)(c.L + L_QX);
    LAS unsigned* badw = (LAS unsigned*)(c.L + L_MISC);
    STAMP(L * 12 + 4);
    const int kb = WS_KV + it * KVSZ + (c.h * 64 + w * 16) * 4096;
    for (int pass = 0;; ++pass) {
      { const float m0 = rbf(m), m1 = rbf(m - m0), m2 = rbf((m - m0) - m1);
        if (hh) { q1[2] = pk(-m0, -m1); q1[3] = pk(-m2, 0.f); } }
      qx[(w * 2) * 64 + lane] = q0; qx[(w * 2 + 1) * 64 + lane] = q1;
      if (threadIdx.x == 0) *badw = 0u;
      if (pass == 0) await(c, 1, 3 * L + 1); else bar();
      STAMP(L * 12 + 5);
      u32x4 qf[8];
#pragma unroll
      for (int j = 0; j < 8; ++j) qf[j] = qx[j * 64 + lane];
      f32x16 oa[4] = {};
      u32x4 kv[4], kn[4];
#pragma unroll
      for (int j = 0; j < 4; ++j) kv[j] = ld16<AUX_SC1>(c.rs, lane * 16, kb, j * 1024);
      for (int t = 0; t < 16; ++t) {
        const int tn = t < 15 ? t + 1 : t;
#pragma unroll
        for (int j = 0; j < 4; ++j) kn[j] = ld16<AUX_SC1>(c.rs, lane * 16, kb + tn * 4096, j * 1024);
#pragma unroll
        for (int b = 0; b < 4; ++b) {
          f32x16 s = mfma(kv[0], qf[2 * b], f32x16{});
          s = mfma(kv[1], qf[2 * b + 1], s);
          f32x8 p0, p1;
#pragma unroll
          for (int i = 0; i < 8; ++i) { p0[i] = __builtin_amdgcn_exp2f(s[i]); p1[i] = __builtin_amdgcn_exp2f(s[8 + i]); }
          oa[b] = __builtin_amdgcn_smfmac_f32_32x32x32_bf16(__builtin_bit_cast(bf16x8, kv[2]), __builtin_bit_cast(bf16x16, p0), oa[b], 0xdddd, 0, 0);
          oa[b] = __builtin_amdgcn_smfmac_f32_32x32x32_bf16(__builtin_bit_cast(bf16x8, kv[3]), __builtin_bit_cast(bf16x16, p1), oa[b], 0xdddd, 0, 0);
        }
#pragma unroll
        for (int j = 0; j < 4; ++j) kv[j] = kn[j];
      }
      STAMP(L * 12 + 6);
      LAS f32x4* ex = (LAS f32x4*)(c.L + L_S);
#pragma unroll
      for (int b = 0; b < 4; ++b) {
        if (b != w) {
          const int slot = b * 3 + (w > b ? w - 1 : w);
#pragma unroll
          for (int g = 0; g < 4; ++g) { const f32x4 t = {oa[b][4 * g], oa[b][4 * g + 1], oa[b][4 * g + 2], oa[b][4 * g + 3]}; ex[(slot * 4 + g) * 64 + lane] = t; }
        } else o = oa[b];
      }
      bar();
#pragma unroll
      for (int k = 0; k < 3; ++k)
#pragma unroll
        for (int g = 0; g < 4; ++g) { const f32x4 t = ex[((w * 3 + k) * 4 + g) * 64 + lane];
#pragma unroll
          for (int j = 0; j < 4; ++j) o[4 * g + j] += t[j]; }
      const float l = xhi(o[12]);
      const float rl = 1.0f / l;
      float chk = 0.f;
#pragma unroll
      for (int i = 0; i < 16; ++i) { chk = __builtin_fmaf(o[i], 0.f, chk); o[i] *= rl; }
      const bool bad = !(l < 1.0e30f) || !(chk == 0.f);
      store_slice(c, WS_O + L * ACT, o);
      if (pass) break;
      if (__any(bad) && lane == 0) *badw = 1u;
      bar();
      if (*badw == 0u) break;
      float mm[4] = {-3.0e38f, -3.0e38f, -3.0e38f, -3.0e38f};
#pragma unroll
      for (int b = 0; b < 4; ++b) if (hh) { qf[2 * b + 1][2] = 0u; qf[2 * b + 1][3] = 0u; }
      for (int t = 0; t < 16; ++t) {
        const u32x4 k0 = ld16<AUX_SC1>(c.rs, lane * 16, kb + t * 4096, 0), k1 = ld16<AUX_SC1>(c.rs, lane * 16, kb + t * 4096, 1024);
#pragma unroll
        for (int b = 0; b < 4; ++b) {
          f32x16 s = mfma(k0, qf[2 * b], f32x16{});
          s = mfma(k1, qf[2 * b + 1], s);
#pragma unroll
          for (int i = 0; i < 16; ++i) mm[b] = fmaxf(mm[b], s[i]);
        }
      }
      LAS float* mx = (LAS float*)(c.L + L_S);
#pragma unroll
      for (int b = 0; b < 4; ++b) mx[(b * 4 + w) * 64 + lane] = mm[b];
      bar();
      m = fmaxf(fmaxf(mx[(w * 4) * 64 + lane], mx[(w * 4 + 1) * 64 + lane]), fmaxf(mx[(w * 4 + 2) * 64 + lane], mx[(w * 4 + 3) * 64 + lane]));
      m = xmax(m);
      bar();
    }
  }
  STAMP(L * 12 + 7);
  signal(c, 0, 3 * L + 2);
  if (L < 3) {
    const u16* wn = SOFTMAX ? p.wa : p.wb;
    load_img(c, wn, 3 * DM, DM + c.h * DH, L_K);
    load_img(c, wn, 3 * DM, 2 * DM + c.h * DH, L_V);
    load_img(c, wn, 3 * DM, c.h * DH, L_Q);
  }
  const int p0 = (7 * c.h + (c.h & 1)) >> 1;
  if (L == 0) {
    const GAS char* src = (const GAS char*)p.x0 + (c.rb * 128 + w * 32 + r) * 896;
    xa = *(const GAS u32x4*)(src + (p0 + hh) * 16);
    if (hh == 0) xb2 = *(const GAS u32x4*)(src + (p0 + 2) * 16);
    else { const u32x2 t = *(const GAS u32x2*)(src + ((c.h & 1) ? (p0 - 1) * 16 + 8 : (p0 + 3) * 16)); xb2[0] = t[0]; xb2[1] = t[1]; xb2[2] = 0u; xb2[3] = 0u; }
  }
  STAMP(L * 12 + 8);
  await(c, 0, 3 * L + 2);
  STAMP(L * 12 + 9);

  {
    const int ob = WS_O + L * ACT + (c.rb * 4 + w) * TILE32;
    u32x4 of[NKS];
#pragma unroll
    for (int s = 0; s < NKS; ++s) of[s] = ld16<AUX_SC1>(c.rs, lane * 16, ob, s * 1024);
    f32x16 y = {};
#pragma unroll
    for (int s = 0; s < NKS; ++s) y = mfma(wfrag(pnat + L_W, s), of[s], y);
    float t1 = 0.f, t2 = 0.f;
    f32x16 xs;
#pragma unroll
    for (int j = 0; j < 4; ++j) { xs[2 * j] = bflo(xa[j]); xs[2 * j + 1] = bfhi(xa[j]); xs[8 + 2 * j] = bflo(xb2[j]); xs[8 + 2 * j + 1] = bfhi(xb2[j]); }
#pragma unroll
    for (int i = 0; i < 16; ++i) { float v = rbf(xs[i] + rbf(y[i])); if (i >= 12 && hh) v = 0.f; xs[i] = v; t1 += v; t2 = __builtin_fmaf(v, v, t2); }
    xa = pack8(xs, 0); xb2 = pack8(xs, 8);
    STAMP(L * 12 + 10);
    if (L < 3) {
      store_slice(c, WS_X + L * ACT, xs);
      t1 = xsum(t1); t2 = xsum(t2);
      if (hh == 0) { const u32x2 t = {__float_as_uint(t1), __float_as_uint(t2)}; __builtin_amdgcn_raw_buffer_store_b64(t, c.rs, WS_ST + L * STSZ + (c.rb * 4 + w) * 4096 + c.h * 256 + r * 8, 0, AUX_SC1); }
      signal(c, 0, 3 * L + 3);
      load_img(c, SOFTMAX ? p.woa : p.wob, DM, c.h * DH, L_W);
    } else {
      GAS char* dst = (GAS char*)p.out + (c.rb * 128 + w * 32 + r) * 896;
      *(GAS u32x4*)(dst + (p0 + hh) * 16) = pack8(xs, 0);
      const u32x4 hi = pack8(xs, 8);
      if (hh == 0) *(GAS u32x4*)(dst + (p0 + 2) * 16) = hi;
      else { const u32x2 t = {hi[0], hi[1]}; *(GAS u32x2*)(dst + ((c.h & 1) ? (p0 - 1) * 16 + 8 : (p0 + 3) * 16)) = t; }
    }
    STAMP(L * 12 + 11);
  }
}

__global__ void __launch_bounds__(256, 1) fused_kernel(Params p) {
  extern __shared__ __attribute__((aligned(16))) char smem[];
  Ctx c;
  c.L = (LAS char*)smem; c.ws = p.ws;
  c.lane = threadIdx.x & 63; c.r = c.lane & 31; c.hh = c.lane >> 5; c.w = __builtin_amdgcn_readfirstlane(threadIdx.x >> 6);
  { const int x = blockIdx.x & 7, j = blockIdx.x >> 3; c.rb = (x & 3) * 4 + (j & 3); c.h = (x >> 2) * 8 + (j >> 2); }
  c.rs = __builtin_amdgcn_make_buffer_rsrc(p.ws, 0, WS_END, 0x00020000);
  STAMP(0);
  { u64 pc; asm volatile("s_getpc_b64 %0" : "=s"(pc));
    const GAS unsigned* cp = (const GAS unsigned*)(pc & ~127ull) + threadIdx.x * 32;
    unsigned a, b;
    asm volatile("global_load_dword %0, %1, off" : "=v"(a) : "v"(cp));
    if (threadIdx.x < 44) asm volatile("global_load_dword %0, %1, off" : "=v"(b) : "v"(cp + 8192));
    asm volatile("s_waitcnt vmcnt(0)" ::: "memory"); }
  const u64 ev = __hip_atomic_load((GAS u64*)(p.ws + WS_EPOCH), RLX_AGENT);
  const u64 epoch = ((u64)(unsigned)__builtin_amdgcn_readfirstlane((int)(ev >> 32)) << 32) | (unsigned)__builtin_amdgcn_readfirstlane((int)ev);
  c.tag = epoch * 0x9E3779B97F4A7C15ull + 0x632BE59BD9B4E019ull;
  u32x4 xa = {}, xb = {};
  for (int it = 0; it < 2; ++it) {
    layer<false>(c, p, it, xa, xb);
    layer<true>(c, p, it, xa, xb);
  }
  if (blockIdx.x == 0 && threadIdx.x == 0) __hip_atomic_store((GAS u64*)(p.ws + WS_EPOCH), epoch + 1, RLX_AGENT);
}

extern "C" void kernel_launch(void* const* d_in, const int* in_sizes, int n_in, void* d_out, int out_size, void* d_ws, size_t ws_size, hipStream_t stream) {
  Params p;
  p.x0 = (const u16*)d_in[0]; p.wa = (const u16*)d_in[1]; p.woa = (const u16*)d_in[2];
  p.wb = (const u16*)d_in[4]; p.wob = (const u16*)d_in[5]; p.out = (u16*)d_out; p.ws = (char*)d_ws;
  (void)hipFuncSetAttribute((const void*)fused_kernel, hipFuncAttributeMaxDynamicSharedMemorySize, LDS_BYTES);
  hipLaunchKernelGGL(fused_kernel, dim3(256), dim3(256), LDS_BYTES, stream, p);
}
```

```cpp
#include <hip/hip_runtime.h>

typedef unsigned short u16;
typedef unsigned long long u64;
typedef __bf16 bf16x2 __attribute__((ext_vector_type(2)));
typedef __bf16 bf16x8 __attribute__((ext_vector_type(8)));
typedef __bf16 bf16x16 __attribute__((ext_vector_type(16)));
typedef float f32x2 __attribute__((ext_vector_type(2)));
typedef float f32x4 __attribute__((ext_vector_type(4)));
typedef float f32x8 __attribute__((ext_vector_type(8)));
typedef float f32x16 __attribute__((ext_vector_type(16)));
typedef unsigned u32x2 __attribute__((ext_vector_type(2)));
typedef unsigned u32x4 __attribute__((ext_vector_type(4)));
typedef short s16x4 __attribute__((ext_vector_type(4)));
#define LAS __attribute__((address_space(3)))
#define GAS __attribute__((address_space(1)))
#define DI __device__ __forceinline__
#define RLX_AGENT __ATOMIC_RELAXED, __HIP_MEMORY_SCOPE_AGENT

constexpr int SEQ = 2048, DM = 448, NH = 16, DH = 28, NKS = 28;
constexpr int WS_FLAG = 0;
constexpr int WS_EPOCH = 131072;
constexpr int TILE32 = NKS * 1024;
constexpr int ACT = 64 * TILE32;
constexpr int WS_X = 1 << 18;
constexpr int WS_O = WS_X + 3 * ACT;
constexpr int WS_KV = WS_O + 4 * ACT;
constexpr int KVSZ = 16 * 64 * 4096;
constexpr int WS_KP = WS_KV + 2 * KVSZ;
constexpr int KPSZ = 16 * 16 * 4096;
constexpr int WS_ST = WS_KP + 2 * KPSZ;
constexpr int STSZ = 64 * 16 * 256;
constexpr int WS_END = WS_ST + 3 * STSZ;
constexpr int IMG = 448 * 56;
constexpr int L_Q = 0, L_K = IMG, L_V = 2 * IMG, L_W = 3 * IMG;
constexpr int L_S = 4 * IMG;
constexpr int L_QX = L_S + 49152;
constexpr int L_CS = L_QX + 8192;
constexpr int L_RS = L_CS + 3072;
constexpr int L_MISC = L_RS + 1024;
constexpr int LDS_BYTES = L_MISC + 256;
static_assert(LDS_BYTES <= 163840, "lds");
constexpr int AUX_SC1 = 16;

struct Params { const u16* x0; const u16* wa; const u16* woa; const u16* wb; const u16* wob; u16* out; char* ws; };

DI f32x16 mfma(u32x4 a, u32x4 b, f32x16 c) { return __builtin_amdgcn_mfma_f32_32x32x16_bf16(__builtin_bit_cast(bf16x8, a), __builtin_bit_cast(bf16x8, b), c, 0, 0, 0); }
DI unsigned pk(float lo, float hi) { f32x2 v = {lo, hi}; return __builtin_bit_cast(unsigned, __builtin_convertvector(v, bf16x2)); }
DI float rbf(float x) { return __uint_as_float(pk(x, x) & 0xffff0000u); }
DI float bflo(unsigned u) { return __uint_as_float(u << 16); }
DI float bfhi(unsigned u) { return __uint_as_float(u & 0xffff0000u); }
DI u32x4 pack8(const f32x16& v, int o) { u32x4 r = {pk(v[o], v[o + 1]), pk(v[o + 2], v[o + 3]), pk(v[o + 4], v[o + 5]), pk(v[o + 6], v[o + 7])}; return r; }
DI float xsum(float v) { auto r = __builtin_amdgcn_permlane32_swap(__float_as_uint(v), __float_as_uint(v), false, false); return __uint_as_float(r[0]) + __uint_as_float(r[1]); }
DI float xmax(float v) { auto r = __builtin_amdgcn_permlane32_swap(__float_as_uint(v), __float_as_uint(v), false, false); return fmaxf(__uint_as_float(r[0]), __uint_as_float(r[1])); }
DI float xhi(float v) { auto r = __builtin_amdgcn_permlane32_swap(__float_as_uint(v), __float_as_uint(v), false, false); return __uint_as_float(r[1]); }
DI u32x4 wfrag(LAS char* p, int s) {
  s16x4 a = __builtin_amdgcn_ds_read_tr16_b64_v4i16((LAS s16x4*)(p + s * 896));
  s16x4 b = __builtin_amdgcn_ds_read_tr16_b64_v4i16((LAS s16x4*)(p + s * 896 + 224));
  u32x2 x = __builtin_bit_cast(u32x2, a), y = __builtin_bit_cast(u32x2, b);
  u32x4 r = {x[0], x[1], y[0], y[1]}; return r;
}
DI void bar() { asm volatile("s_waitcnt lgkmcnt(0)\n\ts_barrier" ::: "memory"); }
DI void drain() { asm volatile("s_waitcnt vmcnt(0)" ::: "memory"); }

typedef __amdgpu_buffer_rsrc_t rsrc_t;
template <int AUX> DI u32x4 ld16(rsrc_t rs, int voff, int soff, int k) { return __builtin_amdgcn_raw_buffer_load_b128(rs, voff + (k & 4095), soff + (k & ~4095), AUX); }
template <int AUX> DI u32x2 ld8(rsrc_t rs, int voff, int soff, int k) { return __builtin_amdgcn_raw_buffer_load_b64(rs, voff + (k & 4095), soff + (k & ~4095), AUX); }
DI rsrc_t mkrs(const void* p, int bytes) { return __builtin_amdgcn_make_buffer_rsrc((void*)p, 0, bytes, 0x00020000); }
DI int opaque(int v) { asm volatile("" : "+v"(v)); return v; }
DI int opaque_s(int v) { asm volatile("" : "+s"(v)); return v; }
#define SB() __builtin_amdgcn_sched_barrier(0)
struct Ctx {
  int rb, h, w, lane, r, hh;
  char* ws; __amdgpu_buffer_rsrc_t rs; u64 tag; LAS char* L;
};

DI void signal(const Ctx& c, int kind, int seq) {
  drain(); bar();
  if (threadIdx.x < 16) {
    const int g = kind ? c.h : c.rb, me = kind ? c.rb : c.h;
    GAS u64* f = (GAS u64*)(c.ws + WS_FLAG + ((kind * 16 + g) * 16 + c.lane) * 256) + me;
    (void)__hip_atomic_exchange(f, c.tag + seq, RLX_AGENT);
  }
}
DI void await(const Ctx& c, int kind, int seq) {
  if (c.w == 3) {
    const int g = kind ? c.h : c.rb, me = kind ? c.rb : c.h;
    GAS u64* f = (GAS u64*)(c.ws + WS_FLAG + ((kind * 16 + g) * 16 + me) * 256) + (c.lane & 15);
    const u64 want = c.tag + seq;
    for (unsigned spins = 0; spins < (1u << 18); ++spins) {
      const u64 v = __hip_atomic_load(f, RLX_AGENT);
      if (__all((v - want) < 64ull)) break;
    }
  }
  __builtin_amdgcn_fence(__ATOMIC_ACQUIRE, "wavefront");
  bar();
}

DI void load_img(const Ctx& c, const u16* W, int ld, int col0, int img) {
  const int lane = opaque(c.lane);
  if (lane < 56) {
    const int rr = lane / 7, g = lane - rr * 7;
    const rsrc_t wr = mkrs(W, 448 * ld * 2);
    const int voff = (rr * ld + 4 * g) * 2, soff = opaque_s((8 * c.w * ld + col0) * 2);
    LAS char* dst = c.L + img + 8 * c.w * 56 + lane * 8;
    u32x2 v[14];
#pragma unroll
    for (int i = 0; i < 14; ++i) v[i] = __builtin_amdgcn_raw_buffer_load_b64(wr, voff, soff + i * 32 * ld * 2, 0);
#pragma unroll
    for (int i = 0; i < 14; ++i) *(LAS u32x2*)(dst + i * 32 * 56) = v[i];
  }
}

#define STAMP(i)

DI float phi(float t) { return (t > 0.f ? t + 1.0f : __expf(t)) + 1e-6f; }

DI void store_slice(const Ctx& c, int base, const f32x16& v) {
  const int p0 = (7 * c.h + (c.h & 1)) >> 1;
  const int rowoff = base + (c.rb * 4 + c.w) * TILE32 + c.r * 16;
  __builtin_amdgcn_raw_buffer_store_b128(pack8(v, 0), c.rs, rowoff + (p0 + c.hh) * 512, 0, AUX_SC1);
  const u32x4 hi = pack8(v, 8);
  if (c.hh == 0) __builtin_amdgcn_raw_buffer_store_b128(hi, c.rs, rowoff + (p0 + 2) * 512, 0, AUX_SC1);
  else { u32x2 t = {hi[0], hi[1]}; __builtin_amdgcn_raw_buffer_store_b64(t, c.rs, rowoff + ((c.h & 1) ? (p0 - 1) * 512 + 8 : (p0 + 3) * 512), 0, AUX_SC1); }
}

template <bool SOFTMAX>
DI void layer(Ctx c, const Params& p, int it, u32x4& xa, u32x4& xb2) {
  const int L = it * 2 + (SOFTMAX ? 1 : 0);
  c.lane = opaque(c.lane); c.r = c.lane & 31; c.hh = c.lane >> 5;
  c.w = opaque_s(c.w); c.h = opaque_s(c.h); c.rb = opaque_s(c.rb);
  const int lane = c.lane, r = c.r, hh = c.hh, w = c.w;
  const int G = ((lane >> 4) & 1) * 4 + (lane & 3), q4 = (lane & 15) >> 2;
  const int rowb = (8 * hh + q4) * 56;
  const int natg = (c.h & 1) ? (G == 5 ? 0 : (G == 0 ? 1 : G == 2 ? 2 : G == 1 ? 3 : G == 3 ? 4 : G == 4 ? 5 : 6))
                             : (G == 5 ? 6 : (G == 0 ? 0 : G == 2 ? 1 : G == 1 ? 2 : G == 3 ? 3 : G == 4 ? 4 : 5));
  const int padoff = 8 * hh * 56 + 224 + 8 * q4;
  LAS char* pid = c.L + (G == 7 ? padoff : rowb + G * 8);
  LAS char* pnat = c.L + (G == 7 ? padoff : rowb + natg * 8);

  u32x4 xf[NKS];
  float s1 = 0.f, s2 = 0.f;
  if (L == 0) {
    const rsrc_t xr = mkrs(p.x0, SEQ * DM * 2);
#pragma unroll
    for (int s = 0; s < NKS; ++s) xf[s] = ld16<0>(xr, r * 896 + hh * 16, (c.rb * 128 + w * 32) * 896, s * 32);
    load_img(c, p.wa, 3 * DM, DM + c.h * DH, L_K);
    load_img(c, p.wa, 3 * DM, 2 * DM + c.h * DH, L_V);
    load_img(c, p.wa, 3 * DM, c.h * DH, L_Q);
    load_img(c, p.woa, DM, c.h * DH, L_W);
    const bf16x2 one = __builtin_bit_cast(bf16x2, opaque(0x3f803f80));
#pragma unroll
    for (int s = 0; s < NKS; ++s)
#pragma unroll
      for (int j = 0; j < 4; ++j) { const unsigned e = xf[s][j]; const bf16x2 v = __builtin_bit_cast(bf16x2, e); s1 = __builtin_amdgcn_fdot2_f32_bf16(v, one, s1, false); s2 = __builtin_amdgcn_fdot2_f32_bf16(v, v, s2, false); }
    bar();
  } else {
    await(c, 0, 3 * L);
    STAMP(L * 12 + 0);
    const int xb = WS_X + (L - 1) * ACT + (c.rb * 4 + w) * TILE32;
    const int sb = WS_ST + (L - 1) * STSZ + (c.rb * 4 + w) * 4096;
    u32x2 st[8];
#pragma unroll
    for (int j = 0; j < 8; ++j) st[j] = ld8<AUX_SC1>(c.rs, hh * 2048 + r * 8, sb, j * 256);
#pragma unroll
    for (int s = 0; s < NKS; ++s) xf[s] = ld16<AUX_SC1>(c.rs, lane * 16, xb, s * 1024);
#pragma unroll
    for (int j = 0; j < 8; ++j) { s1 += __uint_as_float(st[j][0]); s2 += __uint_as_float(st[j][1]); }
  }
  s1 = xsum(s1); s2 = xsum(s2);
  const float mu = s1 * (1.0f / DM);
  const float rstd = __builtin_amdgcn_rsqf(fmaxf(s2 * (1.0f / DM) - mu * mu, 0.f) + 1e-5f);
  const float mr = mu * rstd;
  LAS float* rsl = (LAS float*)(c.L + L_RS) + w * 64;
  if (hh == 0) rsl[r] = rstd; else rsl[32 + r] = mr;

  LAS float* csl = (LAS float*)(c.L + L_CS) + (SOFTMAX ? 384 : 0);
  if (it == 0) {
    const u32x4 ones = {0x3f803f80u, 0x3f803f80u, 0x3f803f80u, 0x3f803f80u};
    f32x16 a0 = {}, a1 = {}, a2 = {};
#pragma unroll
    for (int s = 0; s < 7; ++s) {
      a0 = mfma(ones, wfrag(pid + L_Q + w * 7 * 896, s), a0);
      a1 = mfma(ones, wfrag(pid + L_K + w * 7 * 896, s), a1);
      a2 = mfma(ones, wfrag(pnat + L_V + w * 7 * 896, s), a2);
    }
    if (hh == 0) { csl[w * 96 + r] = a0[0]; csl[w * 96 + 32 + r] = a1[0]; csl[w * 96 + 64 + r] = a2[0]; }
    bar();
  }

  f32x16 ak = {}, av = {};
#pragma unroll
  for (int s = 0; s < NKS; ++s) {
    const u32x4 kf = wfrag(pid + L_K, s), vf = wfrag(pnat + L_V, s);
    if (SOFTMAX) ak = mfma(kf, xf[s], ak); else ak = mfma(xf[s], kf, ak);
    av = mfma(xf[s], vf, av);
  }
  STAMP(L * 12 + 1);
  f32x16 rr, rm;
#pragma unroll
  for (int g = 0; g < 4; ++g) {
    const f32x4 a = *(LAS f32x4*)(rsl + 8 * g + 4 * hh), b = *(LAS f32x4*)(rsl + 32 + 8 * g + 4 * hh);
#pragma unroll
    for (int j = 0; j < 4; ++j) { rr[4 * g + j] = a[j]; rm[4 * g + j] = b[j]; }
  }
  const float csv = (csl[64 + r] + csl[96 + 64 + r]) + (csl[192 + 64 + r] + csl[288 + 64 + r]);
#pragma unroll
  for (int i = 0; i < 16; ++i) { const float v = av[i] * rr[i] - rm[i] * csv; av[i] = r < 28 ? v : (r == 28 ? 1.0f : 0.f); }
  const u32x4 vf0 = pack8(av, 0), vf1 = pack8(av, 8);
  u32x4 kf0, kf1;
  if (SOFTMAX) {
#pragma unroll
    for (int g = 0; g < 4; ++g) {
      const LAS float* cp = csl + 32 + 8 * g + 4 * hh;
      const f32x4 cs = (*(LAS f32x4*)cp + *(LAS f32x4*)(cp + 96)) + (*(LAS f32x4*)(cp + 192) + *(LAS f32x4*)(cp + 288));
#pragma unroll
      for (int j = 0; j < 4; ++j) ak[4 * g + j] = ak[4 * g + j] * rstd - mr * cs[j];
    }
    if (hh) { ak[12] = 1.0f; ak[13] = 1.0f; ak[14] = 1.0f; ak[15] = 0.f; }
    kf0 = pack8(ak, 0); kf1 = pack8(ak, 8);
    const int tb = WS_KV + it * KVSZ + (c.h * 64 + c.rb * 4 + w) * 4096;
    const int rs23 = (r & 19) | ((r & 4) << 1) | ((r & 8) >> 1);
    __builtin_amdgcn_raw_buffer_store_b128(kf0, c.rs, tb + (hh * 32 + rs23) * 16, 0, AUX_SC1);
    __builtin_amdgcn_raw_buffer_store_b128(kf1, c.rs, tb + 1024 + (hh * 32 + rs23) * 16, 0, AUX_SC1);
    __builtin_amdgcn_raw_buffer_store_b128(vf0, c.rs, tb + 2048 + lane * 16, 0, AUX_SC1);
    __builtin_amdgcn_raw_buffer_store_b128(vf1, c.rs, tb + 3072 + lane * 16, 0, AUX_SC1);
  } else {
    const float csk = (csl[32 + r] + csl[96 + 32 + r]) + (csl[192 + 32 + r] + csl[288 + 32 + r]);
#pragma unroll
    for (int i = 0; i < 16; ++i) { const float v = phi(rbf(ak[i] * rr[i] - rm[i] * csk)); ak[i] = r < 28 ? v : 0.f; }
    f32x16 kv = mfma(pack8(ak, 0), vf0, f32x16{});
    kv = mfma(pack8(ak, 8), vf1, kv);
    LAS f32x4* ex = (LAS f32x4*)(c.L + L_S);
#pragma unroll
    for (int g = 0; g < 4; ++g) { const f32x4 t = {kv[4 * g], kv[4 * g + 1], kv[4 * g + 2], kv[4 * g + 3]}; ex[(g * 4 + w) * 64 + lane] = t; }
    bar();
    const f32x4 t = (ex[(w * 4 + 0) * 64 + lane] + ex[(w * 4 + 1) * 64 + lane]) + (ex[(w * 4 + 2) * 64 + lane] + ex[(w * 4 + 3) * 64 + lane]);
    __builtin_amdgcn_raw_buffer_store_b128(__builtin_bit_cast(u32x4, t), c.rs, WS_KP + it * KPSZ + (c.h * 16 + c.rb) * 4096 + w * 1024 + lane * 16, 0, AUX_SC1);
  }
  STAMP(L * 12 + 2);
  signal(c, 1, 3 * L + 1);
  STAMP(L * 12 + 3);

  f32x16 aq = {};
#pragma unroll
  for (int s = 0; s < NKS; ++s) aq = mfma(wfrag(pid + L_Q, s), xf[s], aq);
#pragma unroll
  for (int g = 0; g < 4; ++g) {
    const LAS float* cp = csl + 8 * g + 4 * hh;
    const f32x4 cs = (*(LAS f32x4*)cp + *(LAS f32x4*)(cp + 96)) + (*(LAS f32x4*)(cp + 192) + *(LAS f32x4*)(cp + 288));
#pragma unroll
    for (int j = 0; j < 4; ++j) aq[4 * g + j] = aq[4 * g + j] * rstd - mr * cs[j];
  }
  f32x16 o;
  if (!SOFTMAX) {
#pragma unroll
    for (int i = 0; i < 16; ++i) aq[i] = phi(rbf(aq[i]));
    if (hh) { aq[12] = 0.f; aq[13] = 0.f; aq[14] = 0.f; aq[15] = 0.f; }
    const u32x4 q0 = pack8(aq, 0), q1 = pack8(aq, 8);
    STAMP(L * 12 + 4);
    await(c, 1, 3 * L + 1);
    STAMP(L * 12 + 5);
    u32x4 pt[16];
    const int pb = WS_KP + it * KPSZ + c.h * 16 * 4096 + w * 1024;
#pragma unroll
    for (int j = 0; j < 16; ++j) pt[j] = ld16<AUX_SC1>(c.rs, lane * 16, pb, j * 4096);
    f32x4 t = {};
#pragma unroll
    for (int j = 0; j < 16; ++j) t += __builtin_bit_cast(f32x4, pt[j]);
    LAS u32x2* ex = (LAS u32x2*)(c.L + L_QX);
    { const u32x2 v = {pk(t[0], t[1]), pk(t[2], t[3])}; ex[((w >> 1) * 64 + lane) * 2 + (w & 1)] = v; }
    bar();
    const u32x4 a0 = *(LAS u32x4*)(c.L + L_QX + lane * 16), a1 = *(LAS u32x4*)(c.L + L_QX + 1024 + lane * 16);
    o = mfma(a0, q0, f32x16{});
    o = mfma(a1, q1, o);
    const float rd = 1.0f / (xhi(o[12]) + 1e-6f);
#pragma unroll
    for (int i = 0; i < 16; ++i) o[i] *= rd;
    STAMP(L * 12 + 6);
    store_slice(c, WS_O + L * ACT, o);
  } else {
    const float qs = 0.18898223650461362f * 1.4426950408889634f;
#pragma unroll
    for (int i = 0; i < 16; ++i) aq[i] *= qs;
    if (hh) { aq[12] = 0.f; aq[13] = 0.f; aq[14] = 0.f; aq[15] = 0.f; }
    const u32x4 q0 = pack8(aq, 0); u32x4 q1 = pack8(aq, 8);
    f32x16 s0 = mfma(kf0, q0, f32x16{});
    s0 = mfma(kf1, q1, s0);
    float m = s0[0];
#pragma unroll
    for (int i = 1; i < 16; ++i) m = fmaxf(m, s0[i]);
    m = xmax(m);
    LAS u32x4* qx = (LAS u32x4*)(c.L + L_QX);
    LAS unsigned* badw = (LAS unsigned*)(c.L + L_MISC);
    STAMP(L * 12 + 4);
    const int kb = WS_KV + it * KVSZ + (c.h * 64 + w * 16) * 4096;
    for (int pass = 0;; ++pass) {
      { const float m0 = rbf(m), m1 = rbf(m - m0), m2 = rbf((m - m0) - m1);
        if (hh) { q1[2] = pk(-m0, -m1); q1[3] = pk(-m2, 0.f); } }
      qx[(w * 2) * 64 + lane] = q0; qx[(w * 2 + 1) * 64 + lane] = q1;
      if (threadIdx.x == 0) *badw = 0u;
      if (pass == 0) await(c, 1, 3 * L + 1); else bar();
      STAMP(L * 12 + 5);
      u32x4 qf[8];
#pragma unroll
      for (int j = 0; j < 8; ++j) qf[j] = qx[j * 64 + lane];
      f32x16 oa[4] = {};
      u32x4 kv[4], kn[4];
#pragma unroll
      for (int j = 0; j < 4; ++j) kv[j] = ld16<AUX_SC1>(c.rs, lane * 16, kb, j * 1024);
      for (int t = 0; t < 16; ++t) {
        const int tn = t < 15 ? t + 1 : t;
#pragma unroll
        for (int j = 0; j < 4; ++j) kn[j] = ld16<AUX_SC1>(c.rs, lane * 16, kb + tn * 4096, j * 1024);
#pragma unroll
        for (int b = 0; b < 4; ++b) {
          f32x16 s = mfma(kv[0], qf[2 * b], f32x16{});
          s = mfma(kv[1], qf[2 * b + 1], s);
          f32x8 p0, p1;
#pragma unroll
          for (int i = 0; i < 8; ++i) { p0[i] = __builtin_amdgcn_exp2f(s[i]); p1[i] = __builtin_amdgcn_exp2f(s[8 + i]); }
          oa[b] = __builtin_amdgcn_smfmac_f32_32x32x32_bf16(__builtin_bit_cast(bf16x8, kv[2]), __builtin_bit_cast(bf16x16, p0), oa[b], 0xdddd, 0, 0);
          oa[b] = __builtin_amdgcn_smfmac_f32_32x32x32_bf16(__builtin_bit_cast(bf16x8, kv[3]), __builtin_bit_cast(bf16x16, p1), oa[b], 0xdddd, 0, 0);
        }
#pragma unroll
        for (int j = 0; j < 4; ++j) kv[j] = kn[j];
      }
      STAMP(L * 12 + 6);
      LAS f32x4* ex = (LAS f32x4*)(c.L + L_S);
#pragma unroll
      for (int b = 0; b < 4; ++b) {
        if (b != w) {
          const int slot = b * 3 + (w > b ? w - 1 : w);
#pragma unroll
          for (int g = 0; g < 4; ++g) { const f32x4 t = {oa[b][4 * g], oa[b][4 * g + 1], oa[b][4 * g + 2], oa[b][4 * g + 3]}; ex[(slot * 4 + g) * 64 + lane] = t; }
        } else o = oa[b];
      }
      bar();
#pragma unroll
      for (int k = 0; k < 3; ++k)
#pragma unroll
        for (int g = 0; g < 4; ++g) { const f32x4 t = ex[((w * 3 + k) * 4 + g) * 64 + lane];
#pragma unroll
          for (int j = 0; j < 4; ++j) o[4 * g + j] += t[j]; }
      const float l = xhi(o[12]);
      const float rl = 1.0f / l;
      float chk = 0.f;
#pragma unroll
      for (int i = 0; i < 16; ++i) { chk = __builtin_fmaf(o[i], 0.f, chk); o[i] *= rl; }
      const bool bad = !(l < 1.0e30f) || !(chk == 0.f);
      store_slice(c, WS_O + L * ACT, o);
      if (pass) break;
      if (__any(bad) && lane == 0) *badw = 1u;
      bar();
      if (*badw == 0u) break;
      float mm[4] = {-3.0e38f, -3.0e38f, -3.0e38f, -3.0e38f};
#pragma unroll
      for (int b = 0; b < 4; ++b) if (hh) { qf[2 * b + 1][2] = 0u; qf[2 * b + 1][3] = 0u; }
      for (int t = 0; t < 16; ++t) {
        const u32x4 k0 = ld16<AUX_SC1>(c.rs, lane * 16, kb + t * 4096, 0), k1 = ld16<AUX_SC1>(c.rs, lane * 16, kb + t * 4096, 1024);
#pragma unroll
        for (int b = 0; b < 4; ++b) {
          f32x16 s = mfma(k0, qf[2 * b], f32x16{});
          s = mfma(k1, qf[2 * b + 1], s);
#pragma unroll
          for (int i = 0; i < 16; ++i) mm[b] = fmaxf(mm[b], s[i]);
        }
      }
      LAS float* mx = (LAS float*)(c.L + L_S);
#pragma unroll
      for (int b = 0; b < 4; ++b) mx[(b * 4 + w) * 64 + lane] = mm[b];
      bar();
      m = fmaxf(fmaxf(mx[(w * 4) * 64 + lane], mx[(w * 4 + 1) * 64 + lane]), fmaxf(mx[(w * 4 + 2) * 64 + lane], mx[(w * 4 + 3) * 64 + lane]));
      m = xmax(m);
      bar();
    }
  }
  STAMP(L * 12 + 7);
  signal(c, 0, 3 * L + 2);
  if (L < 3) {
    const u16* wn = SOFTMAX ? p.wa : p.wb;
    load_img(c, wn, 3 * DM, DM + c.h * DH, L_K);
    load_img(c, wn, 3 * DM, 2 * DM + c.h * DH, L_V);
    load_img(c, wn, 3 * DM, c.h * DH, L_Q);
  }
  const int p0 = (7 * c.h + (c.h & 1)) >> 1;
  if (L == 0) {
    const GAS char* src = (const GAS char*)p.x0 + (c.rb * 128 + w * 32 + r) * 896;
    xa = *(const GAS u32x4*)(src + (p0 + hh) * 16);
    if (hh == 0) xb2 = *(const GAS u32x4*)(src + (p0 + 2) * 16);
    else { const u32x2 t = *(const GAS u32x2*)(src + ((c.h & 1) ? (p0 - 1) * 16 + 8 : (p0 + 3) * 16)); xb2[0] = t[0]; xb2[1] = t[1]; xb2[2] = 0u; xb2[3] = 0u; }
  }
  STAMP(L * 12 + 8);
  await(c, 0, 3 * L + 2);
  STAMP(L * 12 + 9);

  {
    const int ob = WS_O + L * ACT + (c.rb * 4 + w) * TILE32;
    u32x4 of[NKS];
#pragma unroll
    for (int s = 0; s < NKS; ++s) of[s] = ld16<AUX_SC1>(c.rs, lane * 16, ob, s * 1024);
    f32x16 y = {};
#pragma unroll
    for (int s = 0; s < NKS; ++s) y = mfma(wfrag(pnat + L_W, s), of[s], y);
    float t1 = 0.f, t2 = 0.f;
    f32x16 xs;
#pragma unroll
    for (int j = 0; j < 4; ++j) { xs[2 * j] = bflo(xa[j]); xs[2 * j + 1] = bfhi(xa[j]); xs[8 + 2 * j] = bflo(xb2[j]); xs[8 + 2 * j + 1] = bfhi(xb2[j]); }
#pragma unroll
    for (int i = 0; i < 16; ++i) { float v = rbf(xs[i] + rbf(y[i])); if (i >= 12 && hh) v = 0.f; xs[i] = v; t1 += v; t2 = __builtin_fmaf(v, v, t2); }
    xa = pack8(xs, 0); xb2 = pack8(xs, 8);
    STAMP(L * 12 + 10);
    if (L < 3) {
      store_slice(c, WS_X + L * ACT, xs);
      t1 = xsum(t1); t2 = xsum(t2);
      if (hh == 0) { const u32x2 t = {__float_as_uint(t1), __float_as_uint(t2)}; __builtin_amdgcn_raw_buffer_store_b64(t, c.rs, WS_ST + L * STSZ + (c.rb * 4 + w) * 4096 + c.h * 256 + r * 8, 0, AUX_SC1); }
      signal(c, 0, 3 * L + 3);
      load_img(c, SOFTMAX ? p.woa : p.wob, DM, c.h * DH, L_W);
    } else {
      GAS char* dst = (GAS char*)p.out + (c.rb * 128 + w * 32 + r) * 896;
      *(GAS u32x4*)(dst + (p0 + hh) * 16) = pack8(xs, 0);
      const u32x4 hi = pack8(xs, 8);
      if (hh == 0) *(GAS u32x4*)(dst + (p0 + 2) * 16) = hi;
      else { const u32x2 t = {hi[0], hi[1]}; *(GAS u32x2*)(dst + ((c.h & 1) ? (p0 - 1) * 16 + 8 : (p0 + 3) * 16)) = t; }
    }
    STAMP(L * 12 + 11);
  }
}

__global__ void __launch_bounds__(256, 1) fused_kernel(Params p) {
  extern __shared__ __attribute__((aligned(16))) char smem[];
  Ctx c;
  c.L = (LAS char*)smem; c.ws = p.ws;
  c.lane = threadIdx.x & 63; c.r = c.lane & 31; c.hh = c.lane >> 5; c.w = __builtin_amdgcn_readfirstlane(threadIdx.x >> 6);
  { const int x = blockIdx.x & 7, j = blockIdx.x >> 3; c.rb = (x & 3) * 4 + (j & 3); c.h = (x >> 2) * 8 + (j >> 2); }
  c.rs = __builtin_amdgcn_make_buffer_rsrc(p.ws, 0, WS_END, 0x00020000);
  STAMP(0);
  { const u64 t0 = __builtin_amdgcn_s_memtime(); while (__builtin_amdgcn_s_memtime() - t0 < 48000ull) {} }
  const u64 ev = __hip_atomic_load((GAS u64*)(p.ws + WS_EPOCH), RLX_AGENT);
  const u64 epoch = ((u64)(unsigned)__builtin_amdgcn_readfirstlane((int)(ev >> 32)) << 32) | (unsigned)__builtin_amdgcn_readfirstlane((int)ev);
  c.tag = epoch * 0x9E3779B97F4A7C15ull + 0x632BE59BD9B4E019ull;
  u32x4 xa = {}, xb = {};
  for (int it = 0; it < 2; ++it) {
    layer<false>(c, p, it, xa, xb);
    layer<true>(c, p, it, xa, xb);
  }
  if (blockIdx.x == 0 && threadIdx.x == 0) __hip_atomic_store((GAS u64*)(p.ws + WS_EPOCH), epoch + 1, RLX_AGENT);
}

extern "C" void kernel_launch(void* const* d_in, const int* in_sizes, int n_in, void* d_out, int out_size, void* d_ws, size_t ws_size, hipStream_t stream) {
  Params p;
  p.x0 = (const u16*)d_in[0]; p.wa = (const u16*)d_in[1]; p.woa = (const u16*)d_in[2];
  p.wb = (const u16*)d_in[4]; p.wob = (const u16*)d_in[5]; p.out = (u16*)d_out; p.ws = (char*)d_ws;
  (void)hipFuncSetAttribute((const void*)fused_kernel, hipFuncAttributeMaxDynamicSharedMemorySize, LDS_BYTES);
  hipLaunchKernelGGL(fused_kernel, dim3(256), dim3(256), LDS_BYTES, stream, p);
}
```

```cpp
#include <hip/hip_runtime.h>
#define TA 0
#define TB 1

typedef unsigned short u16;
typedef unsigned long long u64;
typedef __bf16 bf16x2 __attribute__((ext_vector_type(2)));
typedef __bf16 bf16x8 __attribute__((ext_vector_type(8)));
typedef __bf16 bf16x16 __attribute__((ext_vector_type(16)));
typedef float f32x2 __attribute__((ext_vector_type(2)));
typedef float f32x4 __attribute__((ext_vector_type(4)));
typedef float f32x8 __attribute__((ext_vector_type(8)));
typedef float f32x16 __attribute__((ext_vector_type(16)));
typedef unsigned u32x2 __attribute__((ext_vector_type(2)));
typedef unsigned u32x4 __attribute__((ext_vector_type(4)));
typedef short s16x4 __attribute__((ext_vector_type(4)));
#define LAS __attribute__((address_space(3)))
#define GAS __attribute__((address_space(1)))
#define DI __device__ __forceinline__
#define RLX_AGENT __ATOMIC_RELAXED, __HIP_MEMORY_SCOPE_AGENT

constexpr int SEQ = 2048, DM = 448, NH = 16, DH = 28, NKS = 28;
constexpr int WS_FLAG = 0;
constexpr int WS_EPOCH = 131072;
constexpr int TILE32 = NKS * 1024;
constexpr int ACT = 64 * TILE32;
constexpr int WS_X = 1 << 18;
constexpr int WS_O = WS_X + 3 * ACT;
constexpr int WS_KV = WS_O + 4 * ACT;
constexpr int KVSZ = 16 * 64 * 4096;
constexpr int WS_KP = WS_KV + 2 * KVSZ;
constexpr int KPSZ = 16 * 16 * 4096;
constexpr int WS_ST = WS_KP + 2 * KPSZ;
constexpr int STSZ = 64 * 16 * 256;
constexpr int WS_END = WS_ST + 3 * STSZ;
constexpr int IMG = 448 * 56;
constexpr int L_Q = 0, L_K = IMG, L_V = 2 * IMG, L_W = 3 * IMG;
constexpr int L_S = 4 * IMG;
constexpr int L_QX = L_S + 49152;
constexpr int L_CS = L_QX + 8192;
constexpr int L_RS = L_CS + 3072;
constexpr int L_MISC = L_RS + 1024;
constexpr int LDS_BYTES = L_MISC + 256;
static_assert(LDS_BYTES <= 163840, "lds");
constexpr int AUX_SC1 = 16;

struct Params { const u16* x0; const u16* wa; const u16* woa; const u16* wb; const u16* wob; u16* out; char* ws; };

DI f32x16 mfma(u32x4 a, u32x4 b, f32x16 c) { return __builtin_amdgcn_mfma_f32_32x32x16_bf16(__builtin_bit_cast(bf16x8, a), __builtin_bit_cast(bf16x8, b), c, 0, 0, 0); }
DI unsigned pk(float lo, float hi) { f32x2 v = {lo, hi}; return __builtin_bit_cast(unsigned, __builtin_convertvector(v, bf16x2)); }
DI float rbf(float x) { return __uint_as_float(pk(x, x) & 0xffff0000u); }
DI float bflo(unsigned u) { return __uint_as_float(u << 16); }
DI float bfhi(unsigned u) { return __uint_as_float(u & 0xffff0000u); }
DI u32x4 pack8(const f32x16& v, int o) { u32x4 r = {pk(v[o], v[o + 1]), pk(v[o + 2], v[o + 3]), pk(v[o + 4], v[o + 5]), pk(v[o + 6], v[o + 7])}; return r; }
DI float xsum(float v) { auto r = __builtin_amdgcn_permlane32_swap(__float_as_uint(v), __float_as_uint(v), false, false); return __uint_as_float(r[0]) + __uint_as_float(r[1]); }
DI float xmax(float v) { auto r = __builtin_amdgcn_permlane32_swap(__float_as_uint(v), __float_as_uint(v), false, false); return fmaxf(__uint_as_float(r[0]), __uint_as_float(r[1])); }
DI float xhi(float v) { auto r = __builtin_amdgcn_permlane32_swap(__float_as_uint(v), __float_as_uint(v), false, false); return __uint_as_float(r[1]); }
DI u32x4 wfrag(LAS char* p, int s) {
  s16x4 a = __builtin_amdgcn_ds_read_tr16_b64_v4i16((LAS s16x4*)(p + s * 896));
  s16x4 b = __builtin_amdgcn_ds_read_tr16_b64_v4i16((LAS s16x4*)(p + s * 896 + 224));
  u32x2 x = __builtin_bit_cast(u32x2, a), y = __builtin_bit_cast(u32x2, b);
  u32x4 r = {x[0], x[1], y[0], y[1]}; return r;
}
DI void bar() { asm volatile("s_waitcnt lgkmcnt(0)\n\ts_barrier" ::: "memory"); }
DI void drain() { asm volatile("s_waitcnt vmcnt(0)" ::: "memory"); }

typedef __amdgpu_buffer_rsrc_t rsrc_t;
template <int AUX> DI u32x4 ld16(rsrc_t rs, int voff, int soff, int k) { return __builtin_amdgcn_raw_buffer_load_b128(rs, voff + (k & 4095), soff + (k & ~4095), AUX); }
template <int AUX> DI u32x2 ld8(rsrc_t rs, int voff, int soff, int k) { return __builtin_amdgcn_raw_buffer_load_b64(rs, voff + (k & 4095), soff + (k & ~4095), AUX); }
DI rsrc_t mkrs(const void* p, int bytes) { return __builtin_amdgcn_make_buffer_rsrc((void*)p, 0, bytes, 0x00020000); }
DI int opaque(int v) { asm volatile("" : "+v"(v)); return v; }
DI int opaque_s(int v) { asm volatile("" : "+s"(v)); return v; }
#define SB() __builtin_amdgcn_sched_barrier(0)
struct Ctx {
  int rb, h, w, lane, r, hh;
  char* ws; __amdgpu_buffer_rsrc_t rs; u64 tag; LAS char* L;
};

DI void signal(const Ctx& c, int kind, int seq) {
  drain(); bar();
  if (threadIdx.x < 16) {
    const int g = kind ? c.h : c.rb, me = kind ? c.rb : c.h;
    GAS u64* f = (GAS u64*)(c.ws + WS_FLAG + ((kind * 16 + g) * 16 + c.lane) * 256) + me;
    (void)__hip_atomic_exchange(f, c.tag + seq, RLX_AGENT);
  }
}
DI void await(const Ctx& c, int kind, int seq) {
  if (c.w == 3) {
    const int g = kind ? c.h : c.rb, me = kind ? c.rb : c.h;
    GAS u64* f = (GAS u64*)(c.ws + WS_FLAG + ((kind * 16 + g) * 16 + me) * 256) + (c.lane & 15);
    const u64 want = c.tag + seq;
    for (unsigned spins = 0; spins < (1u << 18); ++spins) {
      const u64 v = __hip_atomic_load(f, RLX_AGENT);
      if (__all((v - want) < 64ull)) break;
    }
  }
  __builtin_amdgcn_fence(__ATOMIC_ACQUIRE, "wavefront");
  bar();
}

DI void load_img(const Ctx& c, const u16* W, int ld, int col0, int img) {
  const int lane = opaque(c.lane);
  if (lane < 56) {
    const int rr = lane / 7, g = lane - rr * 7;
    const rsrc_t wr = mkrs(W, 448 * ld * 2);
    const int voff = (rr * ld + 4 * g) * 2, soff = opaque_s((8 * c.w * ld + col0) * 2);
    LAS char* dst = c.L + img + 8 * c.w * 56 + lane * 8;
    u32x2 v[14];
#pragma unroll
    for (int i = 0; i < 14; ++i) v[i] = __builtin_amdgcn_raw_buffer_load_b64(wr, voff, soff + i * 32 * ld * 2, 0);
#pragma unroll
    for (int i = 0; i < 14; ++i) *(LAS u32x2*)(dst + i * 32 * 56) = v[i];
  }
}

#define STAMP(i) do { if (((i) == TA || (i) == TB) && threadIdx.x == 0) ((LAS u64*)(c.L + L_MISC + 64))[(i) == TB] = __builtin_amdgcn_s_memrealtime(); } while (0)

DI float phi(float t) { return (t > 0.f ? t + 1.0f : __expf(t)) + 1e-6f; }

DI void store_slice(const Ctx& c, int base, const f32x16& v) {
  const int p0 = (7 * c.h + (c.h & 1)) >> 1;
  const int rowoff = base + (c.rb * 4 + c.w) * TILE32 + c.r * 16;
  __builtin_amdgcn_raw_buffer_store_b128(pack8(v, 0), c.rs, rowoff + (p0 + c.hh) * 512, 0, AUX_SC1);
  const u32x4 hi = pack8(v, 8);
  if (c.hh == 0) __builtin_amdgcn_raw_buffer_store_b128(hi, c.rs, rowoff + (p0 + 2) * 512, 0, AUX_SC1);
  else { u32x2 t = {hi[0], hi[1]}; __builtin_amdgcn_raw_buffer_store_b64(t, c.rs, rowoff + ((c.h & 1) ? (p0 - 1) * 512 + 8 : (p0 + 3) * 512), 0, AUX_SC1); }
}

template <bool SOFTMAX>
DI void layer(Ctx c, const Params& p, int it, u32x4& xa, u32x4& xb2) {
  const int L = it * 2 + (SOFTMAX ? 1 : 0);
  c.lane = opaque(c.lane); c.r = c.lane & 31; c.hh = c.lane >> 5;
  c.w = opaque_s(c.w); c.h = opaque_s(c.h); c.rb = opaque_s(c.rb);
  const int lane = c.lane, r = c.r, hh = c.hh, w = c.w;
  const int G = ((lane >> 4) & 1) * 4 + (lane & 3), q4 = (lane & 15) >> 2;
  const int rowb = (8 * hh + q4) * 56;
  const int natg = (c.h & 1) ? (G == 5 ? 0 : (G == 0 ? 1 : G == 2 ? 2 : G == 1 ? 3 : G == 3 ? 4 : G == 4 ? 5 : 6))
                             : (G == 5 ? 6 : (G == 0 ? 0 : G == 2 ? 1 : G == 1 ? 2 : G == 3 ? 3 : G == 4 ? 4 : 5));
  const int padoff = 8 * hh * 56 + 224 + 8 * q4;
  LAS char* pid = c.L + (G == 7 ? padoff : rowb + G * 8);
  LAS char* pnat = c.L + (G == 7 ? padoff : rowb + natg * 8);

  u32x4 xf[NKS];
  float s1 = 0.f, s2 = 0.f;
  if (L == 0) {
    const rsrc_t xr = mkrs(p.x0, SEQ * DM * 2);
#pragma unroll
    for (int s = 0; s < NKS; ++s) xf[s] = ld16<0>(xr, r * 896 + hh * 16, (c.rb * 128 + w * 32) * 896, s * 32);
    load_img(c, p.wa, 3 * DM, DM + c.h * DH, L_K);
    load_img(c, p.wa, 3 * DM, 2 * DM + c.h * DH, L_V);
    load_img(c, p.wa, 3 * DM, c.h * DH, L_Q);
    load_img(c, p.woa, DM, c.h * DH, L_W);
    const bf16x2 one = __builtin_bit_cast(bf16x2, opaque(0x3f803f80));
#pragma unroll
    for (int s = 0; s < NKS; ++s)
#pragma unroll
      for (int j = 0; j < 4; ++j) { const unsigned e = xf[s][j]; const bf16x2 v = __builtin_bit_cast(bf16x2, e); s1 = __builtin_amdgcn_fdot2_f32_bf16(v, one, s1, false); s2 = __builtin_amdgcn_fdot2_f32_bf16(v, v, s2, false); }
    bar();
  } else {
    await(c, 0, 3 * L);
    STAMP(L * 12 + 0);
    const int xb = WS_X + (L - 1) * ACT + (c.rb * 4 + w) * TILE32;
    const int sb = WS_ST + (L - 1) * STSZ + (c.rb * 4 + w) * 4096;
    u32x2 st[8];
#pragma unroll
    for (int j = 0; j < 8; ++j) st[j] = ld8<AUX_SC1>(c.rs, hh * 2048 + r * 8, sb, j * 256);
#pragma unroll
    for (int s = 0; s < NKS; ++s) xf[s] = ld16<AUX_SC1>(c.rs, lane * 16, xb, s * 1024);
#pragma unroll
    for (int j = 0; j < 8; ++j) { s1 += __uint_as_float(st[j][0]); s2 += __uint_as_float(st[j][1]); }
  }
  s1 = xsum(s1); s2 = xsum(s2);
  const float mu = s1 * (1.0f / DM);
  const float rstd = __builtin_amdgcn_rsqf(fmaxf(s2 * (1.0f / DM) - mu * mu, 0.f) + 1e-5f);
  const float mr = mu * rstd;
  LAS float* rsl = (LAS float*)(c.L + L_RS) + w * 64;
  if (hh == 0) rsl[r] = rstd; else rsl[32 + r] = mr;

  LAS float* csl = (LAS float*)(c.L + L_CS) + (SOFTMAX ? 384 : 0);
  if (it == 0) {
    const u32x4 ones = {0x3f803f80u, 0x3f803f80u, 0x3f803f80u, 0x3f803f80u};
    f32x16 a0 = {}, a1 = {}, a2 = {};
#pragma unroll
    for (int s = 0; s < 7; ++s) {
      a0 = mfma(ones, wfrag(pid + L_Q + w * 7 * 896, s), a0);
      a1 = mfma(ones, wfrag(pid + L_K + w * 7 * 896, s), a1);
      a2 = mfma(ones, wfrag(pnat + L_V + w * 7 * 896, s), a2);
    }
    if (hh == 0) { csl[w * 96 + r] = a0[0]; csl[w * 96 + 32 + r] = a1[0]; csl[w * 96 + 64 + r] = a2[0]; }
    bar();
  }

  f32x16 ak = {}, av = {};
#pragma unroll
  for (int s = 0; s < NKS; ++s) {
    const u32x4 kf = wfrag(pid + L_K, s), vf = wfrag(pnat + L_V, s);
    if (SOFTMAX) ak = mfma(kf, xf[s], ak); else ak = mfma(xf[s], kf, ak);
    av = mfma(xf[s], vf, av);
  }
  STAMP(L * 12 + 1);
  f32x16 rr, rm;
#pragma unroll
  for (int g = 0; g < 4; ++g) {
    const f32x4 a = *(LAS f32x4*)(rsl + 8 * g + 4 * hh), b = *(LAS f32x4*)(rsl + 32 + 8 * g + 4 * hh);
#pragma unroll
    for (int j = 0; j < 4; ++j) { rr[4 * g + j] = a[j]; rm[4 * g + j] = b[j]; }
  }
  const float csv = (csl[64 + r] + csl[96 + 64 + r]) + (csl[192 + 64 + r] + csl[288 + 64 + r]);
#pragma unroll
  for (int i = 0; i < 16; ++i) { const float v = av[i] * rr[i] - rm[i] * csv; av[i] = r < 28 ? v : (r == 28 ? 1.0f : 0.f); }
  const u32x4 vf0 = pack8(av, 0), vf1 = pack8(av, 8);
  u32x4 kf0, kf1;
  if (SOFTMAX) {
#pragma unroll
    for (int g = 0; g < 4; ++g) {
      const LAS float* cp = csl + 32 + 8 * g + 4 * hh;
      const f32x4 cs = (*(LAS f32x4*)cp + *(LAS f32x4*)(cp + 96)) + (*(LAS f32x4*)(cp + 192) + *(LAS f32x4*)(cp + 288));
#pragma unroll
      for (int j = 0; j < 4; ++j) ak[4 * g + j] = ak[4 * g + j] * rstd - mr * cs[j];
    }
    if (hh) { ak[12] = 1.0f; ak[13] = 1.0f; ak[14] = 1.0f; ak[15] = 0.f; }
    kf0 = pack8(ak, 0); kf1 = pack8(ak, 8);
    const int tb = WS_KV + it * KVSZ + (c.h * 64 + c.rb * 4 + w) * 4096;
    const int rs23 = (r & 19) | ((r & 4) << 1) | ((r & 8) >> 1);
    __builtin_amdgcn_raw_buffer_store_b128(kf0, c.rs, tb + (hh * 32 + rs23) * 16, 0, AUX_SC1);
    __builtin_amdgcn_raw_buffer_store_b128(kf1, c.rs, tb + 1024 + (hh * 32 + rs23) * 16, 0, AUX_SC1);
    __builtin_amdgcn_raw_buffer_store_b128(vf0, c.rs, tb + 2048 + lane * 16, 0, AUX_SC1);
    __builtin_amdgcn_raw_buffer_store_b128(vf1, c.rs, tb + 3072 + lane * 16, 0, AUX_SC1);
  } else {
    const float csk = (csl[32 + r] + csl[96 + 32 + r]) + (csl[192 + 32 + r] + csl[288 + 32 + r]);
#pragma unroll
    for (int i = 0; i < 16; ++i) { const float v = phi(rbf(ak[i] * rr[i] - rm[i] * csk)); ak[i] = r < 28 ? v : 0.f; }
    f32x16 kv = mfma(pack8(ak, 0), vf0, f32x16{});
    kv = mfma(pack8(ak, 8), vf1, kv);
    LAS f32x4* ex = (LAS f32x4*)(c.L + L_S);
#pragma unroll
    for (int g = 0; g < 4; ++g) { const f32x4 t = {kv[4 * g], kv[4 * g + 1], kv[4 * g + 2], kv[4 * g + 3]}; ex[(g * 4 + w) * 64 + lane] = t; }
    bar();
    const f32x4 t = (ex[(w * 4 + 0) * 64 + lane] + ex[(w * 4 + 1) * 64 + lane]) + (ex[(w * 4 + 2) * 64 + lane] + ex[(w * 4 + 3) * 64 + lane]);
    __builtin_amdgcn_raw_buffer_store_b128(__builtin_bit_cast(u32x4, t), c.rs, WS_KP + it * KPSZ + (c.h * 16 + c.rb) * 4096 + w * 1024 + lane * 16, 0, AUX_SC1);
  }
  STAMP(L * 12 + 2);
  signal(c, 1, 3 * L + 1);
  STAMP(L * 12 + 3);

  f32x16 aq = {};
#pragma unroll
  for (int s = 0; s < NKS; ++s) aq = mfma(wfrag(pid + L_Q, s), xf[s], aq);
#pragma unroll
  for (int g = 0; g < 4; ++g) {
    const LAS float* cp = csl + 8 * g + 4 * hh;
    const f32x4 cs = (*(LAS f32x4*)cp + *(LAS f32x4*)(cp + 96)) + (*(LAS f32x4*)(cp + 192) + *(LAS f32x4*)(cp + 288));
#pragma unroll
    for (int j = 0; j < 4; ++j) aq[4 * g + j] = aq[4 * g + j] * rstd - mr * cs[j];
  }
  f32x16 o;
  if (!SOFTMAX) {
#pragma unroll
    for (int i = 0; i < 16; ++i) aq[i] = phi(rbf(aq[i]));
    if (hh) { aq[12] = 0.f; aq[13] = 0.f; aq[14] = 0.f; aq[15] = 0.f; }
    const u32x4 q0 = pack8(aq, 0), q1 = pack8(aq, 8);
    STAMP(L * 12 + 4);
    await(c, 1, 3 * L + 1);
    STAMP(L * 12 + 5);
    u32x4 pt[16];
    const int pb = WS_KP + it * KPSZ + c.h * 16 * 4096 + w * 1024;
#pragma unroll
    for (int j = 0; j < 16; ++j) pt[j] = ld16<AUX_SC1>(c.rs, lane * 16, pb, j * 4096);
    f32x4 t = {};
#pragma unroll
    for (int j = 0; j < 16; ++j) t += __builtin_bit_cast(f32x4, pt[j]);
    LAS u32x2* ex = (LAS u32x2*)(c.L + L_QX);
    { const u32x2 v = {pk(t[0], t[1]), pk(t[2], t[3])}; ex[((w >> 1) * 64 + lane) * 2 + (w & 1)] = v; }
    bar();
    const u32x4 a0 = *(LAS u32x4*)(c.L + L_QX + lane * 16), a1 = *(LAS u32x4*)(c.L + L_QX + 1024 + lane * 16);
    o = mfma(a0, q0, f32x16{});
    o = mfma(a1, q1, o);
    const float rd = 1.0f / (xhi(o[12]) + 1e-6f);
#pragma unroll
    for (int i = 0; i < 16; ++i) o[i] *= rd;
    STAMP(L * 12 + 6);
    store_slice(c, WS_O + L * ACT, o);
  } else {
    const float qs = 0.18898223650461362f * 1.4426950408889634f;
#pragma unroll
    for (int i = 0; i < 16; ++i) aq[i] *= qs;
    if (hh) { aq[12] = 0.f; aq[13] = 0.f; aq[14] = 0.f; aq[15] = 0.f; }
    const u32x4 q0 = pack8(aq, 0); u32x4 q1 = pack8(aq, 8);
    f32x16 s0 = mfma(kf0, q0, f32x16{});
    s0 = mfma(kf1, q1, s0);
    float m = s0[0];
#pragma unroll
    for (int i = 1; i < 16; ++i) m = fmaxf(m, s0[i]);
    m = xmax(m);
    LAS u32x4* qx = (LAS u32x4*)(c.L + L_QX);
    LAS unsigned* badw = (LAS unsigned*)(c.L + L_MISC);
    STAMP(L * 12 + 4);
    const int kb = WS_KV + it * KVSZ + (c.h * 64 + w * 16) * 4096;
    for (int pass = 0;; ++pass) {
      { const float m0 = rbf(m), m1 = rbf(m - m0), m2 = rbf((m - m0) - m1);
        if (hh) { q1[2] = pk(-m0, -m1); q1[3] = pk(-m2, 0.f); } }
      qx[(w * 2) * 64 + lane] = q0; qx[(w * 2 + 1) * 64 + lane] = q1;
      if (threadIdx.x == 0) *badw = 0u;
      if (pass == 0) await(c, 1, 3 * L + 1); else bar();
      STAMP(L * 12 + 5);
      u32x4 qf[8];
#pragma unroll
      for (int j = 0; j < 8; ++j) qf[j] = qx[j * 64 + lane];
      f32x16 oa[4] = {};
      u32x4 kv[4], kn[4];
#pragma unroll
      for (int j = 0; j < 4; ++j) kv[j] = ld16<AUX_SC1>(c.rs, lane * 16, kb, j * 1024);
      for (int t = 0; t < 16; ++t) {
        const int tn = t < 15 ? t + 1 : t;
#pragma unroll
        for (int j = 0; j < 4; ++j) kn[j] = ld16<AUX_SC1>(c.rs, lane * 16, kb + tn * 4096, j * 1024);
#pragma unroll
        for (int b = 0; b < 4; ++b) {
          f32x16 s = mfma(kv[0], qf[2 * b], f32x16{});
          s = mfma(kv[1], qf[2 * b + 1], s);
          f32x8 p0, p1;
#pragma unroll
          for (int i = 0; i < 8; ++i) { p0[i] = __builtin_amdgcn_exp2f(s[i]); p1[i] = __builtin_amdgcn_exp2f(s[8 + i]); }
          oa[b] = __builtin_amdgcn_smfmac_f32_32x32x32_bf16(__builtin_bit_cast(bf16x8, kv[2]), __builtin_bit_cast(bf16x16, p0), oa[b], 0xdddd, 0, 0);
          oa[b] = __builtin_amdgcn_smfmac_f32_32x32x32_bf16(__builtin_bit_cast(bf16x8, kv[3]), __builtin_bit_cast(bf16x16, p1), oa[b], 0xdddd, 0, 0);
        }
#pragma unroll
        for (int j = 0; j < 4; ++j) kv[j] = kn[j];
      }
      STAMP(L * 12 + 6);
      LAS f32x4* ex = (LAS f32x4*)(c.L + L_S);
#pragma unroll
      for (int b = 0; b < 4; ++b) {
        if (b != w) {
          const int slot = b * 3 + (w > b ? w - 1 : w);
#pragma unroll
          for (int g = 0; g < 4; ++g) { const f32x4 t = {oa[b][4 * g], oa[b][4 * g + 1], oa[b][4 * g + 2], oa[b][4 * g + 3]}; ex[(slot * 4 + g) * 64 + lane] = t; }
        } else o = oa[b];
      }
      bar();
#pragma unroll
      for (int k = 0; k < 3; ++k)
#pragma unroll
        for (int g = 0; g < 4; ++g) { const f32x4 t = ex[((w * 3 + k) * 4 + g) * 64 + lane];
#pragma unroll
          for (int j = 0; j < 4; ++j) o[4 * g + j] += t[j]; }
      const float l = xhi(o[12]);
      const float rl = 1.0f / l;
      float chk = 0.f;
#pragma unroll
      for (int i = 0; i < 16; ++i) { chk = __builtin_fmaf(o[i], 0.f, chk); o[i] *= rl; }
      const bool bad = !(l < 1.0e30f) || !(chk == 0.f);
      store_slice(c, WS_O + L * ACT, o);
      if (pass) break;
      if (__any(bad) && lane == 0) *badw = 1u;
      bar();
      if (*badw == 0u) break;
      float mm[4] = {-3.0e38f, -3.0e38f, -3.0e38f, -3.0e38f};
#pragma unroll
      for (int b = 0; b < 4; ++b) if (hh) { qf[2 * b + 1][2] = 0u; qf[2 * b + 1][3] = 0u; }
      for (int t = 0; t < 16; ++t) {
        const u32x4 k0 = ld16<AUX_SC1>(c.rs, lane * 16, kb + t * 4096, 0), k1 = ld16<AUX_SC1>(c.rs, lane * 16, kb + t * 4096, 1024);
#pragma unroll
        for (int b = 0; b < 4; ++b) {
          f32x16 s = mfma(k0, qf[2 * b], f32x16{});
          s = mfma(k1, qf[2 * b + 1], s);
#pragma unroll
          for (int i = 0; i < 16; ++i) mm[b] = fmaxf(mm[b], s[i]);
        }
      }
      LAS float* mx = (LAS float*)(c.L + L_S);
#pragma unroll
      for (int b = 0; b < 4; ++b) mx[(b * 4 + w) * 64 + lane] = mm[b];
      bar();
      m = fmaxf(fmaxf(mx[(w * 4) * 64 + lane], mx[(w * 4 + 1) * 64 + lane]), fmaxf(mx[(w * 4 + 2) * 64 + lane], mx[(w * 4 + 3) * 64 + lane]));
      m = xmax(m);
      bar();
    }
  }
  STAMP(L * 12 + 7);
  signal(c, 0, 3 * L + 2);
  if (L < 3) {
    const u16* wn = SOFTMAX ? p.wa : p.wb;
    load_img(c, wn, 3 * DM, DM + c.h * DH, L_K);
    load_img(c, wn, 3 * DM, 2 * DM + c.h * DH, L_V);
    load_img(c, wn, 3 * DM, c.h * DH, L_Q);
  }
  const int p0 = (7 * c.h + (c.h & 1)) >> 1;
  if (L == 0) {
    const GAS char* src = (const GAS char*)p.x0 + (c.rb * 128 + w * 32 + r) * 896;
    xa = *(const GAS u32x4*)(src + (p0 + hh) * 16);
    if (hh == 0) xb2 = *(const GAS u32x4*)(src + (p0 + 2) * 16);
    else { const u32x2 t = *(const GAS u32x2*)(src + ((c.h & 1) ? (p0 - 1) * 16 + 8 : (p0 + 3) * 16)); xb2[0] = t[0]; xb2[1] = t[1]; xb2[2] = 0u; xb2[3] = 0u; }
  }
  STAMP(L * 12 + 8);
  await(c, 0, 3 * L + 2);
  STAMP(L * 12 + 9);

  {
    const int ob = WS_O + L * ACT + (c.rb * 4 + w) * TILE32;
    u32x4 of[NKS];
#pragma unroll
    for (int s = 0; s < NKS; ++s) of[s] = ld16<AUX_SC1>(c.rs, lane * 16, ob, s * 1024);
    f32x16 y = {};
#pragma unroll
    for (int s = 0; s < NKS; ++s) y = mfma(wfrag(pnat + L_W, s), of[s], y);
    float t1 = 0.f, t2 = 0.f;
    f32x16 xs;
#pragma unroll
    for (int j = 0; j < 4; ++j) { xs[2 * j] = bflo(xa[j]); xs[2 * j + 1] = bfhi(xa[j]); xs[8 + 2 * j] = bflo(xb2[j]); xs[8 + 2 * j + 1] = bfhi(xb2[j]); }
#pragma unroll
    for (int i = 0; i < 16; ++i) { float v = rbf(xs[i] + rbf(y[i])); if (i >= 12 && hh) v = 0.f; xs[i] = v; t1 += v; t2 = __builtin_fmaf(v, v, t2); }
    xa = pack8(xs, 0); xb2 = pack8(xs, 8);
    STAMP(L * 12 + 10);
    if (L < 3) {
      store_slice(c, WS_X + L * ACT, xs);
      t1 = xsum(t1); t2 = xsum(t2);
      if (hh == 0) { const u32x2 t = {__float_as_uint(t1), __float_as_uint(t2)}; __builtin_amdgcn_raw_buffer_store_b64(t, c.rs, WS_ST + L * STSZ + (c.rb * 4 + w) * 4096 + c.h * 256 + r * 8, 0, AUX_SC1); }
      signal(c, 0, 3 * L + 3);
      load_img(c, SOFTMAX ? p.woa : p.wob, DM, c.h * DH, L_W);
    } else {
      GAS char* dst = (GAS char*)p.out + (c.rb * 128 + w * 32 + r) * 896;
      *(GAS u32x4*)(dst + (p0 + hh) * 16) = pack8(xs, 0);
      const u32x4 hi = pack8(xs, 8);
      if (hh == 0) *(GAS u32x4*)(dst + (p0 + 2) * 16) = hi;
      else { const u32x2 t = {hi[0], hi[1]}; *(GAS u32x2*)(dst + ((c.h & 1) ? (p0 - 1) * 16 + 8 : (p0 + 3) * 16)) = t; }
    }
    STAMP(L * 12 + 11);
  }
}

__global__ void __launch_bounds__(256, 1) fused_kernel(Params p) {
  extern __shared__ __attribute__((aligned(16))) char smem[];
  Ctx c;
  c.L = (LAS char*)smem; c.ws = p.ws;
  c.lane = threadIdx.x & 63; c.r = c.lane & 31; c.hh = c.lane >> 5; c.w = __builtin_amdgcn_readfirstlane(threadIdx.x >> 6);
  { const int x = blockIdx.x & 7, j = blockIdx.x >> 3; c.rb = (x & 3) * 4 + (j & 3); c.h = (x >> 2) * 8 + (j >> 2); }
  c.rs = __builtin_amdgcn_make_buffer_rsrc(p.ws, 0, WS_END, 0x00020000);
  STAMP(0);
  const u64 ev = __hip_atomic_load((GAS u64*)(p.ws + WS_EPOCH), RLX_AGENT);
  const u64 epoch = ((u64)(unsigned)__builtin_amdgcn_readfirstlane((int)(ev >> 32)) << 32) | (unsigned)__builtin_amdgcn_readfirstlane((int)ev);
  c.tag = epoch * 0x9E3779B97F4A7C15ull + 0x632BE59BD9B4E019ull;
  u32x4 xa = {}, xb = {};
  for (int it = 0; it < 2; ++it) {
    layer<false>(c, p, it, xa, xb);
    layer<true>(c, p, it, xa, xb);
  }
  if (threadIdx.x == 0) { const u64 d = ((LAS u64*)(c.L + L_MISC + 64))[1] - ((LAS u64*)(c.L + L_MISC + 64))[0]; const u64 t0 = __builtin_amdgcn_s_memrealtime(); while (__builtin_amdgcn_s_memrealtime() - t0 < 4 * d) {} }
  if (blockIdx.x == 0 && threadIdx.x == 0) __hip_atomic_store((GAS u64*)(p.ws + WS_EPOCH), epoch + 1, RLX_AGENT);
}

extern "C" void kernel_launch(void* const* d_in, const int* in_sizes, int n_in, void* d_out, int out_size, void* d_ws, size_t ws_size, hipStream_t stream) {
  Params p;
  p.x0 = (const u16*)d_in[0]; p.wa = (const u16*)d_in[1]; p.woa = (const u16*)d_in[2];
  p.wb = (const u16*)d_in[4]; p.wob = (const u16*)d_in[5]; p.out = (u16*)d_out; p.ws = (char*)d_ws;
  (void)hipFuncSetAttribute((const void*)fused_kernel, hipFuncAttributeMaxDynamicSharedMemorySize, LDS_BYTES);
  hipLaunchKernelGGL(fused_kernel, dim3(256), dim3(256), LDS_BYTES, stream, p);
}
```

```cpp
#include <hip/hip_runtime.h>
#define TLBPF 128
#define CODEPF 1

typedef unsigned short u16;
typedef unsigned long long u64;
typedef __bf16 bf16x2 __attribute__((ext_vector_type(2)));
typedef __bf16 bf16x8 __attribute__((ext_vector_type(8)));
typedef __bf16 bf16x16 __attribute__((ext_vector_type(16)));
typedef float f32x2 __attribute__((ext_vector_type(2)));
typedef float f32x4 __attribute__((ext_vector_type(4)));
typedef float f32x8 __attribute__((ext_vector_type(8)));
typedef float f32x16 __attribute__((ext_vector_type(16)));
typedef unsigned u32x2 __attribute__((ext_vector_type(2)));
typedef unsigned u32x4 __attribute__((ext_vector_type(4)));
typedef short s16x4 __attribute__((ext_vector_type(4)));
#define LAS __attribute__((address_space(3)))
#define GAS __attribute__((address_space(1)))
#define DI __device__ __forceinline__
#define RLX_AGENT __ATOMIC_RELAXED, __HIP_MEMORY_SCOPE_AGENT

constexpr int SEQ = 2048, DM = 448, NH = 16, DH = 28, NKS = 28;
constexpr int WS_FLAG = 0;
constexpr int WS_EPOCH = 131072;
constexpr int TILE32 = NKS * 1024;
constexpr int ACT = 64 * TILE32;
constexpr int WS_X = 1 << 18;
constexpr int WS_O = WS_X + 3 * ACT;
constexpr int WS_KV = WS_O + 4 * ACT;
constexpr int KVSZ = 16 * 64 * 4096;
constexpr int WS_KP = WS_KV + 2 * KVSZ;
constexpr int KPSZ = 16 * 16 * 4096;
constexpr int WS_ST = WS_KP + 2 * KPSZ;
constexpr int STSZ = 64 * 16 * 256;
constexpr int WS_END = WS_ST + 3 * STSZ;
constexpr int IMG = 448 * 56;
constexpr int L_Q = 0, L_K = IMG, L_V = 2 * IMG, L_W = 3 * IMG;
constexpr int L_S = 4 * IMG;
constexpr int L_QX = L_S + 49152;
constexpr int L_CS = L_QX + 8192;
constexpr int L_RS = L_CS + 3072;
constexpr int L_MISC = L_RS + 1024;
constexpr int LDS_BYTES = L_MISC + 256;
static_assert(LDS_BYTES <= 163840, "lds");
constexpr int AUX_SC1 = 16;

struct Params { const u16* x0; const u16* wa; const u16* woa; const u16* wb; const u16* wob; u16* out; char* ws; };

DI f32x16 mfma(u32x4 a, u32x4 b, f32x16 c) { return __builtin_amdgcn_mfma_f32_32x32x16_bf16(__builtin_bit_cast(bf16x8, a), __builtin_bit_cast(bf16x8, b), c, 0, 0, 0); }
DI unsigned pk(float lo, float hi) { f32x2 v = {lo, hi}; return __builtin_bit_cast(unsigned, __builtin_convertvector(v, bf16x2)); }
DI float rbf(float x) { return __uint_as_float(pk(x, x) & 0xffff0000u); }
DI float bflo(unsigned u) { return __uint_as_float(u << 16); }
DI float bfhi(unsigned u) { return __uint_as_float(u & 0xffff0000u); }
DI u32x4 pack8(const f32x16& v, int o) { u32x4 r = {pk(v[o], v[o + 1]), pk(v[o + 2], v[o + 3]), pk(v[o + 4], v[o + 5]), pk(v[o + 6], v[o + 7])}; return r; }
DI float xsum(float v) { auto r = __builtin_amdgcn_permlane32_swap(__float_as_uint(v), __float_as_uint(v), false, false); return __uint_as_float(r[0]) + __uint_as_float(r[1]); }
DI float xmax(float v) { auto r = __builtin_amdgcn_permlane32_swap(__float_as_uint(v), __float_as_uint(v), false, false); return fmaxf(__uint_as_float(r[0]), __uint_as_float(r[1])); }
DI float xhi(float v) { auto r = __builtin_amdgcn_permlane32_swap(__float_as_uint(v), __float_as_uint(v), false, false); return __uint_as_float(r[1]); }
DI u32x4 wfrag(LAS char* p, int s) {
  s16x4 a = __builtin_amdgcn_ds_read_tr16_b64_v4i16((LAS s16x4*)(p + s * 896));
  s16x4 b = __builtin_amdgcn_ds_read_tr16_b64_v4i16((LAS s16x4*)(p + s * 896 + 224));
  u32x2 x = __builtin_bit_cast(u32x2, a), y = __builtin_bit_cast(u32x2, b);
  u32x4 r = {x[0], x[1], y[0], y[1]}; return r;
}
DI void bar() { asm volatile("s_waitcnt lgkmcnt(0)\n\ts_barrier" ::: "memory"); }
DI void drain() { asm volatile("s_waitcnt vmcnt(0)" ::: "memory"); }

typedef __amdgpu_buffer_rsrc_t rsrc_t;
template <int AUX> DI u32x4 ld16(rsrc_t rs, int voff, int soff, int k) { return __builtin_amdgcn_raw_buffer_load_b128(rs, voff + (k & 4095), soff + (k & ~4095), AUX); }
template <int AUX> DI u32x2 ld8(rsrc_t rs, int voff, int soff, int k) { return __builtin_amdgcn_raw_buffer_load_b64(rs, voff + (k & 4095), soff + (k & ~4095), AUX); }
DI rsrc_t mkrs(const void* p, int bytes) { return __builtin_amdgcn_make_buffer_rsrc((void*)p, 0, bytes, 0x00020000); }
DI int opaque(int v) { asm volatile("" : "+v"(v)); return v; }
DI int opaque_s(int v) { asm volatile("" : "+s"(v)); return v; }
#define SB() __builtin_amdgcn_sched_barrier(0)
struct Ctx {
  int rb, h, w, lane, r, hh;
  char* ws; __amdgpu_buffer_rsrc_t rs; u64 tag; LAS char* L;
};

DI void signal(const Ctx& c, int kind, int seq) {
  drain(); bar();
  if (threadIdx.x < 16) {
    const int g = kind ? c.h : c.rb, me = kind ? c.rb : c.h;
    GAS u64* f = (GAS u64*)(c.ws + WS_FLAG + ((kind * 16 + g) * 16 + c.lane) * 256) + me;
    (void)__hip_atomic_exchange(f, c.tag + seq, RLX_AGENT);
  }
}
DI void await(const Ctx& c, int kind, int seq) {
  if (c.w == 3) {
    const int g = kind ? c.h : c.rb, me = kind ? c.rb : c.h;
    GAS u64* f = (GAS u64*)(c.ws + WS_FLAG + ((kind * 16 + g) * 16 + me) * 256) + (c.lane & 15);
    const u64 want = c.tag + seq;
    for (unsigned spins = 0; spins < (1u << 18); ++spins) {
      const u64 v = __hip_atomic_load(f, RLX_AGENT);
      if (__all((v - want) < 64ull)) break;
    }
  }
  __builtin_amdgcn_fence(__ATOMIC_ACQUIRE, "wavefront");
  bar();
}

DI void load_img(const Ctx& c, const u16* W, int ld, int col0, int img) {
  const int lane = opaque(c.lane);
  if (lane < 56) {
    const int rr = lane / 7, g = lane - rr * 7;
    const rsrc_t wr = mkrs(W, 448 * ld * 2);
    const int voff = (rr * ld + 4 * g) * 2, soff = opaque_s((8 * c.w * ld + col0) * 2);
    LAS char* dst = c.L + img + 8 * c.w * 56 + lane * 8;
    u32x2 v[14];
#pragma unroll
    for (int i = 0; i < 14; ++i) v[i] = __builtin_amdgcn_raw_buffer_load_b64(wr, voff, soff + i * 32 * ld * 2, 0);
#pragma unroll
    for (int i = 0; i < 14; ++i) *(LAS u32x2*)(dst + i * 32 * 56) = v[i];
  }
}

#define STAMP(i)

DI float phi(float t) { return (t > 0.f ? t + 1.0f : __expf(t)) + 1e-6f; }

DI void store_slice(const Ctx& c, int base, const f32x16& v) {
  const int p0 = (7 * c.h + (c.h & 1)) >> 1;
  const int rowoff = base + (c.rb * 4 + c.w) * TILE32 + c.r * 16;
  __builtin_amdgcn_raw_buffer_store_b128(pack8(v, 0), c.rs, rowoff + (p0 + c.hh) * 512, 0, AUX_SC1);
  const u32x4 hi = pack8(v, 8);
  if (c.hh == 0) __builtin_amdgcn_raw_buffer_store_b128(hi, c.rs, rowoff + (p0 + 2) * 512, 0, AUX_SC1);
  else { u32x2 t = {hi[0], hi[1]}; __builtin_amdgcn_raw_buffer_store_b64(t, c.rs, rowoff + ((c.h & 1) ? (p0 - 1) * 512 + 8 : (p0 + 3) * 512), 0, AUX_SC1); }
}

template <bool SOFTMAX>
DI void layer(Ctx c, const Params& p, int it, u32x4& xa, u32x4& xb2) {
  const int L = it * 2 + (SOFTMAX ? 1 : 0);
  c.lane = opaque(c.lane); c.r = c.lane & 31; c.hh = c.lane >> 5;
  c.w = opaque_s(c.w); c.h = opaque_s(c.h); c.rb = opaque_s(c.rb);
  const int lane = c.lane, r = c.r, hh = c.hh, w = c.w;
  const int G = ((lane >> 4) & 1) * 4 + (lane & 3), q4 = (lane & 15) >> 2;
  const int rowb = (8 * hh + q4) * 56;
  const int natg = (c.h & 1) ? (G == 5 ? 0 : (G == 0 ? 1 : G == 2 ? 2 : G == 1 ? 3 : G == 3 ? 4 : G == 4 ? 5 : 6))
                             : (G == 5 ? 6 : (G == 0 ? 0 : G == 2 ? 1 : G == 1 ? 2 : G == 3 ? 3 : G == 4 ? 4 : 5));
  const int padoff = 8 * hh * 56 + 224 + 8 * q4;
  LAS char* pid = c.L + (G == 7 ? padoff : rowb + G * 8);
  LAS char* pnat = c.L + (G == 7 ? padoff : rowb + natg * 8);

  u32x4 xf[NKS];
  float s1 = 0.f, s2 = 0.f;
  if (L == 0) {
    const rsrc_t xr = mkrs(p.x0, SEQ * DM * 2);
#pragma unroll
    for (int s = 0; s < NKS; ++s) xf[s] = ld16<0>(xr, r * 896 + hh * 16, (c.rb * 128 + w * 32) * 896, s * 32);
    load_img(c, p.wa, 3 * DM, DM + c.h * DH, L_K);
    load_img(c, p.wa, 3 * DM, 2 * DM + c.h * DH, L_V);
    load_img(c, p.wa, 3 * DM, c.h * DH, L_Q);
    load_img(c, p.woa, DM, c.h * DH, L_W);
    const bf16x2 one = __builtin_bit_cast(bf16x2, opaque(0x3f803f80));
#pragma unroll
    for (int s = 0; s < NKS; ++s)
#pragma unroll
      for (int j = 0; j < 4; ++j) { const unsigned e = xf[s][j]; const bf16x2 v = __builtin_bit_cast(bf16x2, e); s1 = __builtin_amdgcn_fdot2_f32_bf16(v, one, s1, false); s2 = __builtin_amdgcn_fdot2_f32_bf16(v, v, s2, false); }
    bar();
  } else {
    await(c, 0, 3 * L);
    STAMP(L * 12 + 0);
    const int xb = WS_X + (L - 1) * ACT + (c.rb * 4 + w) * TILE32;
    const int sb = WS_ST + (L - 1) * STSZ + (c.rb * 4 + w) * 4096;
    u32x2 st[8];
#pragma unroll
    for (int j = 0; j < 8; ++j) st[j] = ld8<AUX_SC1>(c.rs, hh * 2048 + r * 8, sb, j * 256);
#pragma unroll
    for (int s = 0; s < NKS; ++s) xf[s] = ld16<AUX_SC1>(c.rs, lane * 16, xb, s * 1024);
#pragma unroll
    for (int j = 0; j < 8; ++j) { s1 += __uint_as_float(st[j][0]); s2 += __uint_as_float(st[j][1]); }
  }
  s1 = xsum(s1); s2 = xsum(s2);
  const float mu = s1 * (1.0f / DM);
  const float rstd = __builtin_amdgcn_rsqf(fmaxf(s2 * (1.0f / DM) - mu * mu, 0.f) + 1e-5f);
  const float mr = mu * rstd;
  LAS float* rsl = (LAS float*)(c.L + L_RS) + w * 64;
  if (hh == 0) rsl[r] = rstd; else rsl[32 + r] = mr;

  LAS float* csl = (LAS float*)(c.L + L_CS) + (SOFTMAX ? 384 : 0);
  if (it == 0) {
    const u32x4 ones = {0x3f803f80u, 0x3f803f80u, 0x3f803f80u, 0x3f803f80u};
    f32x16 a0 = {}, a1 = {}, a2 = {};
#pragma unroll
    for (int s = 0; s < 7; ++s) {
      a0 = mfma(ones, wfrag(pid + L_Q + w * 7 * 896, s), a0);
      a1 = mfma(ones, wfrag(pid + L_K + w * 7 * 896, s), a1);
      a2 = mfma(ones, wfrag(pnat + L_V + w * 7 * 896, s), a2);
    }
    if (hh == 0) { csl[w * 96 + r] = a0[0]; csl[w * 96 + 32 + r] = a1[0]; csl[w * 96 + 64 + r] = a2[0]; }
    bar();
  }

  f32x16 ak = {}, av = {};
#pragma unroll
  for (int s = 0; s < NKS; ++s) {
    const u32x4 kf = wfrag(pid + L_K, s), vf = wfrag(pnat + L_V, s);
    if (SOFTMAX) ak = mfma(kf, xf[s], ak); else ak = mfma(xf[s], kf, ak);
    av = mfma(xf[s], vf, av);
  }
  STAMP(L * 12 + 1);
  f32x16 rr, rm;
#pragma unroll
  for (int g = 0; g < 4; ++g) {
    const f32x4 a = *(LAS f32x4*)(rsl + 8 * g + 4 * hh), b = *(LAS f32x4*)(rsl + 32 + 8 * g + 4 * hh);
#pragma unroll
    for (int j = 0; j < 4; ++j) { rr[4 * g + j] = a[j]; rm[4 * g + j] = b[j]; }
  }
  const float csv = (csl[64 + r] + csl[96 + 64 + r]) + (csl[192 + 64 + r] + csl[288 + 64 + r]);
#pragma unroll
  for (int i = 0; i < 16; ++i) { const float v = av[i] * rr[i] - rm[i] * csv; av[i] = r < 28 ? v : (r == 28 ? 1.0f : 0.f); }
  const u32x4 vf0 = pack8(av, 0), vf1 = pack8(av, 8);
  u32x4 kf0, kf1;
  if (SOFTMAX) {
#pragma unroll
    for (int g = 0; g < 4; ++g) {
      const LAS float* cp = csl + 32 + 8 * g + 4 * hh;
      const f32x4 cs = (*(LAS f32x4*)cp + *(LAS f32x4*)(cp + 96)) + (*(LAS f32x4*)(cp + 192) + *(LAS f32x4*)(cp + 288));
#pragma unroll
      for (int j = 0; j < 4; ++j) ak[4 * g + j] = ak[4 * g + j] * rstd - mr * cs[j];
    }
    if (hh) { ak[12] = 1.0f; ak[13] = 1.0f; ak[14] = 1.0f; ak[15] = 0.f; }
    kf0 = pack8(ak, 0); kf1 = pack8(ak, 8);
    const int tb = WS_KV + it * KVSZ + (c.h * 64 + c.rb * 4 + w) * 4096;
    const int rs23 = (r & 19) | ((r & 4) << 1) | ((r & 8) >> 1);
    __builtin_amdgcn_raw_buffer_store_b128(kf0, c.rs, tb + (hh * 32 + rs23) * 16, 0, AUX_SC1);
    __builtin_amdgcn_raw_buffer_store_b128(kf1, c.rs, tb + 1024 + (hh * 32 + rs23) * 16, 0, AUX_SC1);
    __builtin_amdgcn_raw_buffer_store_b128(vf0, c.rs, tb + 2048 + lane * 16, 0, AUX_SC1);
    __builtin_amdgcn_raw_buffer_store_b128(vf1, c.rs, tb + 3072 + lane * 16, 0, AUX_SC1);
  } else {
    const float csk = (csl[32 + r] + csl[96 + 32 + r]) + (csl[192 + 32 + r] + csl[288 + 32 + r]);
#pragma unroll
    for (int i = 0; i < 16; ++i) { const float v = phi(rbf(ak[i] * rr[i] - rm[i] * csk)); ak[i] = r < 28 ? v : 0.f; }
    f32x16 kv = mfma(pack8(ak, 0), vf0, f32x16{});
    kv = mfma(pack8(ak, 8), vf1, kv);
    LAS f32x4* ex = (LAS f32x4*)(c.L + L_S);
#pragma unroll
    for (int g = 0; g < 4; ++g) { const f32x4 t = {kv[4 * g], kv[4 * g + 1], kv[4 * g + 2], kv[4 * g + 3]}; ex[(g * 4 + w) * 64 + lane] = t; }
    bar();
    const f32x4 t = (ex[(w * 4 + 0) * 64 + lane] + ex[(w * 4 + 1) * 64 + lane]) + (ex[(w * 4 + 2) * 64 + lane] + ex[(w * 4 + 3) * 64 + lane]);
    __builtin_amdgcn_raw_buffer_store_b128(__builtin_bit_cast(u32x4, t), c.rs, WS_KP + it * KPSZ + (c.h * 16 + c.rb) * 4096 + w * 1024 + lane * 16, 0, AUX_SC1);
  }
  STAMP(L * 12 + 2);
  signal(c, 1, 3 * L + 1);
  STAMP(L * 12 + 3);

  f32x16 aq = {};
#pragma unroll
  for (int s = 0; s < NKS; ++s) aq = mfma(wfrag(pid + L_Q, s), xf[s], aq);
#pragma unroll
  for (int g = 0; g < 4; ++g) {
    const LAS float* cp = csl + 8 * g + 4 * hh;
    const f32x4 cs = (*(LAS f32x4*)cp + *(LAS f32x4*)(cp + 96)) + (*(LAS f32x4*)(cp + 192) + *(LAS f32x4*)(cp + 288));
#pragma unroll
    for (int j = 0; j < 4; ++j) aq[4 * g + j] = aq[4 * g + j] * rstd - mr * cs[j];
  }
  f32x16 o;
  if (!SOFTMAX) {
#pragma unroll
    for (int i = 0; i < 16; ++i) aq[i] = phi(rbf(aq[i]));
    if (hh) { aq[12] = 0.f; aq[13] = 0.f; aq[14] = 0.f; aq[15] = 0.f; }
    const u32x4 q0 = pack8(aq, 0), q1 = pack8(aq, 8);
    STAMP(L * 12 + 4);
    await(c, 1, 3 * L + 1);
    STAMP(L * 12 + 5);
    u32x4 pt[16];
    const int pb = WS_KP + it * KPSZ + c.h * 16 * 4096 + w * 1024;
#pragma unroll
    for (int j = 0; j < 16; ++j) pt[j] = ld16<AUX_SC1>(c.rs, lane * 16, pb, j * 4096);
    f32x4 t = {};
#pragma unroll
    for (int j = 0; j < 16; ++j) t += __builtin_bit_cast(f32x4, pt[j]);
    LAS u32x2* ex = (LAS u32x2*)(c.L + L_QX);
    { const u32x2 v = {pk(t[0], t[1]), pk(t[2], t[3])}; ex[((w >> 1) * 64 + lane) * 2 + (w & 1)] = v; }
    bar();
    const u32x4 a0 = *(LAS u32x4*)(c.L + L_QX + lane * 16), a1 = *(LAS u32x4*)(c.L + L_QX + 1024 + lane * 16);
    o = mfma(a0, q0, f32x16{});
    o = mfma(a1, q1, o);
    const float rd = 1.0f / (xhi(o[12]) + 1e-6f);
#pragma unroll
    for (int i = 0; i < 16; ++i) o[i] *= rd;
    STAMP(L * 12 + 6);
    store_slice(c, WS_O + L * ACT, o);
  } else {
    const float qs = 0.18898223650461362f * 1.4426950408889634f;
#pragma unroll
    for (int i = 0; i < 16; ++i) aq[i] *= qs;
    if (hh) { aq[12] = 0.f; aq[13] = 0.f; aq[14] = 0.f; aq[15] = 0.f; }
    const u32x4 q0 = pack8(aq, 0); u32x4 q1 = pack8(aq, 8);
    f32x16 s0 = mfma(kf0, q0, f32x16{});
    s0 = mfma(kf1, q1, s0);
    float m = s0[0];
#pragma unroll
    for (int i = 1; i < 16; ++i) m = fmaxf(m, s0[i]);
    m = xmax(m);
    LAS u32x4* qx = (LAS u32x4*)(c.L + L_QX);
    LAS unsigned* badw = (LAS unsigned*)(c.L + L_MISC);
    STAMP(L * 12 + 4);
    const int kb = WS_KV + it * KVSZ + (c.h * 64 + w * 16) * 4096;
    for (int pass = 0;; ++pass) {
      { const float m0 = rbf(m), m1 = rbf(m - m0), m2 = rbf((m - m0) - m1);
        if (hh) { q1[2] = pk(-m0, -m1); q1[3] = pk(-m2, 0.f); } }
      qx[(w * 2) * 64 + lane] = q0; qx[(w * 2 + 1) * 64 + lane] = q1;
      if (threadIdx.x == 0) *badw = 0u;
      if (pass == 0) await(c, 1, 3 * L + 1); else bar();
      STAMP(L * 12 + 5);
      u32x4 qf[8];
#pragma unroll
      for (int j = 0; j < 8; ++j) qf[j] = qx[j * 64 + lane];
      f32x16 oa[4] = {};
      u32x4 kv[4], kn[4];
#pragma unroll
      for (int j = 0; j < 4; ++j) kv[j] = ld16<AUX_SC1>(c.rs, lane * 16, kb, j * 1024);
      for (int t = 0; t < 16; ++t) {
        const int tn = t < 15 ? t + 1 : t;
#pragma unroll
        for (int j = 0; j < 4; ++j) kn[j] = ld16<AUX_SC1>(c.rs, lane * 16, kb + tn * 4096, j * 1024);
#pragma unroll
        for (int b = 0; b < 4; ++b) {
          f32x16 s = mfma(kv[0], qf[2 * b], f32x16{});
          s = mfma(kv[1], qf[2 * b + 1], s);
          f32x8 p0, p1;
#pragma unroll
          for (int i = 0; i < 8; ++i) { p0[i] = __builtin_amdgcn_exp2f(s[i]); p1[i] = __builtin_amdgcn_exp2f(s[8 + i]); }
          oa[b] = __builtin_amdgcn_smfmac_f32_32x32x32_bf16(__builtin_bit_cast(bf16x8, kv[2]), __builtin_bit_cast(bf16x16, p0), oa[b], 0xdddd, 0, 0);
          oa[b] = __builtin_amdgcn_smfmac_f32_32x32x32_bf16(__builtin_bit_cast(bf16x8, kv[3]), __builtin_bit_cast(bf16x16, p1), oa[b], 0xdddd, 0, 0);
        }
#pragma unroll
        for (int j = 0; j < 4; ++j) kv[j] = kn[j];
      }
      STAMP(L * 12 + 6);
      LAS f32x4* ex = (LAS f32x4*)(c.L + L_S);
#pragma unroll
      for (int b = 0; b < 4; ++b) {
        if (b != w) {
          const int slot = b * 3 + (w > b ? w - 1 : w);
#pragma unroll
          for (int g = 0; g < 4; ++g) { const f32x4 t = {oa[b][4 * g], oa[b][4 * g + 1], oa[b][4 * g + 2], oa[b][4 * g + 3]}; ex[(slot * 4 + g) * 64 + lane] = t; }
        } else o = oa[b];
      }
      bar();
#pragma unroll
      for (int k = 0; k < 3; ++k)
#pragma unroll
        for (int g = 0; g < 4; ++g) { const f32x4 t = ex[((w * 3 + k) * 4 + g) * 64 + lane];
#pragma unroll
          for (int j = 0; j < 4; ++j) o[4 * g + j] += t[j]; }
      const float l = xhi(o[12]);
      const float rl = 1.0f / l;
      float chk = 0.f;
#pragma unroll
      for (int i = 0; i < 16; ++i) { chk = __builtin_fmaf(o[i], 0.f, chk); o[i] *= rl; }
      const bool bad = !(l < 1.0e30f) || !(chk == 0.f);
      store_slice(c, WS_O + L * ACT, o);
      if (pass) break;
      if (__any(bad) && lane == 0) *badw = 1u;
      bar();
      if (*badw == 0u) break;
      float mm[4] = {-3.0e38f, -3.0e38f, -3.0e38f, -3.0e38f};
#pragma unroll
      for (int b = 0; b < 4; ++b) if (hh) { qf[2 * b + 1][2] = 0u; qf[2 * b + 1][3] = 0u; }
      for (int t = 0; t < 16; ++t) {
        const u32x4 k0 = ld16<AUX_SC1>(c.rs, lane * 16, kb + t * 4096, 0), k1 = ld16<AUX_SC1>(c.rs, lane * 16, kb + t * 4096, 1024);
#pragma unroll
        for (int b = 0; b < 4; ++b) {
          f32x16 s = mfma(k0, qf[2 * b], f32x16{});
          s = mfma(k1, qf[2 * b + 1], s);
#pragma unroll
          for (int i = 0; i < 16; ++i) mm[b] = fmaxf(mm[b], s[i]);
        }
      }
      LAS float* mx = (LAS float*)(c.L + L_S);
#pragma unroll
      for (int b = 0; b < 4; ++b) mx[(b * 4 + w) * 64 + lane] = mm[b];
      bar();
      m = fmaxf(fmaxf(mx[(w * 4) * 64 + lane], mx[(w * 4 + 1) * 64 + lane]), fmaxf(mx[(w * 4 + 2) * 64 + lane], mx[(w * 4 + 3) * 64 + lane]));
      m = xmax(m);
      bar();
    }
  }
  STAMP(L * 12 + 7);
  signal(c, 0, 3 * L + 2);
  if (L < 3) {
    const u16* wn = SOFTMAX ? p.wa : p.wb;
    load_img(c, wn, 3 * DM, DM + c.h * DH, L_K);
    load_img(c, wn, 3 * DM, 2 * DM + c.h * DH, L_V);
    load_img(c, wn, 3 * DM, c.h * DH, L_Q);
  }
  const int p0 = (7 * c.h + (c.h & 1)) >> 1;
  if (L == 0) {
    const GAS char* src = (const GAS char*)p.x0 + (c.rb * 128 + w * 32 + r) * 896;
    xa = *(const GAS u32x4*)(src + (p0 + hh) * 16);
    if (hh == 0) xb2 = *(const GAS u32x4*)(src + (p0 + 2) * 16);
    else { const u32x2 t = *(const GAS u32x2*)(src + ((c.h & 1) ? (p0 - 1) * 16 + 8 : (p0 + 3) * 16)); xb2[0] = t[0]; xb2[1] = t[1]; xb2[2] = 0u; xb2[3] = 0u; }
  }
  STAMP(L * 12 + 8);
  await(c, 0, 3 * L + 2);
  STAMP(L * 12 + 9);

  {
    const int ob = WS_O + L * ACT + (c.rb * 4 + w) * TILE32;
    u32x4 of[NKS];
#pragma unroll
    for (int s = 0; s < NKS; ++s) of[s] = ld16<AUX_SC1>(c.rs, lane * 16, ob, s * 1024);
    f32x16 y = {};
#pragma unroll
    for (int s = 0; s < NKS; ++s) y = mfma(wfrag(pnat + L_W, s), of[s], y);
    float t1 = 0.f, t2 = 0.f;
    f32x16 xs;
#pragma unroll
    for (int j = 0; j < 4; ++j) { xs[2 * j] = bflo(xa[j]); xs[2 * j + 1] = bfhi(xa[j]); xs[8 + 2 * j] = bflo(xb2[j]); xs[8 + 2 * j + 1] = bfhi(xb2[j]); }
#pragma unroll
    for (int i = 0; i < 16; ++i) { float v = rbf(xs[i] + rbf(y[i])); if (i >= 12 && hh) v = 0.f; xs[i] = v; t1 += v; t2 = __builtin_fmaf(v, v, t2); }
    xa = pack8(xs, 0); xb2 = pack8(xs, 8);
    STAMP(L * 12 + 10);
    if (L < 3) {
      store_slice(c, WS_X + L * ACT, xs);
      t1 = xsum(t1); t2 = xsum(t2);
      if (hh == 0) { const u32x2 t = {__float_as_uint(t1), __float_as_uint(t2)}; __builtin_amdgcn_raw_buffer_store_b64(t, c.rs, WS_ST + L * STSZ + (c.rb * 4 + w) * 4096 + c.h * 256 + r * 8, 0, AUX_SC1); }
      signal(c, 0, 3 * L + 3);
      load_img(c, SOFTMAX ? p.woa : p.wob, DM, c.h * DH, L_W);
    } else {
      GAS char* dst = (GAS char*)p.out + (c.rb * 128 + w * 32 + r) * 896;
      *(GAS u32x4*)(dst + (p0 + hh) * 16) = pack8(xs, 0);
      const u32x4 hi = pack8(xs, 8);
      if (hh == 0) *(GAS u32x4*)(dst + (p0 + 2) * 16) = hi;
      else { const u32x2 t = {hi[0], hi[1]}; *(GAS u32x2*)(dst + ((c.h & 1) ? (p0 - 1) * 16 + 8 : (p0 + 3) * 16)) = t; }
    }
    STAMP(L * 12 + 11);
  }
}

__global__ void __launch_bounds__(256, 1) fused_kernel(Params p) {
  extern __shared__ __attribute__((aligned(16))) char smem[];
  Ctx c;
  c.L = (LAS char*)smem; c.ws = p.ws;
  c.lane = threadIdx.x & 63; c.r = c.lane & 31; c.hh = c.lane >> 5; c.w = __builtin_amdgcn_readfirstlane(threadIdx.x >> 6);
  { const int x = blockIdx.x & 7, j = blockIdx.x >> 3; c.rb = (x & 3) * 4 + (j & 3); c.h = (x >> 2) * 8 + (j >> 2); }
  c.rs = __builtin_amdgcn_make_buffer_rsrc(p.ws, 0, WS_END, 0x00020000);
  STAMP(0);
  { u64 pc; asm volatile("s_getpc_b64 %0" : "=s"(pc));
    const GAS unsigned* cp = (const GAS unsigned*)(pc & ~127ull) + threadIdx.x * 32;
    unsigned a, b;
    asm volatile("global_load_dword %0, %1, off" : "=v"(a) : "v"(cp));
    if (threadIdx.x < 44) asm volatile("global_load_dword %0, %1, off" : "=v"(b) : "v"(cp + 8192));
    asm volatile("s_waitcnt vmcnt(0)" ::: "memory"); }
  { unsigned a = __builtin_amdgcn_raw_buffer_load_b32(c.rs, threadIdx.x * (TLBPF * 1024) + 128 * (blockIdx.x >> 3), 0, AUX_SC1);
    const u16* q = (c.w == 0) ? p.wb : (c.w == 1) ? p.wob : (c.w == 2) ? p.woa : p.out;
    const rsrc_t qr = mkrs(q, (c.w == 0) ? 448 * 1344 * 2 : (c.w == 3) ? 2048 * 448 * 2 : 448 * 448 * 2);
    unsigned b = __builtin_amdgcn_raw_buffer_load_b32(qr, c.lane * 32768, 0, 0);
    asm volatile("" :: "v"(a), "v"(b)); }
  const u64 ev = __hip_atomic_load((GAS u64*)(p.ws + WS_EPOCH), RLX_AGENT);
  const u64 epoch = ((u64)(unsigned)__builtin_amdgcn_readfirstlane((int)(ev >> 32)) << 32) | (unsigned)__builtin_amdgcn_readfirstlane((int)ev);
  c.tag = epoch * 0x9E3779B97F4A7C15ull + 0x632BE59BD9B4E019ull;
  u32x4 xa = {}, xb = {};
  for (int it = 0; it < 2; ++it) {
    layer<false>(c, p, it, xa, xb);
    layer<true>(c, p, it, xa, xb);
  }
  if (blockIdx.x == 0 && threadIdx.x == 0) __hip_atomic_store((GAS u64*)(p.ws + WS_EPOCH), epoch + 1, RLX_AGENT);
}

extern "C" void kernel_launch(void* const* d_in, const int* in_sizes, int n_in, void* d_out, int out_size, void* d_ws, size_t ws_size, hipStream_t stream) {
  Params p;
  p.x0 = (const u16*)d_in[0]; p.wa = (const u16*)d_in[1]; p.woa = (const u16*)d_in[2];
  p.wb = (const u16*)d_in[4]; p.wob = (const u16*)d_in[5]; p.out = (u16*)d_out; p.ws = (char*)d_ws;
  (void)hipFuncSetAttribute((const void*)fused_kernel, hipFuncAttributeMaxDynamicSharedMemorySize, LDS_BYTES);
  hipLaunchKernelGGL(fused_kernel, dim3(256), dim3(256), LDS_BYTES, stream, p);
}
```

```cpp
#include <hip/hip_runtime.h>
#define TA 100
#define TB 101

typedef unsigned short u16;
typedef unsigned long long u64;
typedef __bf16 bf16x2 __attribute__((ext_vector_type(2)));
typedef __bf16 bf16x8 __attribute__((ext_vector_type(8)));
typedef __bf16 bf16x16 __attribute__((ext_vector_type(16)));
typedef float f32x2 __attribute__((ext_vector_type(2)));
typedef float f32x4 __attribute__((ext_vector_type(4)));
typedef float f32x8 __attribute__((ext_vector_type(8)));
typedef float f32x16 __attribute__((ext_vector_type(16)));
typedef unsigned u32x2 __attribute__((ext_vector_type(2)));
typedef unsigned u32x4 __attribute__((ext_vector_type(4)));
typedef short s16x4 __attribute__((ext_vector_type(4)));
#define LAS __attribute__((address_space(3)))
#define GAS __attribute__((address_space(1)))
#define DI __device__ __forceinline__
#define RLX_AGENT __ATOMIC_RELAXED, __HIP_MEMORY_SCOPE_AGENT

constexpr int SEQ = 2048, DM = 448, NH = 16, DH = 28, NKS = 28;
constexpr int WS_FLAG = 0;
constexpr int WS_EPOCH = 131072;
constexpr int TILE32 = NKS * 1024;
constexpr int ACT = 64 * TILE32;
constexpr int WS_X = 1 << 18;
constexpr int WS_O = WS_X + 3 * ACT;
constexpr int WS_KV = WS_O + 4 * ACT;
constexpr int KVSZ = 16 * 64 * 4096;
constexpr int WS_KP = WS_KV + 2 * KVSZ;
constexpr int KPSZ = 16 * 16 * 4096;
constexpr int WS_ST = WS_KP + 2 * KPSZ;
constexpr int STSZ = 64 * 16 * 256;
constexpr int WS_END = WS_ST + 3 * STSZ;
constexpr int IMG = 448 * 56;
constexpr int L_Q = 0, L_K = IMG, L_V = 2 * IMG, L_W = 3 * IMG;
constexpr int L_S = 4 * IMG;
constexpr int L_QX = L_S + 49152;
constexpr int L_CS = L_QX + 8192;
constexpr int L_RS = L_CS + 3072;
constexpr int L_MISC = L_RS + 1024;
constexpr int LDS_BYTES = L_MISC + 256;
static_assert(LDS_BYTES <= 163840, "lds");
constexpr int AUX_SC1 = 16;

struct Params { const u16* x0; const u16* wa; const u16* woa; const u16* wb; const u16* wob; u16* out; char* ws; };

DI f32x16 mfma(u32x4 a, u32x4 b, f32x16 c) { return __builtin_amdgcn_mfma_f32_32x32x16_bf16(__builtin_bit_cast(bf16x8, a), __builtin_bit_cast(bf16x8, b), c, 0, 0, 0); }
DI unsigned pk(float lo, float hi) { f32x2 v = {lo, hi}; return __builtin_bit_cast(unsigned, __builtin_convertvector(v, bf16x2)); }
DI float rbf(float x) { return __uint_as_float(pk(x, x) & 0xffff0000u); }
DI float bflo(unsigned u) { return __uint_as_float(u << 16); }
DI float bfhi(unsigned u) { return __uint_as_float(u & 0xffff0000u); }
DI u32x4 pack8(const f32x16& v, int o) { u32x4 r = {pk(v[o], v[o + 1]), pk(v[o + 2], v[o + 3]), pk(v[o + 4], v[o + 5]), pk(v[o + 6], v[o + 7])}; return r; }
DI float xsum(float v) { auto r = __builtin_amdgcn_permlane32_swap(__float_as_uint(v), __float_as_uint(v), false, false); return __uint_as_float(r[0]) + __uint_as_float(r[1]); }
DI float xmax(float v) { auto r = __builtin_amdgcn_permlane32_swap(__float_as_uint(v), __float_as_uint(v), false, false); return fmaxf(__uint_as_float(r[0]), __uint_as_float(r[1])); }
DI float xhi(float v) { auto r = __builtin_amdgcn_permlane32_swap(__float_as_uint(v), __float_as_uint(v), false, false); return __uint_as_float(r[1]); }
DI u32x4 wfrag(LAS char* p, int s) {
  s16x4 a = __builtin_amdgcn_ds_read_tr16_b64_v4i16((LAS s16x4*)(p + s * 896));
  s16x4 b = __builtin_amdgcn_ds_read_tr16_b64_v4i16((LAS s16x4*)(p + s * 896 + 224));
  u32x2 x = __builtin_bit_cast(u32x2, a), y = __builtin_bit_cast(u32x2, b);
  u32x4 r = {x[0], x[1], y[0], y[1]}; return r;
}
DI void bar() { asm volatile("s_waitcnt lgkmcnt(0)\n\ts_barrier" ::: "memory"); }
DI void drain() { asm volatile("s_waitcnt vmcnt(0)" ::: "memory"); }

typedef __amdgpu_buffer_rsrc_t rsrc_t;
template <int AUX> DI u32x4 ld16(rsrc_t rs, int voff, int soff, int k) { return __builtin_amdgcn_raw_buffer_load_b128(rs, voff + (k & 4095), soff + (k & ~4095), AUX); }
template <int AUX> DI u32x2 ld8(rsrc_t rs, int voff, int soff, int k) { return __builtin_amdgcn_raw_buffer_load_b64(rs, voff + (k & 4095), soff + (k & ~4095), AUX); }
DI rsrc_t mkrs(const void* p, int bytes) { return __builtin_amdgcn_make_buffer_rsrc((void*)p, 0, bytes, 0x00020000); }
DI int opaque(int v) { asm volatile("" : "+v"(v)); return v; }
DI int opaque_s(int v) { asm volatile("" : "+s"(v)); return v; }
#define SB() __builtin_amdgcn_sched_barrier(0)
struct Ctx {
  int rb, h, w, lane, r, hh;
  char* ws; __amdgpu_buffer_rsrc_t rs; u64 tag; LAS char* L;
};

DI void signal(const Ctx& c, int kind, int seq) {
  drain(); bar();
  if (threadIdx.x < 16) {
    const int g = kind ? c.h : c.rb, me = kind ? c.rb : c.h;
    GAS u64* f = (GAS u64*)(c.ws + WS_FLAG + ((kind * 16 + g) * 16 + c.lane) * 256) + me;
    (void)__hip_atomic_exchange(f, c.tag + seq, RLX_AGENT);
  }
}
DI void await(const Ctx& c, int kind, int seq) {
  if (c.w == 3) {
    const int g = kind ? c.h : c.rb, me = kind ? c.rb : c.h;
    GAS u64* f = (GAS u64*)(c.ws + WS_FLAG + ((kind * 16 + g) * 16 + me) * 256) + (c.lane & 15);
    const u64 want = c.tag + seq;
    for (unsigned spins = 0; spins < (1u << 18); ++spins) {
      const u64 v = __hip_atomic_load(f, RLX_AGENT);
      if (__all((v - want) < 64ull)) break;
    }
  }
  __builtin_amdgcn_fence(__ATOMIC_ACQUIRE, "wavefront");
  bar();
}

DI void load_img(const Ctx& c, const u16* W, int ld, int col0, int img) {
  const int lane = opaque(c.lane);
  if (lane < 56) {
    const int rr = lane / 7, g = lane - rr * 7;
    const rsrc_t wr = mkrs(W, 448 * ld * 2);
    const int voff = (rr * ld + 4 * g) * 2, soff = opaque_s((8 * c.w * ld + col0) * 2);
    LAS char* dst = c.L + img + 8 * c.w * 56 + lane * 8;
    u32x2 v[14];
#pragma unroll
    for (int i = 0; i < 14; ++i) v[i] = __builtin_amdgcn_raw_buffer_load_b64(wr, voff, soff + i * 32 * ld * 2, 0);
#pragma unroll
    for (int i = 0; i < 14; ++i) *(LAS u32x2*)(dst + i * 32 * 56) = v[i];
  }
}

#define STAMP(i) do { if (((i) == TA || (i) == TB) && threadIdx.x == 0) ((LAS u64*)(c.L + L_MISC + 64))[(i) == TB] = __builtin_amdgcn_s_memrealtime(); } while (0)

DI float phi(float t) { return (t > 0.f ? t + 1.0f : __expf(t)) + 1e-6f; }

DI void store_slice(const Ctx& c, int base, const f32x16& v) {
  const int p0 = (7 * c.h + (c.h & 1)) >> 1;
  const int rowoff = base + (c.rb * 4 + c.w) * TILE32 + c.r * 16;
  __builtin_amdgcn_raw_buffer_store_b128(pack8(v, 0), c.rs, rowoff + (p0 + c.hh) * 512, 0, AUX_SC1);
  const u32x4 hi = pack8(v, 8);
  if (c.hh == 0) __builtin_amdgcn_raw_buffer_store_b128(hi, c.rs, rowoff + (p0 + 2) * 512, 0, AUX_SC1);
  else { u32x2 t = {hi[0], hi[1]}; __builtin_amdgcn_raw_buffer_store_b64(t, c.rs, rowoff + ((c.h & 1) ? (p0 - 1) * 512 + 8 : (p0 + 3) * 512), 0, AUX_SC1); }
}

template <bool SOFTMAX>
DI void layer(Ctx c, const Params& p, int it, u32x4& xa, u32x4& xb2) {
  const int L = it * 2 + (SOFTMAX ? 1 : 0);
  c.lane = opaque(c.lane); c.r = c.lane & 31; c.hh = c.lane >> 5;
  c.w = opaque_s(c.w); c.h = opaque_s(c.h); c.rb = opaque_s(c.rb);
  const int lane = c.lane, r = c.r, hh = c.hh, w = c.w;
  const int G = ((lane >> 4) & 1) * 4 + (lane & 3), q4 = (lane & 15) >> 2;
  const int rowb = (8 * hh + q4) * 56;
  const int natg = (c.h & 1) ? (G == 5 ? 0 : (G == 0 ? 1 : G == 2 ? 2 : G == 1 ? 3 : G == 3 ? 4 : G == 4 ? 5 : 6))
                             : (G == 5 ? 6 : (G == 0 ? 0 : G == 2 ? 1 : G == 1 ? 2 : G == 3 ? 3 : G == 4 ? 4 : 5));
  const int padoff = 8 * hh * 56 + 224 + 8 * q4;
  LAS char* pid = c.L + (G == 7 ? padoff : rowb + G * 8);
  LAS char* pnat = c.L + (G == 7 ? padoff : rowb + natg * 8);

  u32x4 xf[NKS];
  float s1 = 0.f, s2 = 0.f;
  if (L == 0) {
    const rsrc_t xr = mkrs(p.x0, SEQ * DM * 2);
#pragma unroll
    for (int s = 0; s < NKS; ++s) xf[s] = ld16<0>(xr, r * 896 + hh * 16, (c.rb * 128 + w * 32) * 896, s * 32);
    load_img(c, p.wa, 3 * DM, DM + c.h * DH, L_K);
    load_img(c, p.wa, 3 * DM, 2 * DM + c.h * DH, L_V);
    load_img(c, p.wa, 3 * DM, c.h * DH, L_Q);
    load_img(c, p.woa, DM, c.h * DH, L_W);
    const bf16x2 one = __builtin_bit_cast(bf16x2, opaque(0x3f803f80));
#pragma unroll
    for (int s = 0; s < NKS; ++s)
#pragma unroll
      for (int j = 0; j < 4; ++j) { const unsigned e = xf[s][j]; const bf16x2 v = __builtin_bit_cast(bf16x2, e); s1 = __builtin_amdgcn_fdot2_f32_bf16(v, one, s1, false); s2 = __builtin_amdgcn_fdot2_f32_bf16(v, v, s2, false); }
    bar();
  } else {
    await(c, 0, 3 * L);
    STAMP(L * 12 + 0);
    const int xb = WS_X + (L - 1) * ACT + (c.rb * 4 + w) * TILE32;
    const int sb = WS_ST + (L - 1) * STSZ + (c.rb * 4 + w) * 4096;
    u32x2 st[8];
#pragma unroll
    for (int j = 0; j < 8; ++j) st[j] = ld8<AUX_SC1>(c.rs, hh * 2048 + r * 8, sb, j * 256);
#pragma unroll
    for (int s = 0; s < NKS; ++s) xf[s] = ld16<AUX_SC1>(c.rs, lane * 16, xb, s * 1024);
#pragma unroll
    for (int j = 0; j < 8; ++j) { s1 += __uint_as_float(st[j][0]); s2 += __uint_as_float(st[j][1]); }
  }
  s1 = xsum(s1); s2 = xsum(s2);
  const float mu = s1 * (1.0f / DM);
  const float rstd = __builtin_amdgcn_rsqf(fmaxf(s2 * (1.0f / DM) - mu * mu, 0.f) + 1e-5f);
  const float mr = mu * rstd;
  LAS float* rsl = (LAS float*)(c.L + L_RS) + w * 64;
  if (hh == 0) rsl[r] = rstd; else rsl[32 + r] = mr;

  LAS float* csl = (LAS float*)(c.L + L_CS) + (SOFTMAX ? 384 : 0);
  if (it == 0) {
    const u32x4 ones = {0x3f803f80u, 0x3f803f80u, 0x3f803f80u, 0x3f803f80u};
    f32x16 a0 = {}, a1 = {}, a2 = {};
#pragma unroll
    for (int s = 0; s < 7; ++s) {
      a0 = mfma(ones, wfrag(pid + L_Q + w * 7 * 896, s), a0);
      a1 = mfma(ones, wfrag(pid + L_K + w * 7 * 896, s), a1);
      a2 = mfma(ones, wfrag(pnat + L_V + w * 7 * 896, s), a2);
    }
    if (hh == 0) { csl[w * 96 + r] = a0[0]; csl[w * 96 + 32 + r] = a1[0]; csl[w * 96 + 64 + r] = a2[0]; }
    bar();
  }

  f32x16 ak = {}, av = {};
#pragma unroll
  for (int s = 0; s < NKS; ++s) {
    const u32x4 kf = wfrag(pid + L_K, s), vf = wfrag(pnat + L_V, s);
    if (SOFTMAX) ak = mfma(kf, xf[s], ak); else ak = mfma(xf[s], kf, ak);
    av = mfma(xf[s], vf, av);
  }
  STAMP(L * 12 + 1);
  f32x16 rr, rm;
#pragma unroll
  for (int g = 0; g < 4; ++g) {
    const f32x4 a = *(LAS f32x4*)(rsl + 8 * g + 4 * hh), b = *(LAS f32x4*)(rsl + 32 + 8 * g + 4 * hh);
#pragma unroll
    for (int j = 0; j < 4; ++j) { rr[4 * g + j] = a[j]; rm[4 * g + j] = b[j]; }
  }
  const float csv = (csl[64 + r] + csl[96 + 64 + r]) + (csl[192 + 64 + r] + csl[288 + 64 + r]);
#pragma unroll
  for (int i = 0; i < 16; ++i) { const float v = av[i] * rr[i] - rm[i] * csv; av[i] = r < 28 ? v : (r == 28 ? 1.0f : 0.f); }
  const u32x4 vf0 = pack8(av, 0), vf1 = pack8(av, 8);
  u32x4 kf0, kf1;
  if (SOFTMAX) {
#pragma unroll
    for (int g = 0; g < 4; ++g) {
      const LAS float* cp = csl + 32 + 8 * g + 4 * hh;
      const f32x4 cs = (*(LAS f32x4*)cp + *(LAS f32x4*)(cp + 96)) + (*(LAS f32x4*)(cp + 192) + *(LAS f32x4*)(cp + 288));
#pragma unroll
      for (int j = 0; j < 4; ++j) ak[4 * g + j] = ak[4 * g + j] * rstd - mr * cs[j];
    }
    if (hh) { ak[12] = 1.0f; ak[13] = 1.0f; ak[14] = 1.0f; ak[15] = 0.f; }
    kf0 = pack8(ak, 0); kf1 = pack8(ak, 8);
    const int tb = WS_KV + it * KVSZ + (c.h * 64 + c.rb * 4 + w) * 4096;
    const int rs23 = (r & 19) | ((r & 4) << 1) | ((r & 8) >> 1);
    __builtin_amdgcn_raw_buffer_store_b128(kf0, c.rs, tb + (hh * 32 + rs23) * 16, 0, AUX_SC1);
    __builtin_amdgcn_raw_buffer_store_b128(kf1, c.rs, tb + 1024 + (hh * 32 + rs23) * 16, 0, AUX_SC1);
    __builtin_amdgcn_raw_buffer_store_b128(vf0, c.rs, tb + 2048 + lane * 16, 0, AUX_SC1);
    __builtin_amdgcn_raw_buffer_store_b128(vf1, c.rs, tb + 3072 + lane * 16, 0, AUX_SC1);
  } else {
    const float csk = (csl[32 + r] + csl[96 + 32 + r]) + (csl[192 + 32 + r] + csl[288 + 32 + r]);
#pragma unroll
    for (int i = 0; i < 16; ++i) { const float v = phi(rbf(ak[i] * rr[i] - rm[i] * csk)); ak[i] = r < 28 ? v : 0.f; }
    f32x16 kv = mfma(pack8(ak, 0), vf0, f32x16{});
    kv = mfma(pack8(ak, 8), vf1, kv);
    LAS f32x4* ex = (LAS f32x4*)(c.L + L_S);
#pragma unroll
    for (int g = 0; g < 4; ++g) { const f32x4 t = {kv[4 * g], kv[4 * g + 1], kv[4 * g + 2], kv[4 * g + 3]}; ex[(g * 4 + w) * 64 + lane] = t; }
    bar();
    const f32x4 t = (ex[(w * 4 + 0) * 64 + lane] + ex[(w * 4 + 1) * 64 + lane]) + (ex[(w * 4 + 2) * 64 + lane] + ex[(w * 4 + 3) * 64 + lane]);
    __builtin_amdgcn_raw_buffer_store_b128(__builtin_bit_cast(u32x4, t), c.rs, WS_KP + it * KPSZ + (c.h * 16 + c.rb) * 4096 + w * 1024 + lane * 16, 0, AUX_SC1);
  }
  STAMP(L * 12 + 2);
  signal(c, 1, 3 * L + 1);
  STAMP(L * 12 + 3);

  f32x16 aq = {};
#pragma unroll
  for (int s = 0; s < NKS; ++s) aq = mfma(wfrag(pid + L_Q, s), xf[s], aq);
#pragma unroll
  for (int g = 0; g < 4; ++g) {
    const LAS float* cp = csl + 8 * g + 4 * hh;
    const f32x4 cs = (*(LAS f32x4*)cp + *(LAS f32x4*)(cp + 96)) + (*(LAS f32x4*)(cp + 192) + *(LAS f32x4*)(cp + 288));
#pragma unroll
    for (int j = 0; j < 4; ++j) aq[4 * g + j] = aq[4 * g + j] * rstd - mr * cs[j];
  }
  f32x16 o;
  if (!SOFTMAX) {
#pragma unroll
    for (int i = 0; i < 16; ++i) aq[i] = phi(rbf(aq[i]));
    if (hh) { aq[12] = 0.f; aq[13] = 0.f; aq[14] = 0.f; aq[15] = 0.f; }
    const u32x4 q0 = pack8(aq, 0), q1 = pack8(aq, 8);
    STAMP(L * 12 + 4);
    await(c, 1, 3 * L + 1);
    STAMP(L * 12 + 5);
    u32x4 pt[16];
    const int pb = WS_KP + it * KPSZ + c.h * 16 * 4096 + w * 1024;
#pragma unroll
    for (int j = 0; j < 16; ++j) pt[j] = ld16<AUX_SC1>(c.rs, lane * 16, pb, j * 4096);
    f32x4 t = {};
#pragma unroll
    for (int j = 0; j < 16; ++j) t += __builtin_bit_cast(f32x4, pt[j]);
    LAS u32x2* ex = (LAS u32x2*)(c.L + L_QX);
    { const u32x2 v = {pk(t[0], t[1]), pk(t[2], t[3])}; ex[((w >> 1) * 64 + lane) * 2 + (w & 1)] = v; }
    bar();
    const u32x4 a0 = *(LAS u32x4*)(c.L + L_QX + lane * 16), a1 = *(LAS u32x4*)(c.L + L_QX + 1024 + lane * 16);
    o = mfma(a0, q0, f32x16{});
    o = mfma(a1, q1, o);
    const float rd = 1.0f / (xhi(o[12]) + 1e-6f);
#pragma unroll
    for (int i = 0; i < 16; ++i) o[i] *= rd;
    STAMP(L * 12 + 6);
    store_slice(c, WS_O + L * ACT, o);
  } else {
    const float qs = 0.18898223650461362f * 1.4426950408889634f;
#pragma unroll
    for (int i = 0; i < 16; ++i) aq[i] *= qs;
    if (hh) { aq[12] = 0.f; aq[13] = 0.f; aq[14] = 0.f; aq[15] = 0.f; }
    const u32x4 q0 = pack8(aq, 0); u32x4 q1 = pack8(aq, 8);
    f32x16 s0 = mfma(kf0, q0, f32x16{});
    s0 = mfma(kf1, q1, s0);
    float m = s0[0];
#pragma unroll
    for (int i = 1; i < 16; ++i) m = fmaxf(m, s0[i]);
    m = xmax(m);
    LAS u32x4* qx = (LAS u32x4*)(c.L + L_QX);
    LAS unsigned* badw = (LAS unsigned*)(c.L + L_MISC);
    STAMP(L * 12 + 4);
    const int kb = WS_KV + it * KVSZ + (c.h * 64 + w * 16) * 4096;
    for (int pass = 0;; ++pass) {
      { const float m0 = rbf(m), m1 = rbf(m - m0), m2 = rbf((m - m0) - m1);
        if (hh) { q1[2] = pk(-m0, -m1); q1[3] = pk(-m2, 0.f); } }
      qx[(w * 2) * 64 + lane] = q0; qx[(w * 2 + 1) * 64 + lane] = q1;
      if (threadIdx.x == 0) *badw = 0u;
      if (pass == 0) await(c, 1, 3 * L + 1); else bar();
      STAMP(L * 12 + 5);
      u32x4 qf[8];
#pragma unroll
      for (int j = 0; j < 8; ++j) qf[j] = qx[j * 64 + lane];
      f32x16 oa[4] = {};
      u32x4 kv[4], kn[4];
#pragma unroll
      for (int j = 0; j < 4; ++j) kv[j] = ld16<AUX_SC1>(c.rs, lane * 16, kb, j * 1024);
      for (int t = 0; t < 16; ++t) {
        const int tn = t < 15 ? t + 1 : t;
#pragma unroll
        for (int j = 0; j < 4; ++j) kn[j] = ld16<AUX_SC1>(c.rs, lane * 16, kb + tn * 4096, j * 1024);
#pragma unroll
        for (int b = 0; b < 4; ++b) {
          f32x16 s = mfma(kv[0], qf[2 * b], f32x16{});
          s = mfma(kv[1], qf[2 * b + 1], s);
          f32x8 p0, p1;
#pragma unroll
          for (int i = 0; i < 8; ++i) { p0[i] = __builtin_amdgcn_exp2f(s[i]); p1[i] = __builtin_amdgcn_exp2f(s[8 + i]); }
          oa[b] = __builtin_amdgcn_smfmac_f32_32x32x32_bf16(__builtin_bit_cast(bf16x8, kv[2]), __builtin_bit_cast(bf16x16, p0), oa[b], 0xdddd, 0, 0);
          oa[b] = __builtin_amdgcn_smfmac_f32_32x32x32_bf16(__builtin_bit_cast(bf16x8, kv[3]), __builtin_bit_cast(bf16x16, p1), oa[b], 0xdddd, 0, 0);
        }
#pragma unroll
        for (int j = 0; j < 4; ++j) kv[j] = kn[j];
      }
      STAMP(L * 12 + 6);
      LAS f32x4* ex = (LAS f32x4*)(c.L + L_S);
#pragma unroll
      for (int b = 0; b < 4; ++b) {
        if (b != w) {
          const int slot = b * 3 + (w > b ? w - 1 : w);
#pragma unroll
          for (int g = 0; g < 4; ++g) { const f32x4 t = {oa[b][4 * g], oa[b][4 * g + 1], oa[b][4 * g + 2], oa[b][4 * g + 3]}; ex[(slot * 4 + g) * 64 + lane] = t; }
        } else o = oa[b];
      }
      bar();
#pragma unroll
      for (int k = 0; k < 3; ++k)
#pragma unroll
        for (int g = 0; g < 4; ++g) { const f32x4 t = ex[((w * 3 + k) * 4 + g) * 64 + lane];
#pragma unroll
          for (int j = 0; j < 4; ++j) o[4 * g + j] += t[j]; }
      const float l = xhi(o[12]);
      const float rl = 1.0f / l;
      float chk = 0.f;
#pragma unroll
      for (int i = 0; i < 16; ++i) { chk = __builtin_fmaf(o[i], 0.f, chk); o[i] *= rl; }
      const bool bad = !(l < 1.0e30f) || !(chk == 0.f);
      store_slice(c, WS_O + L * ACT, o);
      if (pass) break;
      if (__any(bad) && lane == 0) *badw = 1u;
      bar();
      if (*badw == 0u) break;
      float mm[4] = {-3.0e38f, -3.0e38f, -3.0e38f, -3.0e38f};
#pragma unroll
      for (int b = 0; b < 4; ++b) if (hh) { qf[2 * b + 1][2] = 0u; qf[2 * b + 1][3] = 0u; }
      for (int t = 0; t < 16; ++t) {
        const u32x4 k0 = ld16<AUX_SC1>(c.rs, lane * 16, kb + t * 4096, 0), k1 = ld16<AUX_SC1>(c.rs, lane * 16, kb + t * 4096, 1024);
#pragma unroll
        for (int b = 0; b < 4; ++b) {
          f32x16 s = mfma(k0, qf[2 * b], f32x16{});
          s = mfma(k1, qf[2 * b + 1], s);
#pragma unroll
          for (int i = 0; i < 16; ++i) mm[b] = fmaxf(mm[b], s[i]);
        }
      }
      LAS float* mx = (LAS float*)(c.L + L_S);
#pragma unroll
      for (int b = 0; b < 4; ++b) mx[(b * 4 + w) * 64 + lane] = mm[b];
      bar();
      m = fmaxf(fmaxf(mx[(w * 4) * 64 + lane], mx[(w * 4 + 1) * 64 + lane]), fmaxf(mx[(w * 4 + 2) * 64 + lane], mx[(w * 4 + 3) * 64 + lane]));
      m = xmax(m);
      bar();
    }
  }
  STAMP(L * 12 + 7);
  signal(c, 0, 3 * L + 2);
  if (L < 3) {
    const u16* wn = SOFTMAX ? p.wa : p.wb;
    load_img(c, wn, 3 * DM, DM + c.h * DH, L_K);
    load_img(c, wn, 3 * DM, 2 * DM + c.h * DH, L_V);
    load_img(c, wn, 3 * DM, c.h * DH, L_Q);
  }
  const int p0 = (7 * c.h + (c.h & 1)) >> 1;
  if (L == 0) {
    const GAS char* src = (const GAS char*)p.x0 + (c.rb * 128 + w * 32 + r) * 896;
    xa = *(const GAS u32x4*)(src + (p0 + hh) * 16);
    if (hh == 0) xb2 = *(const GAS u32x4*)(src + (p0 + 2) * 16);
    else { const u32x2 t = *(const GAS u32x2*)(src + ((c.h & 1) ? (p0 - 1) * 16 + 8 : (p0 + 3) * 16)); xb2[0] = t[0]; xb2[1] = t[1]; xb2[2] = 0u; xb2[3] = 0u; }
  }
  STAMP(L * 12 + 8);
  await(c, 0, 3 * L + 2);
  STAMP(L * 12 + 9);

  {
    const int ob = WS_O + L * ACT + (c.rb * 4 + w) * TILE32;
    u32x4 of[NKS];
#pragma unroll
    for (int s = 0; s < NKS; ++s) of[s] = ld16<AUX_SC1>(c.rs, lane * 16, ob, s * 1024);
    f32x16 y = {};
#pragma unroll
    for (int s = 0; s < NKS; ++s) y = mfma(wfrag(pnat + L_W, s), of[s], y);
    float t1 = 0.f, t2 = 0.f;
    f32x16 xs;
#pragma unroll
    for (int j = 0; j < 4; ++j) { xs[2 * j] = bflo(xa[j]); xs[2 * j + 1] = bfhi(xa[j]); xs[8 + 2 * j] = bflo(xb2[j]); xs[8 + 2 * j + 1] = bfhi(xb2[j]); }
#pragma unroll
    for (int i = 0; i < 16; ++i) { float v = rbf(xs[i] + rbf(y[i])); if (i >= 12 && hh) v = 0.f; xs[i] = v; t1 += v; t2 = __builtin_fmaf(v, v, t2); }
    xa = pack8(xs, 0); xb2 = pack8(xs, 8);
    STAMP(L * 12 + 10);
    if (L < 3) {
      store_slice(c, WS_X + L * ACT, xs);
      t1 = xsum(t1); t2 = xsum(t2);
      if (hh == 0) { const u32x2 t = {__float_as_uint(t1), __float_as_uint(t2)}; __builtin_amdgcn_raw_buffer_store_b64(t, c.rs, WS_ST + L * STSZ + (c.rb * 4 + w) * 4096 + c.h * 256 + r * 8, 0, AUX_SC1); }
      signal(c, 0, 3 * L + 3);
      load_img(c, SOFTMAX ? p.woa : p.wob, DM, c.h * DH, L_W);
    } else {
      GAS char* dst = (GAS char*)p.out + (c.rb * 128 + w * 32 + r) * 896;
      *(GAS u32x4*)(dst + (p0 + hh) * 16) = pack8(xs, 0);
      const u32x4 hi = pack8(xs, 8);
      if (hh == 0) *(GAS u32x4*)(dst + (p0 + 2) * 16) = hi;
      else { const u32x2 t = {hi[0], hi[1]}; *(GAS u32x2*)(dst + ((c.h & 1) ? (p0 - 1) * 16 + 8 : (p0 + 3) * 16)) = t; }
    }
    STAMP(L * 12 + 11);
  }
}

__global__ void __launch_bounds__(256, 1) fused_kernel(Params p) {
  extern __shared__ __attribute__((aligned(16))) char smem[];
  u64 tk0; asm volatile("s_memrealtime %0" : "=s"(tk0));
  __builtin_amdgcn_sched_barrier(0);
  Ctx c;
  c.L = (LAS char*)smem; c.ws = p.ws;
  c.lane = threadIdx.x & 63; c.r = c.lane & 31; c.hh = c.lane >> 5; c.w = __builtin_amdgcn_readfirstlane(threadIdx.x >> 6);
  { const int x = blockIdx.x & 7, j = blockIdx.x >> 3; c.rb = (x & 3) * 4 + (j & 3); c.h = (x >> 2) * 8 + (j >> 2); }
  c.rs = __builtin_amdgcn_make_buffer_rsrc(p.ws, 0, WS_END, 0x00020000);
  asm volatile("s_waitcnt lgkmcnt(0)" :: "s"(p.ws), "s"(p.x0), "s"(p.out) : "memory");
  u64 tk1; asm volatile("s_memrealtime %0\n s_waitcnt lgkmcnt(0)" : "=s"(tk1));
  if (threadIdx.x == 0) { ((LAS u64*)(c.L + L_MISC + 64))[0] = tk0; ((LAS u64*)(c.L + L_MISC + 64))[1] = tk1; }
  const u64 ev = __hip_atomic_load((GAS u64*)(p.ws + WS_EPOCH), RLX_AGENT);
  const u64 epoch = ((u64)(unsigned)__builtin_amdgcn_readfirstlane((int)(ev >> 32)) << 32) | (unsigned)__builtin_amdgcn_readfirstlane((int)ev);
  c.tag = epoch * 0x9E3779B97F4A7C15ull + 0x632BE59BD9B4E019ull;
  u32x4 xa = {}, xb = {};
  for (int it = 0; it < 2; ++it) {
    layer<false>(c, p, it, xa, xb);
    layer<true>(c, p, it, xa, xb);
  }
  if (threadIdx.x == 0) { const u64 d = ((LAS u64*)(c.L + L_MISC + 64))[1] - ((LAS u64*)(c.L + L_MISC + 64))[0]; const u64 t0 = __builtin_amdgcn_s_memrealtime(); while (__builtin_amdgcn_s_memrealtime() - t0 < 20 * d) {} }
  if (blockIdx.x == 0 && threadIdx.x == 0) __hip_atomic_store((GAS u64*)(p.ws + WS_EPOCH), epoch + 1, RLX_AGENT);
}

extern "C" void kernel_launch(void* const* d_in, const int* in_sizes, int n_in, void* d_out, int out_size, void* d_ws, size_t ws_size, hipStream_t stream) {
  Params p;
  p.x0 = (const u16*)d_in[0]; p.wa = (const u16*)d_in[1]; p.woa = (const u16*)d_in[2];
  p.wb = (const u16*)d_in[4]; p.wob = (const u16*)d_in[5]; p.out = (u16*)d_out; p.ws = (char*)d_ws;
  (void)hipFuncSetAttribute((const void*)fused_kernel, hipFuncAttributeMaxDynamicSharedMemorySize, LDS_BYTES);
  hipLaunchKernelGGL(fused_kernel, dim3(256), dim3(256), LDS_BYTES, stream, p);
}
```

```cpp
#include <hip/hip_runtime.h>

typedef unsigned short u16;
typedef unsigned long long u64;
typedef __bf16 bf16x2 __attribute__((ext_vector_type(2)));
typedef __bf16 bf16x8 __attribute__((ext_vector_type(8)));
typedef __bf16 bf16x16 __attribute__((ext_vector_type(16)));
typedef float f32x2 __attribute__((ext_vector_type(2)));
typedef float f32x4 __attribute__((ext_vector_type(4)));
typedef float f32x8 __attribute__((ext_vector_type(8)));
typedef float f32x16 __attribute__((ext_vector_type(16)));
typedef unsigned u32x2 __attribute__((ext_vector_type(2)));
typedef unsigned u32x4 __attribute__((ext_vector_type(4)));
typedef short s16x4 __attribute__((ext_vector_type(4)));
#define LAS __attribute__((address_space(3)))
#define GAS __attribute__((address_space(1)))
#define DI __device__ __forceinline__
#define RLX_AGENT __ATOMIC_RELAXED, __HIP_MEMORY_SCOPE_AGENT

constexpr int SEQ = 2048, DM = 448, NH = 16, DH = 28, NKS = 28;
constexpr int WS_FLAG = 0;
constexpr int WS_EPOCH = 131072;
constexpr int TILE32 = NKS * 1024;
constexpr int ACT = 64 * TILE32;
constexpr int WS_X = 1 << 18;
constexpr int WS_O = WS_X + 3 * ACT;
constexpr int WS_KV = WS_O + 4 * ACT;
constexpr int KVSZ = 16 * 64 * 4096;
constexpr int WS_KP = WS_KV + 2 * KVSZ;
constexpr int KPSZ = 16 * 16 * 4096;
constexpr int WS_ST = WS_KP + 2 * KPSZ;
constexpr int STSZ = 64 * 16 * 256;
constexpr int WS_END = WS_ST + 3 * STSZ;
constexpr int IMG = 448 * 56;
constexpr int L_Q = 0, L_K = IMG, L_V = 2 * IMG, L_W = 3 * IMG;
constexpr int L_S = 4 * IMG;
constexpr int L_QX = L_S + 49152;
constexpr int L_CS = L_QX + 8192;
constexpr int L_RS = L_CS + 3072;
constexpr int L_MISC = L_RS + 1024;
constexpr int LDS_BYTES = L_MISC + 256;
static_assert(LDS_BYTES <= 163840, "lds");
constexpr int AUX_SC1 = 16;

struct Params { const u16* x0; const u16* wa; const u16* woa; const u16* wb; const u16* wob; u16* out; char* ws; };

DI f32x16 mfma(u32x4 a, u32x4 b, f32x16 c) { return __builtin_amdgcn_mfma_f32_32x32x16_bf16(__builtin_bit_cast(bf16x8, a), __builtin_bit_cast(bf16x8, b), c, 0, 0, 0); }
DI unsigned pk(float lo, float hi) { f32x2 v = {lo, hi}; return __builtin_bit_cast(unsigned, __builtin_convertvector(v, bf16x2)); }
DI float rbf(float x) { return __uint_as_float(pk(x, x) & 0xffff0000u); }
DI float bflo(unsigned u) { return __uint_as_float(u << 16); }
DI float bfhi(unsigned u) { return __uint_as_float(u & 0xffff0000u); }
DI u32x4 pack8(const f32x16& v, int o) { u32x4 r = {pk(v[o], v[o + 1]), pk(v[o + 2], v[o + 3]), pk(v[o + 4], v[o + 5]), pk(v[o + 6], v[o + 7])}; return r; }
DI float xsum(float v) { auto r = __builtin_amdgcn_permlane32_swap(__float_as_uint(v), __float_as_uint(v), false, false); return __uint_as_float(r[0]) + __uint_as_float(r[1]); }
DI float xmax(float v) { auto r = __builtin_amdgcn_permlane32_swap(__float_as_uint(v), __float_as_uint(v), false, false); return fmaxf(__uint_as_float(r[0]), __uint_as_float(r[1])); }
DI float xhi(float v) { auto r = __builtin_amdgcn_permlane32_swap(__float_as_uint(v), __float_as_uint(v), false, false); return __uint_as_float(r[1]); }
DI u32x4 wfrag(LAS char* p, int s) {
  s16x4 a = __builtin_amdgcn_ds_read_tr16_b64_v4i16((LAS s16x4*)(p + s * 896));
  s16x4 b = __builtin_amdgcn_ds_read_tr16_b64_v4i16((LAS s16x4*)(p + s * 896 + 224));
  u32x2 x = __builtin_bit_cast(u32x2, a), y = __builtin_bit_cast(u32x2, b);
  u32x4 r = {x[0], x[1], y[0], y[1]}; return r;
}
DI void bar() { asm volatile("s_waitcnt lgkmcnt(0)\n\ts_barrier" ::: "memory"); }
DI void drain() { asm volatile("s_waitcnt vmcnt(0)" ::: "memory"); }

typedef __amdgpu_buffer_rsrc_t rsrc_t;
template <int AUX> DI u32x4 ld16(rsrc_t rs, int voff, int soff, int k) { return __builtin_amdgcn_raw_buffer_load_b128(rs, voff + (k & 4095), soff + (k & ~4095), AUX); }
template <int AUX> DI u32x2 ld8(rsrc_t rs, int voff, int soff, int k) { return __builtin_amdgcn_raw_buffer_load_b64(rs, voff + (k & 4095), soff + (k & ~4095), AUX); }
DI rsrc_t mkrs(const void* p, int bytes) { return __builtin_amdgcn_make_buffer_rsrc((void*)p, 0, bytes, 0x00020000); }
DI int opaque(int v) { asm volatile("" : "+v"(v)); return v; }
DI int opaque_s(int v) { asm volatile("" : "+s"(v)); return v; }
#define SB() __builtin_amdgcn_sched_barrier(0)
struct Ctx {
  int rb, h, w, lane, r, hh;
  char* ws; __amdgpu_buffer_rsrc_t rs; u64 tag; LAS char* L;
};

DI void signal(const Ctx& c, int kind, int seq) {
  drain(); bar();
  if (threadIdx.x < 16) {
    const int g = kind ? c.h : c.rb, me = kind ? c.rb : c.h;
    GAS u64* f = (GAS u64*)(c.ws + WS_FLAG + ((kind * 16 + g) * 16 + c.lane) * 256) + me;
    (void)__hip_atomic_exchange(f, c.tag + seq, RLX_AGENT);
  }
}
DI void await(const Ctx& c, int kind, int seq) {
  if (c.w == 3) {
    const int g = kind ? c.h : c.rb, me = kind ? c.rb : c.h;
    GAS u64* f = (GAS u64*)(c.ws + WS_FLAG + ((kind * 16 + g) * 16 + me) * 256) + (c.lane & 15);
    const u64 want = c.tag + seq;
    for (unsigned spins = 0; spins < (1u << 18); ++spins) {
      const u64 v = __hip_atomic_load(f, RLX_AGENT);
      if (__all((v - want) < 64ull)) break;
    }
  }
  __builtin_amdgcn_fence(__ATOMIC_ACQUIRE, "wavefront");
  bar();
}

DI void load_img(const Ctx& c, const u16* W, int ld, int col0, int img) {
  const int lane = opaque(c.lane);
  if (lane < 56) {
    const int rr = lane / 7, g = lane - rr * 7;
    const rsrc_t wr = mkrs(W, 448 * ld * 2);
    const int voff = (rr * ld + 4 * g) * 2, soff = opaque_s((8 * c.w * ld + col0) * 2);
    LAS char* dst = c.L + img + 8 * c.w * 56 + lane * 8;
    u32x2 v[14];
#pragma unroll
    for (int i = 0; i < 14; ++i) v[i] = __builtin_amdgcn_raw_buffer_load_b64(wr, voff, soff + i * 32 * ld * 2, 0);
#pragma unroll
    for (int i = 0; i < 14; ++i) *(LAS u32x2*)(dst + i * 32 * 56) = v[i];
  }
}

#define STAMP(i)

DI float phi(float t) { return (t > 0.f ? t + 1.0f : __expf(t)) + 1e-6f; }

DI void store_slice(const Ctx& c, int base, const f32x16& v) {
  const int p0 = (7 * c.h + (c.h & 1)) >> 1;
  const int rowoff = base + (c.rb * 4 + c.w) * TILE32 + c.r * 16;
  __builtin_amdgcn_raw_buffer_store_b128(pack8(v, 0), c.rs, rowoff + (p0 + c.hh) * 512, 0, AUX_SC1);
  const u32x4 hi = pack8(v, 8);
  if (c.hh == 0) __builtin_amdgcn_raw_buffer_store_b128(hi, c.rs, rowoff + (p0 + 2) * 512, 0, AUX_SC1);
  else { u32x2 t = {hi[0], hi[1]}; __builtin_amdgcn_raw_buffer_store_b64(t, c.rs, rowoff + ((c.h & 1) ? (p0 - 1) * 512 + 8 : (p0 + 3) * 512), 0, AUX_SC1); }
}

template <bool SOFTMAX>
DI void layer(Ctx c, const Params& p, int it, u32x4& xa, u32x4& xb2) {
  const int L = it * 2 + (SOFTMAX ? 1 : 0);
  c.lane = opaque(c.lane); c.r = c.lane & 31; c.hh = c.lane >> 5;
  c.w = opaque_s(c.w); c.h = opaque_s(c.h); c.rb = opaque_s(c.rb);
  const int lane = c.lane, r = c.r, hh = c.hh, w = c.w;
  const int G = ((lane >> 4) & 1) * 4 + (lane & 3), q4 = (lane & 15) >> 2;
  const int rowb = (8 * hh + q4) * 56;
  const int natg = (c.h & 1) ? (G == 5 ? 0 : (G == 0 ? 1 : G == 2 ? 2 : G == 1 ? 3 : G == 3 ? 4 : G == 4 ? 5 : 6))
                             : (G == 5 ? 6 : (G == 0 ? 0 : G == 2 ? 1 : G == 1 ? 2 : G == 3 ? 3 : G == 4 ? 4 : 5));
  const int padoff = 8 * hh * 56 + 224 + 8 * q4;
  LAS char* pid = c.L + (G == 7 ? padoff : rowb + G * 8);
  LAS char* pnat = c.L + (G == 7 ? padoff : rowb + natg * 8);

  u32x4 xf[NKS];
  float s1 = 0.f, s2 = 0.f;
  if (L == 0) {
    const rsrc_t xr = mkrs(p.x0, SEQ * DM * 2);
#pragma unroll
    for (int s = 0; s < NKS; ++s) xf[s] = ld16<0>(xr, r * 896 + hh * 16, (c.rb * 128 + w * 32) * 896, s * 32);
    load_img(c, p.wa, 3 * DM, DM + c.h * DH, L_K);
    load_img(c, p.wa, 3 * DM, 2 * DM + c.h * DH, L_V);
    load_img(c, p.wa, 3 * DM, c.h * DH, L_Q);
    load_img(c, p.woa, DM, c.h * DH, L_W);
    const bf16x2 one = __builtin_bit_cast(bf16x2, opaque(0x3f803f80));
#pragma unroll
    for (int s = 0; s < NKS; ++s)
#pragma unroll
      for (int j = 0; j < 4; ++j) { const unsigned e = xf[s][j]; const bf16x2 v = __builtin_bit_cast(bf16x2, e); s1 = __builtin_amdgcn_fdot2_f32_bf16(v, one, s1, false); s2 = __builtin_amdgcn_fdot2_f32_bf16(v, v, s2, false); }
    bar();
  } else {
    await(c, 0, 3 * L);
    STAMP(L * 12 + 0);
    const int xb = WS_X + (L - 1) * ACT + (c.rb * 4 + w) * TILE32;
    const int sb = WS_ST + (L - 1) * STSZ + (c.rb * 4 + w) * 4096;
    u32x2 st[8];
#pragma unroll
    for (int j = 0; j < 8; ++j) st[j] = ld8<AUX_SC1>(c.rs, hh * 2048 + r * 8, sb, j * 256);
#pragma unroll
    for (int s = 0; s < NKS; ++s) xf[s] = ld16<AUX_SC1>(c.rs, lane * 16, xb, s * 1024);
#pragma unroll
    for (int j = 0; j < 8; ++j) { s1 += __uint_as_float(st[j][0]); s2 += __uint_as_float(st[j][1]); }
  }
  s1 = xsum(s1); s2 = xsum(s2);
  const float mu = s1 * (1.0f / DM);
  const float rstd = __builtin_amdgcn_rsqf(fmaxf(s2 * (1.0f / DM) - mu * mu, 0.f) + 1e-5f);
  const float mr = mu * rstd;
  LAS float* rsl = (LAS float*)(c.L + L_RS) + w * 64;
  if (hh == 0) rsl[r] = rstd; else rsl[32 + r] = mr;

  LAS float* csl = (LAS float*)(c.L + L_CS) + (SOFTMAX ? 384 : 0);
  if (it == 0) {
    const u32x4 ones = {0x3f803f80u, 0x3f803f80u, 0x3f803f80u, 0x3f803f80u};
    f32x16 a0 = {}, a1 = {}, a2 = {};
#pragma unroll
    for (int s = 0; s < 7; ++s) {
      a0 = mfma(ones, wfrag(pid + L_Q + w * 7 * 896, s), a0);
      a1 = mfma(ones, wfrag(pid + L_K + w * 7 * 896, s), a1);
      a2 = mfma(ones, wfrag(pnat + L_V + w * 7 * 896, s), a2);
    }
    if (hh == 0) { csl[w * 96 + r] = a0[0]; csl[w * 96 + 32 + r] = a1[0]; csl[w * 96 + 64 + r] = a2[0]; }
    bar();
  }

  f32x16 ak = {}, av = {};
#pragma unroll
  for (int s = 0; s < NKS; ++s) {
    const u32x4 kf = wfrag(pid + L_K, s), vf = wfrag(pnat + L_V, s);
    if (SOFTMAX) ak = mfma(kf, xf[s], ak); else ak = mfma(xf[s], kf, ak);
    av = mfma(xf[s], vf, av);
  }
  STAMP(L * 12 + 1);
  f32x16 rr, rm;
#pragma unroll
  for (int g = 0; g < 4; ++g) {
    const f32x4 a = *(LAS f32x4*)(rsl + 8 * g + 4 * hh), b = *(LAS f32x4*)(rsl + 32 + 8 * g + 4 * hh);
#pragma unroll
    for (int j = 0; j < 4; ++j) { rr[4 * g + j] = a[j]; rm[4 * g + j] = b[j]; }
  }
  const float csv = (csl[64 + r] + csl[96 + 64 + r]) + (csl[192 + 64 + r] + csl[288 + 64 + r]);
#pragma unroll
  for (int i = 0; i < 16; ++i) { const float v = av[i] * rr[i] - rm[i] * csv; av[i] = r < 28 ? v : (r == 28 ? 1.0f : 0.f); }
  const u32x4 vf0 = pack8(av, 0), vf1 = pack8(av, 8);
  u32x4 kf0, kf1;
  if (SOFTMAX) {
#pragma unroll
    for (int g = 0; g < 4; ++g) {
      const LAS float* cp = csl + 32 + 8 * g + 4 * hh;
      const f32x4 cs = (*(LAS f32x4*)cp + *(LAS f32x4*)(cp + 96)) + (*(LAS f32x4*)(cp + 192) + *(LAS f32x4*)(cp + 288));
#pragma unroll
      for (int j = 0; j < 4; ++j) ak[4 * g + j] = ak[4 * g + j] * rstd - mr * cs[j];
    }
    if (hh) { ak[12] = 1.0f; ak[13] = 1.0f; ak[14] = 1.0f; ak[15] = 0.f; }
    kf0 = pack8(ak, 0); kf1 = pack8(ak, 8);
    const int tb = WS_KV + it * KVSZ + (c.h * 64 + c.rb * 4 + w) * 4096;
    const int rs23 = (r & 19) | ((r & 4) << 1) | ((r & 8) >> 1);
    __builtin_amdgcn_raw_buffer_store_b128(kf0, c.rs, tb + (hh * 32 + rs23) * 16, 0, AUX_SC1);
    __builtin_amdgcn_raw_buffer_store_b128(kf1, c.rs, tb + 1024 + (hh * 32 + rs23) * 16, 0, AUX_SC1);
    __builtin_amdgcn_raw_buffer_store_b128(vf0, c.rs, tb + 2048 + lane * 16, 0, AUX_SC1);
    __builtin_amdgcn_raw_buffer_store_b128(vf1, c.rs, tb + 3072 + lane * 16, 0, AUX_SC1);
  } else {
    const float csk = (csl[32 + r] + csl[96 + 32 + r]) + (csl[192 + 32 + r] + csl[288 + 32 + r]);
#pragma unroll
    for (int i = 0; i < 16; ++i) { const float v = phi(rbf(ak[i] * rr[i] - rm[i] * csk)); ak[i] = r < 28 ? v : 0.f; }
    f32x16 kv = mfma(pack8(ak, 0), vf0, f32x16{});
    kv = mfma(pack8(ak, 8), vf1, kv);
    LAS f32x4* ex = (LAS f32x4*)(c.L + L_S);
#pragma unroll
    for (int g = 0; g < 4; ++g) { const f32x4 t = {kv[4 * g], kv[4 * g + 1], kv[4 * g + 2], kv[4 * g + 3]}; ex[(g * 4 + w) * 64 + lane] = t; }
    bar();
    const f32x4 t = (ex[(w * 4 + 0) * 64 + lane] + ex[(w * 4 + 1) * 64 + lane]) + (ex[(w * 4 + 2) * 64 + lane] + ex[(w * 4 + 3) * 64 + lane]);
    __builtin_amdgcn_raw_buffer_store_b128(__builtin_bit_cast(u32x4, t), c.rs, WS_KP + it * KPSZ + (c.h * 16 + c.rb) * 4096 + w * 1024 + lane * 16, 0, AUX_SC1);
  }
  STAMP(L * 12 + 2);
  signal(c, 1, 3 * L + 1);
  STAMP(L * 12 + 3);

  f32x16 aq = {};
#pragma unroll
  for (int s = 0; s < NKS; ++s) aq = mfma(wfrag(pid + L_Q, s), xf[s], aq);
#pragma unroll
  for (int g = 0; g < 4; ++g) {
    const LAS float* cp = csl + 8 * g + 4 * hh;
    const f32x4 cs = (*(LAS f32x4*)cp + *(LAS f32x4*)(cp + 96)) + (*(LAS f32x4*)(cp + 192) + *(LAS f32x4*)(cp + 288));
#pragma unroll
    for (int j = 0; j < 4; ++j) aq[4 * g + j] = aq[4 * g + j] * rstd - mr * cs[j];
  }
  f32x16 o;
  if (!SOFTMAX) {
#pragma unroll
    for (int i = 0; i < 16; ++i) aq[i] = phi(rbf(aq[i]));
    if (hh) { aq[12] = 0.f; aq[13] = 0.f; aq[14] = 0.f; aq[15] = 0.f; }
    const u32x4 q0 = pack8(aq, 0), q1 = pack8(aq, 8);
    STAMP(L * 12 + 4);
    await(c, 1, 3 * L + 1);
    STAMP(L * 12 + 5);
    u32x4 pt[16];
    const int pb = WS_KP + it * KPSZ + c.h * 16 * 4096 + w * 1024;
#pragma unroll
    for (int j = 0; j < 16; ++j) pt[j] = ld16<AUX_SC1>(c.rs, lane * 16, pb, j * 4096);
    f32x4 t = {};
#pragma unroll
    for (int j = 0; j < 16; ++j) t += __builtin_bit_cast(f32x4, pt[j]);
    LAS u32x2* ex = (LAS u32x2*)(c.L + L_QX);
    { const u32x2 v = {pk(t[0], t[1]), pk(t[2], t[3])}; ex[((w >> 1) * 64 + lane) * 2 + (w & 1)] = v; }
    bar();
    const u32x4 a0 = *(LAS u32x4*)(c.L + L_QX + lane * 16), a1 = *(LAS u32x4*)(c.L + L_QX + 1024 + lane * 16);
    o = mfma(a0, q0, f32x16{});
    o = mfma(a1, q1, o);
    const float rd = 1.0f / (xhi(o[12]) + 1e-6f);
#pragma unroll
    for (int i = 0; i < 16; ++i) o[i] *= rd;
    STAMP(L * 12 + 6);
    store_slice(c, WS_O + L * ACT, o);
  } else {
    const float qs = 0.18898223650461362f * 1.4426950408889634f;
#pragma unroll
    for (int i = 0; i < 16; ++i) aq[i] *= qs;
    if (hh) { aq[12] = 0.f; aq[13] = 0.f; aq[14] = 0.f; aq[15] = 0.f; }
    const u32x4 q0 = pack8(aq, 0); u32x4 q1 = pack8(aq, 8);
    f32x16 s0 = mfma(kf0, q0, f32x16{});
    s0 = mfma(kf1, q1, s0);
    float m = s0[0];
#pragma unroll
    for (int i = 1; i < 16; ++i) m = fmaxf(m, s0[i]);
    m = xmax(m);
    LAS u32x4* qx = (LAS u32x4*)(c.L + L_QX);
    LAS unsigned* badw = (LAS unsigned*)(c.L + L_MISC);
    STAMP(L * 12 + 4);
    const int kb = WS_KV + it * KVSZ + (c.h * 64 + w * 16) * 4096;
    for (int pass = 0;; ++pass) {
      { const float m0 = rbf(m), m1 = rbf(m - m0), m2 = rbf((m - m0) - m1);
        if (hh) { q1[2] = pk(-m0, -m1); q1[3] = pk(-m2, 0.f); } }
      qx[(w * 2) * 64 + lane] = q0; qx[(w * 2 + 1) * 64 + lane] = q1;
      if (threadIdx.x == 0) *badw = 0u;
      if (pass == 0) await(c, 1, 3 * L + 1); else bar();
      STAMP(L * 12 + 5);
      u32x4 qf[8];
#pragma unroll
      for (int j = 0; j < 8; ++j) qf[j] = qx[j * 64 + lane];
      f32x16 oa[4] = {};
      u32x4 kv[4], kn[4];
#pragma unroll
      for (int j = 0; j < 4; ++j) kv[j] = ld16<AUX_SC1>(c.rs, lane * 16, kb, j * 1024);
      for (int t = 0; t < 16; ++t) {
        const int tn = t < 15 ? t + 1 : t;
#pragma unroll
        for (int j = 0; j < 4; ++j) kn[j] = ld16<AUX_SC1>(c.rs, lane * 16, kb + tn * 4096, j * 1024);
#pragma unroll
        for (int b = 0; b < 4; ++b) {
          f32x16 s = mfma(kv[0], qf[2 * b], f32x16{});
          s = mfma(kv[1], qf[2 * b + 1], s);
          f32x8 p0, p1;
#pragma unroll
          for (int i = 0; i < 8; ++i) { p0[i] = __builtin_amdgcn_exp2f(s[i]); p1[i] = __builtin_amdgcn_exp2f(s[8 + i]); }
          oa[b] = __builtin_amdgcn_smfmac_f32_32x32x32_bf16(__builtin_bit_cast(bf16x8, kv[2]), __builtin_bit_cast(bf16x16, p0), oa[b], 0xdddd, 0, 0);
          oa[b] = __builtin_amdgcn_smfmac_f32_32x32x32_bf16(__builtin_bit_cast(bf16x8, kv[3]), __builtin_bit_cast(bf16x16, p1), oa[b], 0xdddd, 0, 0);
        }
#pragma unroll
        for (int j = 0; j < 4; ++j) kv[j] = kn[j];
      }
      STAMP(L * 12 + 6);
      LAS f32x4* ex = (LAS f32x4*)(c.L + L_S);
#pragma unroll
      for (int b = 0; b < 4; ++b) {
        if (b != w) {
          const int slot = b * 3 + (w > b ? w - 1 : w);
#pragma unroll
          for (int g = 0; g < 4; ++g) { const f32x4 t = {oa[b][4 * g], oa[b][4 * g + 1], oa[b][4 * g + 2], oa[b][4 * g + 3]}; ex[(slot * 4 + g) * 64 + lane] = t; }
        } else o = oa[b];
      }
      bar();
#pragma unroll
      for (int k = 0; k < 3; ++k)
#pragma unroll
        for (int g = 0; g < 4; ++g) { const f32x4 t = ex[((w * 3 + k) * 4 + g) * 64 + lane];
#pragma unroll
          for (int j = 0; j < 4; ++j) o[4 * g + j] += t[j]; }
      const float l = xhi(o[12]);
      const float rl = 1.0f / l;
      float chk = 0.f;
#pragma unroll
      for (int i = 0; i < 16; ++i) { chk = __builtin_fmaf(o[i], 0.f, chk); o[i] *= rl; }
      const bool bad = !(l < 1.0e30f) || !(chk == 0.f);
      store_slice(c, WS_O + L * ACT, o);
      if (pass) break;
      if (__any(bad) && lane == 0) *badw = 1u;
      bar();
      if (*badw == 0u) break;
      float mm[4] = {-3.0e38f, -3.0e38f, -3.0e38f, -3.0e38f};
#pragma unroll
      for (int b = 0; b < 4; ++b) if (hh) { qf[2 * b + 1][2] = 0u; qf[2 * b + 1][3] = 0u; }
      for (int t = 0; t < 16; ++t) {
        const u32x4 k0 = ld16<AUX_SC1>(c.rs, lane * 16, kb + t * 4096, 0), k1 = ld16<AUX_SC1>(c.rs, lane * 16, kb + t * 4096, 1024);
#pragma unroll
        for (int b = 0; b < 4; ++b) {
          f32x16 s = mfma(k0, qf[2 * b], f32x16{});
          s = mfma(k1, qf[2 * b + 1], s);
#pragma unroll
          for (int i = 0; i < 16; ++i) mm[b] = fmaxf(mm[b], s[i]);
        }
      }
      LAS float* mx = (LAS float*)(c.L + L_S);
#pragma unroll
      for (int b = 0; b < 4; ++b) mx[(b * 4 + w) * 64 + lane] = mm[b];
      bar();
      m = fmaxf(fmaxf(mx[(w * 4) * 64 + lane], mx[(w * 4 + 1) * 64 + lane]), fmaxf(mx[(w * 4 + 2) * 64 + lane], mx[(w * 4 + 3) * 64 + lane]));
      m = xmax(m);
      bar();
    }
  }
  STAMP(L * 12 + 7);
  signal(c, 0, 3 * L + 2);
  if (L < 3) {
    const u16* wn = SOFTMAX ? p.wa : p.wb;
    load_img(c, wn, 3 * DM, DM + c.h * DH, L_K);
    load_img(c, wn, 3 * DM, 2 * DM + c.h * DH, L_V);
    load_img(c, wn, 3 * DM, c.h * DH, L_Q);
  }
  const int p0 = (7 * c.h + (c.h & 1)) >> 1;
  if (L == 0) {
    const GAS char* src = (const GAS char*)p.x0 + (c.rb * 128 + w * 32 + r) * 896;
    xa = *(const GAS u32x4*)(src + (p0 + hh) * 16);
    if (hh == 0) xb2 = *(const GAS u32x4*)(src + (p0 + 2) * 16);
    else { const u32x2 t = *(const GAS u32x2*)(src + ((c.h & 1) ? (p0 - 1) * 16 + 8 : (p0 + 3) * 16)); xb2[0] = t[0]; xb2[1] = t[1]; xb2[2] = 0u; xb2[3] = 0u; }
  }
  STAMP(L * 12 + 8);
  await(c, 0, 3 * L + 2);
  STAMP(L * 12 + 9);

  {
    const int ob = WS_O + L * ACT + (c.rb * 4 + w) * TILE32;
    u32x4 of[NKS];
#pragma unroll
    for (int s = 0; s < NKS; ++s) of[s] = ld16<AUX_SC1>(c.rs, lane * 16, ob, s * 1024);
    f32x16 y = {};
#pragma unroll
    for (int s = 0; s < NKS; ++s) y = mfma(wfrag(pnat + L_W, s), of[s], y);
    float t1 = 0.f, t2 = 0.f;
    f32x16 xs;
#pragma unroll
    for (int j = 0; j < 4; ++j) { xs[2 * j] = bflo(xa[j]); xs[2 * j + 1] = bfhi(xa[j]); xs[8 + 2 * j] = bflo(xb2[j]); xs[8 + 2 * j + 1] = bfhi(xb2[j]); }
#pragma unroll
    for (int i = 0; i < 16; ++i) { float v = rbf(xs[i] + rbf(y[i])); if (i >= 12 && hh) v = 0.f; xs[i] = v; t1 += v; t2 = __builtin_fmaf(v, v, t2); }
    xa = pack8(xs, 0); xb2 = pack8(xs, 8);
    STAMP(L * 12 + 10);
    if (L < 3) {
      store_slice(c, WS_X + L * ACT, xs);
      t1 = xsum(t1); t2 = xsum(t2);
      if (hh == 0) { const u32x2 t = {__float_as_uint(t1), __float_as_uint(t2)}; __builtin_amdgcn_raw_buffer_store_b64(t, c.rs, WS_ST + L * STSZ + (c.rb * 4 + w) * 4096 + c.h * 256 + r * 8, 0, AUX_SC1); }
      signal(c, 0, 3 * L + 3);
      load_img(c, SOFTMAX ? p.woa : p.wob, DM, c.h * DH, L_W);
    } else {
      GAS char* dst = (GAS char*)p.out + (c.rb * 128 + w * 32 + r) * 896;
      *(GAS u32x4*)(dst + (p0 + hh) * 16) = pack8(xs, 0);
      const u32x4 hi = pack8(xs, 8);
      if (hh == 0) *(GAS u32x4*)(dst + (p0 + 2) * 16) = hi;
      else { const u32x2 t = {hi[0], hi[1]}; *(GAS u32x2*)(dst + ((c.h & 1) ? (p0 - 1) * 16 + 8 : (p0 + 3) * 16)) = t; }
    }
    STAMP(L * 12 + 11);
  }
}

__global__ void __launch_bounds__(256, 1) fused_kernel(Params p) {
  extern __shared__ __attribute__((aligned(16))) char smem[];
  Ctx c;
  c.L = (LAS char*)smem; c.ws = p.ws;
  c.lane = threadIdx.x & 63; c.r = c.lane & 31; c.hh = c.lane >> 5; c.w = __builtin_amdgcn_readfirstlane(threadIdx.x >> 6);
  { const int x = blockIdx.x & 7, j = blockIdx.x >> 3; c.rb = (x & 3) * 4 + (j & 3); c.h = (x >> 2) * 8 + (j >> 2); }
  c.rs = __builtin_amdgcn_make_buffer_rsrc(p.ws, 0, WS_END, 0x00020000);
  STAMP(0);
  if (c.w == 1 && blockIdx.x < 8) asm volatile("buffer_wbl2 sc0 sc1" ::: "memory");
  const u64 ev = __hip_atomic_load((GAS u64*)(p.ws + WS_EPOCH), RLX_AGENT);
  const u64 epoch = ((u64)(unsigned)__builtin_amdgcn_readfirstlane((int)(ev >> 32)) << 32) | (unsigned)__builtin_amdgcn_readfirstlane((int)ev);
  c.tag = epoch * 0x9E3779B97F4A7C15ull + 0x632BE59BD9B4E019ull;
  u32x4 xa = {}, xb = {};
  for (int it = 0; it < 2; ++it) {
    layer<false>(c, p, it, xa, xb);
    layer<true>(c, p, it, xa, xb);
  }
  if (blockIdx.x == 0 && threadIdx.x == 0) __hip_atomic_store((GAS u64*)(p.ws + WS_EPOCH), epoch + 1, RLX_AGENT);
}

extern "C" void kernel_launch(void* const* d_in, const int* in_sizes, int n_in, void* d_out, int out_size, void* d_ws, size_t ws_size, hipStream_t stream) {
  Params p;
  p.x0 = (const u16*)d_in[0]; p.wa = (const u16*)d_in[1]; p.woa = (const u16*)d_in[2];
  p.wb = (const u16*)d_in[4]; p.wob = (const u16*)d_in[5]; p.out = (u16*)d_out; p.ws = (char*)d_ws;
  (void)hipFuncSetAttribute((const void*)fused_kernel, hipFuncAttributeMaxDynamicSharedMemorySize, LDS_BYTES);
  hipLaunchKernelGGL(fused_kernel, dim3(256), dim3(256), LDS_BYTES, stream, p);
}
```

```cpp
#include <hip/hip_runtime.h>

struct Params { const void* x0; const void* wa; const void* woa; const void* wb; const void* wob; void* out; void* ws; };

__global__ void __launch_bounds__(256, 1) fused_kernel(Params p) {
  asm volatile(
      "; ---- prologue ----\n"
      "s_load_dwordx8 s[20:27], s[0:1], 0x0\n"
      "s_load_dwordx4 s[28:31], s[0:1], 0x20\n"
      "s_load_dwordx2 s[32:33], s[0:1], 0x30\n"
      "s_getpc_b64 s[36:37]\n"
      "v_lshrrev_b32 v225, 6, v0\n"
      "v_and_b32 v224, 63, v0\n"
      "v_readfirstlane_b32 s14, v225\n"
      "s_mov_b32 s15, 0\n"
      "s_and_b32 s34, s2, 3\n"
      "s_lshl_b32 s34, s34, 2\n"
      "s_bfe_u32 s35, s2, 0x20003\n"
      "s_or_b32 s12, s34, s35\n"
      "s_bfe_u32 s34, s2, 0x10002\n"
      "s_lshl_b32 s34, s34, 3\n"
      "s_lshr_b32 s35, s2, 5\n"
      "s_or_b32 s13, s34, s35\n"
      "s_mov_b32 s6, 27836416\n"
      "s_mov_b32 s7, 0x20000\n"
      "s_mov_b32 s11, 0x20000\n"
      "v_mov_b32 v189, 131072\n"
      "s_cmp_ge_u32 s14, 2\n"
      "s_cbranch_scc1 L_DMA\n"
      "; waves 0 / 1: gather 32 rows of this head's Wv, Wk, Wq columns (linear / softmax layer); a lane collects two 8-byte pieces\n"
      "s_movk_i32 s34, 2688\n"
      "v_lshlrev_b32 v225, 1, v224\n"
      "v_mul_u32_u24 v226, 9363, v225\n"
      "v_lshrrev_b32 v226, 16, v226\n"
      "v_mul_u32_u24 v228, 7, v226\n"
      "v_sub_u32 v228, v225, v228\n"
      "v_lshlrev_b32 v228, 3, v228\n"
      "v_mad_u32_u24 v100, v226, s34, v228\n"
      "v_add_u32 v229, 1, v225\n"
      "v_mul_u32_u24 v226, 9363, v229\n"
      "v_lshrrev_b32 v226, 16, v226\n"
      "v_mul_u32_u24 v228, 7, v226\n"
      "v_sub_u32 v228, v229, v228\n"
      "v_lshlrev_b32 v228, 3, v228\n"
      "v_mad_u32_u24 v101, v226, s34, v228\n"
      "v_add_u32 v229, 128, v225\n"
      "v_mul_u32_u24 v226, 9363, v229\n"
      "v_lshrrev_b32 v226, 16, v226\n"
      "v_mul_u32_u24 v228, 7, v226\n"
      "v_sub_u32 v228, v229, v228\n"
      "v_lshlrev_b32 v228, 3, v228\n"
      "v_mad_u32_u24 v102, v226, s34, v228\n"
      "v_add_u32 v229, 129, v225\n"
      "v_mul_u32_u24 v226, 9363, v229\n"
      "v_lshrrev_b32 v226, 16, v226\n"
      "v_mul_u32_u24 v228, 7, v226\n"
      "v_sub_u32 v228, v229, v228\n"
      "v_lshlrev_b32 v228, 3, v228\n"
      "v_mad_u32_u24 v103, v226, s34, v228\n"
      "s_mul_i32 s38, s13, 56\n"
      "s_mul_i32 s57, s12, 86016\n"
      "s_add_u32 s57, s57, s38\n"
      "s_mov_b32 s10, 1204224\n"
      "s_cmp_eq_u32 s14, 0\n"
      "s_waitcnt lgkmcnt(0)\n"
      "s_cselect_b64 s[8:9], s[22:23], s[26:27]\n"
      "s_and_b32 s9, s9, 0xffff\n"
      "s_mov_b32 s4, s32\n"
      "s_and_b32 s5, s33, 0xffff\n"
      "s_cmp_ge_u32 s12, 14\n"
      "s_cbranch_scc1 L_NOG\n"
      "buffer_load_dwordx2 v[168:169], v100, s[8:11], s57 offen offset:896\n"
      "buffer_load_dwordx2 v[160:161], v100, s[8:11], s57 offen offset:1792\n"
      "buffer_load_dwordx2 v[176:177], v100, s[8:11], s57 offen offset:0\n"
      "buffer_load_dwordx2 v[170:171], v101, s[8:11], s57 offen offset:896\n"
      "buffer_load_dwordx2 v[162:163], v101, s[8:11], s57 offen offset:1792\n"
      "buffer_load_dwordx2 v[178:179], v101, s[8:11], s57 offen offset:0\n"
      "s_mov_b32 exec_hi, 0xffff\n"
      "buffer_load_dwordx2 v[172:173], v102, s[8:11], s57 offen offset:896\n"
      "buffer_load_dwordx2 v[164:165], v102, s[8:11], s57 offen offset:1792\n"
      "buffer_load_dwordx2 v[180:181], v102, s[8:11], s57 offen offset:0\n"
      "buffer_load_dwordx2 v[174:175], v103, s[8:11], s57 offen offset:896\n"
      "buffer_load_dwordx2 v[166:167], v103, s[8:11], s57 offen offset:1792\n"
      "buffer_load_dwordx2 v[182:183], v103, s[8:11], s57 offen offset:0\n"
      "s_mov_b64 exec, -1\n"
      "L_NOG:\n"
      "buffer_load_dwordx2 v[184:185], v189, s[4:7], 0 offen sc1\n"
      "s_barrier\n"
      "s_branch L_CONST\n"
      "L_DMA:\n"
      "; waves 2 / 3: pull the code into L2 (an eighth per wave); x0 rows -> LDS in whole 128-byte lines, swizzled on the source side\n"
      "s_bfe_u32 s34, s2, 0x20003\n"
      "s_lshl_b32 s34, s34, 13\n"
      "v_and_b32 v187, 0x5f, v0\n"
      "v_lshlrev_b32 v187, 7, v187\n"
      "v_bfe_u32 v206, v187, 13, 1\n"
      "v_and_b32 v187, 0xfff, v187\n"
      "v_lshl_add_u32 v187, v206, 12, v187\n"
      "v_add_u32 v187, s34, v187\n"
      "v_min_u32 v187, 0x7bd0, v187\n"
      "; waves 2 / 3: 64 x0 rows each -> LDS, fetched in whole 128-byte lines, stored swizzled\n"
      "v_lshrrev_b32 v205, 3, v224\n"
      "v_mul_u32_u24 v205, 896, v205\n"
      "v_and_b32 v207, 7, v224\n"
      "v_lshl_add_u32 v200, v207, 4, v205\n"
      "v_and_b32 v206, 0x30, v224\n"
      "v_lshlrev_b32 v205, 4, v224\n"
      "v_xor_b32 v206, v206, v205\n"
      "s_and_b32 s34, s14, 1\n"
      "s_mul_i32 s34, s34, 57344\n"
      "v_add_u32 v201, s34, v206\n"
      "v_xor_b32 v202, 64, v201\n"
      "s_and_b32 s34, s14, 1\n"
      "s_lshl_b32 s35, s12, 1\n"
      "s_add_u32 s34, s34, s35\n"
      "s_mul_i32 s57, s34, 57344\n"
      "s_mov_b32 s10, 1835008\n"
      "s_and_b32 s34, s13, 7\n"
      "s_mul_i32 s34, s34, 7168\n"
      "s_add_u32 s34, s34, s57\n"
      "v_lshlrev_b32 v188, 7, v224\n"
      "v_mul_u32_u24 v186, 896, v224\n"
      "v_mov_b32 v227, 159744\n"
      "v_mov_b64 v[228:229], 0\n"
      "ds_write_b64 v227, v[228:229]\n"
      "s_bfe_u32 s35, s2, 0x30003\n"
      "s_mul_i32 s35, s35, 50176\n"
      "s_lshr_b32 s38, s13, 1\n"
      "s_lshl_b32 s38, s38, 7\n"
      "s_add_u32 s58, s35, s38\n"
      "s_mov_b32 s54, 401408\n"
      "s_mov_b32 s55, 0x20000\n"
      "s_cmp_eq_u32 s14, 2\n"
      "s_waitcnt lgkmcnt(0)\n"
      "s_cselect_b64 s[52:53], s[24:25], s[28:29]\n"
      "s_and_b32 s53, s53, 0xffff\n"
      "s_mov_b32 s8, s20\n"
      "s_and_b32 s9, s21, 0xffff\n"
      "s_mov_b32 s4, s32\n"
      "s_and_b32 s5, s33, 0xffff\n"
      "; touch an eighth of the x rows: the eight workgroups that share them warm L2 together\n"
      "s_mov_b32 exec_hi, 0x00ffffff\n"
      "buffer_load_dword v188, v188, s[8:11], s34 offen\n"
      "s_mov_b32 exec_hi, 0\n"
      "global_load_dword v187, v187, s[36:37]\n"
      "s_barrier\n"
      "s_mov_b32 exec_hi, 0x00ffffff\n"
      "s_cmp_ge_u32 s13, 14\n"
      "s_cbranch_scc1 L_NOWT\n"
      "buffer_load_dword v186, v186, s[52:55], s58 offen\n"
      "L_NOWT:\n"
      "s_mov_b64 exec, -1\n"
      "buffer_load_dwordx2 v[184:185], v189, s[4:7], 0 offen sc1\n"
      "L_CONST:\n"
      "v_lshlrev_b32 v0, 4, v224\n"
      "v_bfrev_b32 v230, 1\n"
      "v_cmp_gt_u32 vcc, 16, v224\n"
      "v_lshlrev_b32 v7, 3, v224\n"
      "v_lshlrev_b32 v231, 8, v224\n"
      "s_lshl_b32 s34, s12, 12\n"
      "s_lshl_b32 s36, s13, 3\n"
      "s_add_u32 s80, s36, s34\n"
      "v_cndmask_b32 v6, v230, v231, vcc\n"
      "s_lshl_b32 s36, s13, 8\n"
      "s_add_u32 s82, s36, s34\n"
      "s_lshl_b32 s34, s13, 12\n"
      "s_add_u32 s34, s34, 0x10000\n"
      "s_lshl_b32 s36, s12, 3\n"
      "s_add_u32 s81, s36, s34\n"
      "s_lshl_b32 s36, s12, 8\n"
      "s_add_u32 s83, s36, s34\n"
      "s_mul_i32 s50, s13, 150528\n"
      "s_add_u32 s50, s50, 524288\n"
      "s_cmp_lt_u32 s14, 2\n"
      "s_cbranch_scc1 L_CONST2\n"
      "; first touch of the own flag lines\n"
      "s_cmp_eq_u32 s14, 2\n"
      "s_cselect_b32 s36, s83, s82\n"
      "s_mov_b32 exec_hi, 0\n"
      "buffer_load_dwordx2 v[190:191], v7, s[4:7], s36 offen sc1\n"
      "s_mov_b64 exec, -1\n"
      "L_CONST2:\n"
      "; lane constants\n"
      "v_and_b32 v225, 31, v224\n"
      "v_lshrrev_b32 v226, 5, v224\n"
      "s_mov_b32 s60, 0\n"
      "s_mov_b32 s61, -1\n"
      "s_lshl_b32 s34, s12, 2\n"
      "s_add_u32 s35, s34, s14\n"
      "s_mul_i32 s42, s35, 28672\n"
      "s_add_u32 s42, s42, 2932736\n"
      "s_lshl_b32 s46, s35, 12\n"
      "s_add_u32 s46, s46, 26787840\n"
      "s_lshl_b32 s45, s35, 15\n"
      "s_lshl_b32 s36, s13, 11\n"
      "s_add_u32 s45, s45, s36\n"
      "s_add_u32 s45, s45, 8437760\n"
      "s_and_b32 s37, s13, 1\n"
      "s_lshl_b32 s37, s37, 1\n"
      "s_and_b32 s36, s14, 1\n"
      "s_add_u32 s37, s37, s36\n"
      "s_lshr_b32 s38, s14, 1\n"
      "s_and_b32 s39, s13, 14\n"
      "s_add_u32 s39, s39, s38\n"
      "s_add_u32 s35, s34, s37\n"
      "s_mul_i32 s43, s35, 28672\n"
      "s_lshl_b32 s36, s39, 11\n"
      "s_add_u32 s43, s43, s36\n"
      "s_add_u32 s43, s43, 2932736\n"
      "s_lshl_b32 s47, s35, 12\n"
      "s_lshl_b32 s36, s39, 8\n"
      "s_add_u32 s47, s47, s36\n"
      "s_add_u32 s47, s47, 26787840\n"
      "s_lshl_b32 s44, s35, 15\n"
      "s_lshl_b32 s36, s38, 14\n"
      "s_add_u32 s44, s44, s36\n"
      "s_add_u32 s44, s44, 8437760\n"
      "s_mul_i32 s3, s35, 28672\n"
      "s_lshl_b32 s36, s39, 6\n"
      "s_add_u32 s3, s3, s36\n"
      "s_cmp_lt_u32 s13, 14\n"
      "s_cselect_b64 s[40:41], -1, 0\n"
      "v_lshlrev_b32 v231, 3, v225\n"
      "v_lshl_add_u32 v231, v226, 2, v231\n"
      "v_cndmask_b32 v3, v230, v0, s[40:41]\n"
      "v_cndmask_b32 v9, v230, v231, s[40:41]\n"
      "v_cndmask_b32 v216, v0, v230, s[60:61]\n"
      "s_lshr_b32 s36, s13, 1\n"
      "s_lshl_b32 s36, s36, 7\n"
      "s_mul_i32 s84, s14, 100352\n"
      "s_add_u32 s84, s84, s36\n"
      "s_mul_i32 s85, s14, 14336\n"
      "s_add_u32 s85, s85, 100352\n"
      "v_lshrrev_b32 v228, 2, v224\n"
      "v_and_b32 v229, 3, v228\n"
      "v_and_b32 v228, 4, v228\n"
      "v_and_or_b32 v228, v224, 3, v228\n"
      "v_lshl_add_u32 v234, v226, 3, v229\n"
      "v_mul_u32_u24 v234, 56, v234\n"
      "v_mul_u32_u24 v235, 448, v226\n"
      "v_lshl_add_u32 v235, v229, 3, v235\n"
      "v_add_u32 v235, 224, v235\n"
      "v_cmp_eq_u32 vcc, 7, v228\n"
      "v_lshl_add_u32 v236, v228, 3, v234\n"
      "s_mov_b32 s36, 0x75643120\n"
      "v_lshlrev_b32 v233, 2, v228\n"
      "v_lshrrev_b32 v233, v233, s36\n"
      "v_and_b32 v233, 7, v233\n"
      "v_cndmask_b32 v2, v236, v235, vcc\n"
      "v_add_u32 v1, 50176, v2\n"
      "v_add_u32 v218, 100352, v2\n"
      "v_lshl_add_u32 v234, v226, 2, v229\n"
      "v_lshlrev_b32 v234, 7, v234\n"
      "v_lshl_add_u32 v234, v233, 3, v234\n"
      "v_lshrrev_b32 v235, 1, v229\n"
      "v_xor_b32 v235, s38, v235\n"
      "v_lshl_add_u32 v234, v235, 6, v234\n"
      "s_mul_i32 s36, s38, 28672\n"
      "s_add_u32 s36, s36, 100352\n"
      "v_add_u32 v4, s36, v234\n"
      "v_xor_b32 v5, 64, v4\n"
      "s_mov_b32 s62, 0x3e8b97f6\n"
      "s_mov_b32 s63, 0x3fb8aa3b\n"
      "v_and_b32 v231, 51, v224\n"
      "v_and_b32 v232, 4, v224\n"
      "v_lshl_or_b32 v231, v232, 1, v231\n"
      "v_and_b32 v232, 8, v224\n"
      "v_lshrrev_b32 v232, 1, v232\n"
      "v_or_b32 v231, v231, v232\n"
      "v_lshlrev_b32 v10, 4, v231\n"
      "s_mul_i32 s34, s14, 384\n"
      "s_add_u32 s34, s34, 158208\n"
      "v_lshl_add_u32 v11, v225, 2, s34\n"
      "v_and_b32 v231, 31, v224\n"
      "v_lshlrev_b32 v231, 3, v231\n"
      "v_lshl_add_u32 v231, v226, 2, v231\n"
      "v_add3_u32 v8, v231, s34, 64\n"
      "v_add_u32 v8, 64, v8\n"
      "v_lshl_add_u32 v231, v226, 4, s34\n"
      "v_mov_b32 v232, 157696\n"
      "v_cmp_lt_u32 vcc, 27, v225\n"
      "v_cmp_eq_u32 s[40:41], 28, v225\n"
      "v_mov_b32 v15, 0xdddd\n"
      "v_mov_b32 v22, 0x358637bd\n"
      "v_cndmask_b32 v12, v231, v232, vcc\n"
      "v_cndmask_b32 v20, 0, 1.0, s[40:41]\n"
      "v_cndmask_b32 v21, 0, 1.0, s[60:61]\n"
      "v_cndmask_b32 v23, 1.0, 0, s[60:61]\n"
      "s_mul_i32 s34, s14, 6272\n"
      "v_add_u32 v13, s34, v0\n"
      "v_lshrrev_b32 v231, 3, v224\n"
      "v_mul_u32_u24 v231, 25088, v231\n"
      "v_and_b32 v232, 7, v224\n"
      "v_lshl_add_u32 v231, v232, 4, v231\n"
      "v_cmp_gt_u32 vcc, 24, v224\n"
      "s_add_u32 s34, s34, 6144\n"
      "v_add_u32 v231, s34, v231\n"
      "v_cndmask_b32 v14, v230, v231, vcc\n"
      "s_add_u32 s34, s14, 1\n"
      "s_and_b32 s34, s34, 3\n"
      "s_mul_i32 s34, s34, 14336\n"
      "s_add_u32 s34, s34, 100352\n"
      "v_add_u32 v16, s34, v0\n"
      "s_add_u32 s34, s14, 2\n"
      "s_and_b32 s34, s34, 3\n"
      "s_mul_i32 s34, s34, 14336\n"
      "s_add_u32 s34, s34, 100352\n"
      "v_add_u32 v17, s34, v0\n"
      "s_add_u32 s34, s14, 3\n"
      "s_and_b32 s34, s34, 3\n"
      "s_mul_i32 s34, s34, 14336\n"
      "s_add_u32 s34, s34, 100352\n"
      "v_add_u32 v18, s34, v0\n"
      "s_add_u32 s34, s14, 4\n"
      "s_and_b32 s34, s34, 3\n"
      "s_mul_i32 s34, s34, 14336\n"
      "s_add_u32 s34, s34, 100352\n"
      "v_add_u32 v19, s34, v0\n"
      "v_add_u32 v231, 125440, v0\n"
      "v_lshl_add_u32 v26, s14, 10, v231\n"
      "v_lshl_add_u32 v27, s14, 12, v231\n"
      "s_xor_b32 s34, s14, 2\n"
      "v_lshl_add_u32 v217, s34, 12, v231\n"
      "v_lshlrev_b32 v231, 4, v225\n"
      "v_lshl_add_u32 v219, v226, 11, v231\n"
      "v_add_u32 v221, 159872, v0\n"
      "s_lshl_b32 s34, s38, 10\n"
      "s_and_b32 s36, s14, 1\n"
      "s_lshl_b32 s36, s36, 3\n"
      "s_add_u32 s34, s34, s36\n"
      "s_add_u32 s34, s34, 159872\n"
      "v_lshl_add_u32 v231, v225, 1, v226\n"
      "v_lshl_add_u32 v220, v231, 4, s34\n"
      "s_lshl_b32 s49, s13, 15\n"
      "s_lshl_b32 s36, s14, 9\n"
      "s_add_u32 s49, s49, s36\n"
      "s_add_u32 s49, s49, 25214976\n"
      "s_lshl_b32 s48, s13, 18\n"
      "s_lshl_b32 s36, s14, 12\n"
      "s_add_u32 s48, s48, s36\n"
      "s_add_u32 s48, s48, 16826368\n"
      "v_lshrrev_b32 v231, 3, v225\n"
      "v_mul_u32_u24 v231, 7168, v231\n"
      "v_and_b32 v232, 7, v225\n"
      "v_lshl_add_u32 v237, v232, 7, v231\n"
      "v_bfe_u32 v238, v225, 1, 3\n"
      "v_xor_b32 v232, v238, v226\n"
      "s_mul_i32 s34, s14, 28672\n"
      "v_add_u32 v239, s34, v237\n"
      "v_xor_b32 v231, 0, v232\n"
      "v_lshl_add_u32 v240, v231, 4, v239\n"
      "v_xor_b32 v231, 2, v232\n"
      "v_lshl_add_u32 v241, v231, 4, v239\n"
      "v_xor_b32 v231, 4, v232\n"
      "v_lshl_add_u32 v242, v231, 4, v239\n"
      "v_xor_b32 v231, 6, v232\n"
      "v_lshl_add_u32 v243, v231, 4, v239\n"
      "s_mul_i32 s34, s37, 28672\n"
      "s_lshr_b32 s36, s13, 1\n"
      "s_min_u32 s36, s36, 6\n"
      "s_lshl_b32 s36, s36, 10\n"
      "s_add_u32 s34, s34, s36\n"
      "s_lshl_b32 s36, s38, 2\n"
      "v_add_u32 v239, s34, v237\n"
      "v_xor_b32 v231, s36, v232\n"
      "v_lshl_add_u32 v244, v231, 4, v239\n"
      "v_xor_b32 v245, 32, v244\n"
      "s_add_u32 s34, s12, 1\n"
      "s_and_b32 s34, s34, 15\n"
      "s_lshl_b32 s64, s34, 14\n"
      "s_add_u32 s34, s12, 2\n"
      "s_and_b32 s34, s34, 15\n"
      "s_lshl_b32 s65, s34, 14\n"
      "s_add_u32 s34, s12, 3\n"
      "s_and_b32 s34, s34, 15\n"
      "s_lshl_b32 s66, s34, 14\n"
      "s_add_u32 s34, s12, 4\n"
      "s_and_b32 s34, s34, 15\n"
      "s_lshl_b32 s67, s34, 14\n"
      "s_add_u32 s34, s12, 5\n"
      "s_and_b32 s34, s34, 15\n"
      "s_lshl_b32 s68, s34, 14\n"
      "s_add_u32 s34, s12, 6\n"
      "s_and_b32 s34, s34, 15\n"
      "s_lshl_b32 s69, s34, 14\n"
      "s_add_u32 s34, s12, 7\n"
      "s_and_b32 s34, s34, 15\n"
      "s_lshl_b32 s70, s34, 14\n"
      "s_add_u32 s34, s12, 8\n"
      "s_and_b32 s34, s34, 15\n"
      "s_lshl_b32 s71, s34, 14\n"
      "s_add_u32 s34, s12, 9\n"
      "s_and_b32 s34, s34, 15\n"
      "s_lshl_b32 s72, s34, 14\n"
      "s_add_u32 s34, s12, 10\n"
      "s_and_b32 s34, s34, 15\n"
      "s_lshl_b32 s73, s34, 14\n"
      "s_add_u32 s34, s12, 11\n"
      "s_and_b32 s34, s34, 15\n"
      "s_lshl_b32 s74, s34, 14\n"
      "s_add_u32 s34, s12, 12\n"
      "s_and_b32 s34, s34, 15\n"
      "s_lshl_b32 s75, s34, 14\n"
      "s_add_u32 s34, s12, 13\n"
      "s_and_b32 s34, s34, 15\n"
      "s_lshl_b32 s76, s34, 14\n"
      "s_add_u32 s34, s12, 14\n"
      "s_and_b32 s34, s34, 15\n"
      "s_lshl_b32 s77, s34, 14\n"
      "s_add_u32 s34, s12, 15\n"
      "s_and_b32 s34, s34, 15\n"
      "s_lshl_b32 s78, s34, 14\n"
      "s_lshl_b32 s79, s12, 14\n"
      "v_mov_b64 v[234:235], 0\n"
      "v_lshrrev_b32 v233, 1, v0\n"
      "v_add_u32 v233, 157696, v233\n"
      "ds_write_b64 v233, v[234:235]\n"
      "v_mov_b64 v[192:193], 0\n"
      "v_mov_b64 v[194:195], 0\n"
      "v_mov_b64 v[196:197], 0\n"
      "v_mov_b64 v[198:199], 0\n"
      "v_mov_b32 v252, 0x3f803f80\n"
      "v_mov_b32 v253, 0x3f803f80\n"
      "v_mov_b32 v254, 0x3f803f80\n"
      "v_mov_b32 v255, 0x3f803f80\n"
      "v_accvgpr_write_b32 a169, 0\n"
      "v_accvgpr_write_b32 a173, 0\n"
      "v_mov_b64 v[248:249], -1\n"
      "v_mov_b64 v[250:251], -1\n"
      "s_mov_b32 s1, 1\n"
      "s_cmp_lt_u32 s14, 2\n"
      "s_cbranch_scc1 L_PUB\n"
      "buffer_load_dwordx4 v[48:51], v200, s[8:11], s57 offen offset:0\n"
      "buffer_load_dwordx4 v[52:55], v200, s[8:11], s57 offen offset:128\n"
      "buffer_load_dwordx4 v[56:59], v200, s[8:11], s57 offen offset:256\n"
      "buffer_load_dwordx4 v[60:63], v200, s[8:11], s57 offen offset:384\n"
      "buffer_load_dwordx4 v[64:67], v200, s[8:11], s57 offen offset:512\n"
      "buffer_load_dwordx4 v[68:71], v200, s[8:11], s57 offen offset:640\n"
      "buffer_load_dwordx4 v[72:75], v200, s[8:11], s57 offen offset:768\n"
      "s_add_u32 s57, s57, 7168\n"
      "buffer_load_dwordx4 v[76:79], v200, s[8:11], s57 offen offset:0\n"
      "buffer_load_dwordx4 v[80:83], v200, s[8:11], s57 offen offset:128\n"
      "buffer_load_dwordx4 v[84:87], v200, s[8:11], s57 offen offset:256\n"
      "buffer_load_dwordx4 v[88:91], v200, s[8:11], s57 offen offset:384\n"
      "buffer_load_dwordx4 v[92:95], v200, s[8:11], s57 offen offset:512\n"
      "buffer_load_dwordx4 v[96:99], v200, s[8:11], s57 offen offset:640\n"
      "buffer_load_dwordx4 v[100:103], v200, s[8:11], s57 offen offset:768\n"
      "s_add_u32 s57, s57, 7168\n"
      "buffer_load_dwordx4 v[104:107], v200, s[8:11], s57 offen offset:0\n"
      "buffer_load_dwordx4 v[108:111], v200, s[8:11], s57 offen offset:128\n"
      "buffer_load_dwordx4 v[112:115], v200, s[8:11], s57 offen offset:256\n"
      "buffer_load_dwordx4 v[116:119], v200, s[8:11], s57 offen offset:384\n"
      "buffer_load_dwordx4 v[120:123], v200, s[8:11], s57 offen offset:512\n"
      "buffer_load_dwordx4 v[124:127], v200, s[8:11], s57 offen offset:640\n"
      "buffer_load_dwordx4 v[128:131], v200, s[8:11], s57 offen offset:768\n"
      "s_add_u32 s57, s57, 7168\n"
      "buffer_load_dwordx4 v[132:135], v200, s[8:11], s57 offen offset:0\n"
      "buffer_load_dwordx4 v[136:139], v200, s[8:11], s57 offen offset:128\n"
      "buffer_load_dwordx4 v[140:143], v200, s[8:11], s57 offen offset:256\n"
      "buffer_load_dwordx4 v[144:147], v200, s[8:11], s57 offen offset:384\n"
      "buffer_load_dwordx4 v[148:151], v200, s[8:11], s57 offen offset:512\n"
      "buffer_load_dwordx4 v[152:155], v200, s[8:11], s57 offen offset:640\n"
      "buffer_load_dwordx4 v[156:159], v200, s[8:11], s57 offen offset:768\n"
      "s_add_u32 s57, s57, 7168\n"
      "s_waitcnt vmcnt(27)\n"
      "ds_write_b128 v201, v[48:51] offset:0\n"
      "buffer_load_dwordx4 v[48:51], v200, s[8:11], s57 offen offset:0\n"
      "s_waitcnt vmcnt(27)\n"
      "ds_write_b128 v201, v[52:55] offset:1024\n"
      "buffer_load_dwordx4 v[52:55], v200, s[8:11], s57 offen offset:128\n"
      "s_waitcnt vmcnt(27)\n"
      "ds_write_b128 v201, v[56:59] offset:2048\n"
      "buffer_load_dwordx4 v[56:59], v200, s[8:11], s57 offen offset:256\n"
      "s_waitcnt vmcnt(27)\n"
      "ds_write_b128 v201, v[60:63] offset:3072\n"
      "buffer_load_dwordx4 v[60:63], v200, s[8:11], s57 offen offset:384\n"
      "s_waitcnt vmcnt(27)\n"
      "ds_write_b128 v201, v[64:67] offset:4096\n"
      "buffer_load_dwordx4 v[64:67], v200, s[8:11], s57 offen offset:512\n"
      "s_waitcnt vmcnt(27)\n"
      "ds_write_b128 v201, v[68:71] offset:5120\n"
      "buffer_load_dwordx4 v[68:71], v200, s[8:11], s57 offen offset:640\n"
      "s_waitcnt vmcnt(27)\n"
      "ds_write_b128 v201, v[72:75] offset:6144\n"
      "buffer_load_dwordx4 v[72:75], v200, s[8:11], s57 offen offset:768\n"
      "s_add_u32 s57, s57, 7168\n"
      "s_waitcnt vmcnt(27)\n"
      "ds_write_b128 v202, v[76:79] offset:7168\n"
      "buffer_load_dwordx4 v[76:79], v200, s[8:11], s57 offen offset:0\n"
      "s_waitcnt vmcnt(27)\n"
      "ds_write_b128 v202, v[80:83] offset:8192\n"
      "buffer_load_dwordx4 v[80:83], v200, s[8:11], s57 offen offset:128\n"
      "s_waitcnt vmcnt(27)\n"
      "ds_write_b128 v202, v[84:87] offset:9216\n"
      "buffer_load_dwordx4 v[84:87], v200, s[8:11], s57 offen offset:256\n"
      "s_waitcnt vmcnt(27)\n"
      "ds_write_b128 v202, v[88:91] offset:10240\n"
      "buffer_load_dwordx4 v[88:91], v200, s[8:11], s57 offen offset:384\n"
      "s_waitcnt vmcnt(27)\n"
      "ds_write_b128 v202, v[92:95] offset:11264\n"
      "buffer_load_dwordx4 v[92:95], v200, s[8:11], s57 offen offset:512\n"
      "s_waitcnt vmcnt(27)\n"
      "ds_write_b128 v202, v[96:99] offset:12288\n"
      "buffer_load_dwordx4 v[96:99], v200, s[8:11], s57 offen offset:640\n"
      "s_waitcnt vmcnt(27)\n"
      "ds_write_b128 v202, v[100:103] offset:13312\n"
      "buffer_load_dwordx4 v[100:103], v200, s[8:11], s57 offen offset:768\n"
      "s_add_u32 s57, s57, 7168\n"
      "s_waitcnt vmcnt(27)\n"
      "ds_write_b128 v201, v[104:107] offset:14336\n"
      "buffer_load_dwordx4 v[104:107], v200, s[8:11], s57 offen offset:0\n"
      "s_waitcnt vmcnt(27)\n"
      "ds_write_b128 v201, v[108:111] offset:15360\n"
      "buffer_load_dwordx4 v[108:111], v200, s[8:11], s57 offen offset:128\n"
      "s_waitcnt vmcnt(27)\n"
      "ds_write_b128 v201, v[112:115] offset:16384\n"
      "buffer_load_dwordx4 v[112:115], v200, s[8:11], s57 offen offset:256\n"
      "s_waitcnt vmcnt(27)\n"
      "ds_write_b128 v201, v[116:119] offset:17408\n"
      "buffer_load_dwordx4 v[116:119], v200, s[8:11], s57 offen offset:384\n"
      "s_waitcnt vmcnt(27)\n"
      "ds_write_b128 v201, v[120:123] offset:18432\n"
      "buffer_load_dwordx4 v[120:123], v200, s[8:11], s57 offen offset:512\n"
      "s_waitcnt vmcnt(27)\n"
      "ds_write_b128 v201, v[124:127] offset:19456\n"
      "buffer_load_dwordx4 v[124:127], v200, s[8:11], s57 offen offset:640\n"
      "s_waitcnt vmcnt(27)\n"
      "ds_write_b128 v201, v[128:131] offset:20480\n"
      "buffer_load_dwordx4 v[128:131], v200, s[8:11], s57 offen offset:768\n"
      "s_add_u32 s57, s57, 7168\n"
      "s_waitcnt vmcnt(27)\n"
      "ds_write_b128 v202, v[132:135] offset:21504\n"
      "buffer_load_dwordx4 v[132:135], v200, s[8:11], s57 offen offset:0\n"
      "s_waitcnt vmcnt(27)\n"
      "ds_write_b128 v202, v[136:139] offset:22528\n"
      "buffer_load_dwordx4 v[136:139], v200, s[8:11], s57 offen offset:128\n"
      "s_waitcnt vmcnt(27)\n"
      "ds_write_b128 v202, v[140:143] offset:23552\n"
      "buffer_load_dwordx4 v[140:143], v200, s[8:11], s57 offen offset:256\n"
      "s_waitcnt vmcnt(27)\n"
      "ds_write_b128 v202, v[144:147] offset:24576\n"
      "buffer_load_dwordx4 v[144:147], v200, s[8:11], s57 offen offset:384\n"
      "s_waitcnt vmcnt(27)\n"
      "ds_write_b128 v202, v[148:151] offset:25600\n"
      "buffer_load_dwordx4 v[148:151], v200, s[8:11], s57 offen offset:512\n"
      "s_waitcnt vmcnt(27)\n"
      "ds_write_b128 v202, v[152:155] offset:26624\n"
      "buffer_load_dwordx4 v[152:155], v200, s[8:11], s57 offen offset:640\n"
      "s_waitcnt vmcnt(27)\n"
      "ds_write_b128 v202, v[156:159] offset:27648\n"
      "buffer_load_dwordx4 v[156:159], v200, s[8:11], s57 offen offset:768\n"
      "s_add_u32 s57, s57, 7168\n"
      "s_waitcnt vmcnt(27)\n"
      "ds_write_b128 v201, v[48:51] offset:28672\n"
      "s_waitcnt vmcnt(26)\n"
      "ds_write_b128 v201, v[52:55] offset:29696\n"
      "s_waitcnt vmcnt(25)\n"
      "ds_write_b128 v201, v[56:59] offset:30720\n"
      "s_waitcnt vmcnt(24)\n"
      "ds_write_b128 v201, v[60:63] offset:31744\n"
      "s_waitcnt vmcnt(23)\n"
      "ds_write_b128 v201, v[64:67] offset:32768\n"
      "s_waitcnt vmcnt(22)\n"
      "ds_write_b128 v201, v[68:71] offset:33792\n"
      "s_waitcnt vmcnt(21)\n"
      "ds_write_b128 v201, v[72:75] offset:34816\n"
      "s_waitcnt vmcnt(20)\n"
      "ds_write_b128 v202, v[76:79] offset:35840\n"
      "s_waitcnt vmcnt(19)\n"
      "ds_write_b128 v202, v[80:83] offset:36864\n"
      "s_waitcnt vmcnt(18)\n"
      "ds_write_b128 v202, v[84:87] offset:37888\n"
      "s_waitcnt vmcnt(17)\n"
      "ds_write_b128 v202, v[88:91] offset:38912\n"
      "s_waitcnt vmcnt(16)\n"
      "ds_write_b128 v202, v[92:95] offset:39936\n"
      "s_waitcnt vmcnt(15)\n"
      "ds_write_b128 v202, v[96:99] offset:40960\n"
      "s_waitcnt vmcnt(14)\n"
      "ds_write_b128 v202, v[100:103] offset:41984\n"
      "s_waitcnt vmcnt(13)\n"
      "ds_write_b128 v201, v[104:107] offset:43008\n"
      "s_waitcnt vmcnt(12)\n"
      "ds_write_b128 v201, v[108:111] offset:44032\n"
      "s_waitcnt vmcnt(11)\n"
      "ds_write_b128 v201, v[112:115] offset:45056\n"
      "s_waitcnt vmcnt(10)\n"
      "ds_write_b128 v201, v[116:119] offset:46080\n"
      "s_waitcnt vmcnt(9)\n"
      "ds_write_b128 v201, v[120:123] offset:47104\n"
      "s_waitcnt vmcnt(8)\n"
      "ds_write_b128 v201, v[124:127] offset:48128\n"
      "s_waitcnt vmcnt(7)\n"
      "ds_write_b128 v201, v[128:131] offset:49152\n"
      "s_waitcnt vmcnt(6)\n"
      "ds_write_b128 v202, v[132:135] offset:50176\n"
      "s_waitcnt vmcnt(5)\n"
      "ds_write_b128 v202, v[136:139] offset:51200\n"
      "s_waitcnt vmcnt(4)\n"
      "ds_write_b128 v202, v[140:143] offset:52224\n"
      "s_waitcnt vmcnt(3)\n"
      "ds_write_b128 v202, v[144:147] offset:53248\n"
      "s_waitcnt vmcnt(2)\n"
      "ds_write_b128 v202, v[148:151] offset:54272\n"
      "s_waitcnt vmcnt(1)\n"
      "ds_write_b128 v202, v[152:155] offset:55296\n"
      "s_waitcnt vmcnt(0)\n"
      "ds_write_b128 v202, v[156:159] offset:56320\n"
      "v_readfirstlane_b32 s18, v184\n"
      "v_readfirstlane_b32 s19, v185\n"
      "s_mul_i32 s16, s18, 0x7f4a7c15\n"
      "s_mul_hi_u32 s17, s18, 0x7f4a7c15\n"
      "s_mul_i32 s34, s18, 0x9e3779b9\n"
      "s_add_u32 s17, s17, s34\n"
      "s_mul_i32 s34, s19, 0x7f4a7c15\n"
      "s_add_u32 s17, s17, s34\n"
      "s_xor_b32 s17, s17, 0x632be59b\n"
      "s_and_b32 s16, s16, 0xffffff00\n"
      "s_branch L_X0\n"
      "L_PUB:\n"
      "s_mul_i32 s34, s14, 75264\n"
      "s_add_u32 s34, s34, s50\n"
      "s_mul_i32 s36, s12, 1792\n"
      "s_add_u32 s56, s34, s36\n"
      "s_add_u32 s57, s56, 25088\n"
      "s_add_u32 s58, s56, 50176\n"
      "s_lshl_b32 s34, s14, 2\n"
      "s_add_u32 s34, s34, 159744\n"
      "v_mov_b32 v203, s34\n"
      "v_mov_b32 v204, 1\n"
      "s_cmp_ge_u32 s12, 14\n"
      "s_waitcnt vmcnt(0)\n"
      "s_cbranch_scc1 L_PUB1\n"
      "buffer_store_dwordx4 v[168:171], v0, s[4:7], s57 offen offset:0 sc1\n"
      "buffer_store_dwordx4 v[160:163], v0, s[4:7], s56 offen offset:0 sc1\n"
      "buffer_store_dwordx4 v[176:179], v0, s[4:7], s58 offen offset:0 sc1\n"
      "s_mov_b32 exec_hi, 0xffff\n"
      "buffer_store_dwordx4 v[172:175], v0, s[4:7], s57 offen offset:1024 sc1\n"
      "buffer_store_dwordx4 v[164:167], v0, s[4:7], s56 offen offset:1024 sc1\n"
      "buffer_store_dwordx4 v[180:183], v0, s[4:7], s58 offen offset:1024 sc1\n"
      "s_mov_b64 exec, -1\n"
      "ds_write_b32 v203, v204\n"
      "; column sums over these 32 rows (LDS bounce, ones x W); summed over the row blocks by the consumers\n"
      "s_lshl_b32 s34, s14, 13\n"
      "s_add_u32 s34, s34, 114688\n"
      "v_add_u32 v205, s34, v0\n"
      "v_add_u32 v206, s34, v2\n"
      "ds_write_b128 v205, v[160:163] offset:0\n"
      "ds_write_b128 v205, v[164:167] offset:1024\n"
      "ds_write_b128 v205, v[168:171] offset:2048\n"
      "ds_write_b128 v205, v[172:175] offset:3072\n"
      "ds_write_b128 v205, v[176:179] offset:4096\n"
      "ds_write_b128 v205, v[180:183] offset:5120\n"
      "ds_read_b64_tr_b16 a[176:177], v206 offset:0\n"
      "ds_read_b64_tr_b16 a[178:179], v206 offset:224\n"
      "ds_read_b64_tr_b16 a[180:181], v206 offset:896\n"
      "ds_read_b64_tr_b16 a[182:183], v206 offset:1120\n"
      "ds_read_b64_tr_b16 a[184:185], v206 offset:2048\n"
      "ds_read_b64_tr_b16 a[186:187], v206 offset:2272\n"
      "ds_read_b64_tr_b16 a[188:189], v206 offset:2944\n"
      "ds_read_b64_tr_b16 a[190:191], v206 offset:3168\n"
      "ds_read_b64_tr_b16 a[192:193], v206 offset:4096\n"
      "ds_read_b64_tr_b16 a[194:195], v206 offset:4320\n"
      "ds_read_b64_tr_b16 a[196:197], v206 offset:4992\n"
      "ds_read_b64_tr_b16 a[198:199], v206 offset:5216\n"
      "s_waitcnt lgkmcnt(0)\n"
      "v_mfma_f32_32x32x16_bf16 v[48:63], v[252:255], a[176:179], 0\n"
      "v_mfma_f32_32x32x16_bf16 v[48:63], v[252:255], a[180:183], v[48:63]\n"
      "v_mfma_f32_32x32x16_bf16 v[64:79], v[252:255], a[184:187], 0\n"
      "v_mfma_f32_32x32x16_bf16 v[64:79], v[252:255], a[188:191], v[64:79]\n"
      "v_mfma_f32_32x32x16_bf16 v[80:95], v[252:255], a[192:195], 0\n"
      "v_mfma_f32_32x32x16_bf16 v[80:95], v[252:255], a[196:199], v[80:95]\n"
      "v_cmp_gt_u32 vcc, 28, v225\n"
      "s_nop 10\n"
      "v_cndmask_b32 v96, 0, v64, vcc\n"
      "v_cndmask_b32 v98, 0, v80, vcc\n"
      "v_cndmask_b32 v99, 0, v48, vcc\n"
      "s_branch L_PUB2\n"
      "L_PUB1:\n"
      "ds_write_b32 v203, v204\n"
      "v_mov_b32 v96, 0\n"
      "v_mov_b64 v[98:99], 0\n"
      "L_PUB2:\n"
      "s_lshl_b32 s34, s13, 1\n"
      "s_add_u32 s34, s34, s14\n"
      "s_lshl_b32 s34, s34, 4\n"
      "s_add_u32 s34, s34, s12\n"
      "s_lshl_b32 s34, s34, 9\n"
      "s_add_u32 s34, s34, 262144\n"
      "v_mov_b32 v97, 0\n"
      "s_mov_b32 s37, s34\n"
      "v_readfirstlane_b32 s18, v184\n"
      "v_readfirstlane_b32 s19, v185\n"
      "s_mul_i32 s16, s18, 0x7f4a7c15\n"
      "s_mul_hi_u32 s17, s18, 0x7f4a7c15\n"
      "s_mul_i32 s34, s18, 0x9e3779b9\n"
      "s_add_u32 s17, s17, s34\n"
      "s_mul_i32 s34, s19, 0x7f4a7c15\n"
      "s_add_u32 s17, s17, s34\n"
      "s_xor_b32 s17, s17, 0x632be59b\n"
      "s_and_b32 s16, s16, 0xffffff00\n"
      "s_add_u32 s34, s16, 1\n"
      "v_mov_b32 v102, s34\n"
      "v_mov_b32 v103, s17\n"
      "s_lshl_b32 s36, s14, 7\n"
      "s_add_u32 s36, s36, s81\n"
      "buffer_store_dwordx4 v[96:99], v216, s[4:7], s37 offen sc1\n"
      "s_waitcnt vmcnt(0)\n"
      "buffer_atomic_swap_x2 v[102:103], v6, s[4:7], s36 offen\n"
      "L_X0:\n"
      "s_waitcnt lgkmcnt(0)\n"
      "s_barrier\n"
      "s_mov_b32 s8, s24\n"
      "s_and_b32 s9, s25, 0xffff\n"
      "s_mov_b32 s10, 401408\n"
      "s_mov_b32 s38, s84\n"
      "v_lshrrev_b32 v246, 7, v0\n"
      "v_mul_u32_u24 v246, 896, v246\n"
      "v_lshrrev_b32 v247, 2, v0\n"
      "v_and_b32 v247, 64, v247\n"
      "v_xor_b32 v247, v247, v3\n"
      "v_and_b32 v247, 0x80000070, v247\n"
      "v_add_u32 v246, v246, v247\n"
      "v_add_u32 v247, 7168, v246\n"
      "buffer_load_dwordx4 a[56:59], v246, s[8:11], s38 offen\n"
      "buffer_load_dwordx4 a[60:63], v247, s[8:11], s38 offen\n"
      "s_addk_i32 s38, 14336\n"
      "ds_read_b128 v[48:51], v240 offset:0\n"
      "ds_read_b128 v[52:55], v241 offset:0\n"
      "ds_read_b128 v[56:59], v242 offset:0\n"
      "ds_read_b128 v[60:63], v243 offset:0\n"
      "ds_read_b128 v[64:67], v240 offset:1024\n"
      "ds_read_b128 v[68:71], v241 offset:1024\n"
      "ds_read_b128 v[72:75], v242 offset:1024\n"
      "ds_read_b128 v[76:79], v243 offset:1024\n"
      "ds_read_b128 v[80:83], v240 offset:2048\n"
      "ds_read_b128 v[84:87], v241 offset:2048\n"
      "ds_read_b128 v[88:91], v242 offset:2048\n"
      "ds_read_b128 v[92:95], v243 offset:2048\n"
      "ds_read_b128 v[96:99], v240 offset:3072\n"
      "ds_read_b128 v[100:103], v241 offset:3072\n"
      "buffer_load_dwordx4 a[64:67], v246, s[8:11], s38 offen\n"
      "buffer_load_dwordx4 a[68:71], v247, s[8:11], s38 offen\n"
      "s_addk_i32 s38, 14336\n"
      "ds_read_b128 v[104:107], v242 offset:3072\n"
      "ds_read_b128 v[108:111], v243 offset:3072\n"
      "ds_read_b128 v[112:115], v240 offset:4096\n"
      "ds_read_b128 v[116:119], v241 offset:4096\n"
      "ds_read_b128 v[120:123], v242 offset:4096\n"
      "ds_read_b128 v[124:127], v243 offset:4096\n"
      "ds_read_b128 v[128:131], v240 offset:5120\n"
      "ds_read_b128 v[132:135], v241 offset:5120\n"
      "ds_read_b128 v[136:139], v242 offset:5120\n"
      "ds_read_b128 v[140:143], v243 offset:5120\n"
      "ds_read_b128 v[144:147], v240 offset:6144\n"
      "ds_read_b128 v[148:151], v241 offset:6144\n"
      "ds_read_b128 v[152:155], v242 offset:6144\n"
      "ds_read_b128 v[156:159], v243 offset:6144\n"
      "buffer_load_dwordx4 a[72:75], v246, s[8:11], s38 offen\n"
      "buffer_load_dwordx4 a[76:79], v247, s[8:11], s38 offen\n"
      "s_addk_i32 s38, 14336\n"
      "ds_read_b128 v[160:163], v244\n"
      "ds_read_b128 v[164:167], v245\n"
      "; row sums of x0, residual slice; the weight images arrive meanwhile\n"
      "s_waitcnt lgkmcnt(0)\n"
      "buffer_load_dwordx4 a[80:83], v246, s[8:11], s38 offen\n"
      "buffer_load_dwordx4 a[84:87], v247, s[8:11], s38 offen\n"
      "s_addk_i32 s38, 14336\n"
      "v_dot2c_f32_bf16 v192, v48, v252\n"
      "v_dot2c_f32_bf16 v193, v48, v48\n"
      "v_dot2c_f32_bf16 v194, v49, v252\n"
      "v_dot2c_f32_bf16 v195, v49, v49\n"
      "v_dot2c_f32_bf16 v196, v50, v252\n"
      "v_dot2c_f32_bf16 v197, v50, v50\n"
      "v_dot2c_f32_bf16 v198, v51, v252\n"
      "v_dot2c_f32_bf16 v199, v51, v51\n"
      "v_dot2c_f32_bf16 v192, v52, v252\n"
      "v_dot2c_f32_bf16 v193, v52, v52\n"
      "v_dot2c_f32_bf16 v194, v53, v252\n"
      "v_dot2c_f32_bf16 v195, v53, v53\n"
      "v_dot2c_f32_bf16 v196, v54, v252\n"
      "v_dot2c_f32_bf16 v197, v54, v54\n"
      "v_dot2c_f32_bf16 v198, v55, v252\n"
      "v_dot2c_f32_bf16 v199, v55, v55\n"
      "v_dot2c_f32_bf16 v192, v56, v252\n"
      "v_dot2c_f32_bf16 v193, v56, v56\n"
      "v_dot2c_f32_bf16 v194, v57, v252\n"
      "v_dot2c_f32_bf16 v195, v57, v57\n"
      "v_dot2c_f32_bf16 v196, v58, v252\n"
      "v_dot2c_f32_bf16 v197, v58, v58\n"
      "v_dot2c_f32_bf16 v198, v59, v252\n"
      "v_dot2c_f32_bf16 v199, v59, v59\n"
      "buffer_load_dwordx4 a[88:91], v246, s[8:11], s38 offen\n"
      "buffer_load_dwordx4 a[92:95], v247, s[8:11], s38 offen\n"
      "s_addk_i32 s38, 14336\n"
      "v_dot2c_f32_bf16 v192, v60, v252\n"
      "v_dot2c_f32_bf16 v193, v60, v60\n"
      "v_dot2c_f32_bf16 v194, v61, v252\n"
      "v_dot2c_f32_bf16 v195, v61, v61\n"
      "v_dot2c_f32_bf16 v196, v62, v252\n"
      "v_dot2c_f32_bf16 v197, v62, v62\n"
      "v_dot2c_f32_bf16 v198, v63, v252\n"
      "v_dot2c_f32_bf16 v199, v63, v63\n"
      "v_dot2c_f32_bf16 v192, v64, v252\n"
      "v_dot2c_f32_bf16 v193, v64, v64\n"
      "v_dot2c_f32_bf16 v194, v65, v252\n"
      "v_dot2c_f32_bf16 v195, v65, v65\n"
      "v_dot2c_f32_bf16 v196, v66, v252\n"
      "v_dot2c_f32_bf16 v197, v66, v66\n"
      "v_dot2c_f32_bf16 v198, v67, v252\n"
      "v_dot2c_f32_bf16 v199, v67, v67\n"
      "v_dot2c_f32_bf16 v192, v68, v252\n"
      "v_dot2c_f32_bf16 v193, v68, v68\n"
      "v_dot2c_f32_bf16 v194, v69, v252\n"
      "v_dot2c_f32_bf16 v195, v69, v69\n"
      "v_dot2c_f32_bf16 v196, v70, v252\n"
      "v_dot2c_f32_bf16 v197, v70, v70\n"
      "v_dot2c_f32_bf16 v198, v71, v252\n"
      "v_dot2c_f32_bf16 v199, v71, v71\n"
      "buffer_load_dwordx4 a[96:99], v246, s[8:11], s38 offen\n"
      "buffer_load_dwordx4 a[100:103], v247, s[8:11], s38 offen\n"
      "s_addk_i32 s38, 14336\n"
      "v_dot2c_f32_bf16 v192, v72, v252\n"
      "v_dot2c_f32_bf16 v193, v72, v72\n"
      "v_dot2c_f32_bf16 v194, v73, v252\n"
      "v_dot2c_f32_bf16 v195, v73, v73\n"
      "v_dot2c_f32_bf16 v196, v74, v252\n"
      "v_dot2c_f32_bf16 v197, v74, v74\n"
      "v_dot2c_f32_bf16 v198, v75, v252\n"
      "v_dot2c_f32_bf16 v199, v75, v75\n"
      "v_dot2c_f32_bf16 v192, v76, v252\n"
      "v_dot2c_f32_bf16 v193, v76, v76\n"
      "v_dot2c_f32_bf16 v194, v77, v252\n"
      "v_dot2c_f32_bf16 v195, v77, v77\n"
      "v_dot2c_f32_bf16 v196, v78, v252\n"
      "v_dot2c_f32_bf16 v197, v78, v78\n"
      "v_dot2c_f32_bf16 v198, v79, v252\n"
      "v_dot2c_f32_bf16 v199, v79, v79\n"
      "v_dot2c_f32_bf16 v192, v80, v252\n"
      "v_dot2c_f32_bf16 v193, v80, v80\n"
      "v_dot2c_f32_bf16 v194, v81, v252\n"
      "v_dot2c_f32_bf16 v195, v81, v81\n"
      "v_dot2c_f32_bf16 v196, v82, v252\n"
      "v_dot2c_f32_bf16 v197, v82, v82\n"
      "v_dot2c_f32_bf16 v198, v83, v252\n"
      "v_dot2c_f32_bf16 v199, v83, v83\n"
      "buffer_load_dwordx4 a[104:107], v246, s[8:11], s38 offen\n"
      "buffer_load_dwordx4 a[108:111], v247, s[8:11], s38 offen\n"
      "s_addk_i32 s38, 14336\n"
      "v_dot2c_f32_bf16 v192, v84, v252\n"
      "v_dot2c_f32_bf16 v193, v84, v84\n"
      "v_dot2c_f32_bf16 v194, v85, v252\n"
      "v_dot2c_f32_bf16 v195, v85, v85\n"
      "v_dot2c_f32_bf16 v196, v86, v252\n"
      "v_dot2c_f32_bf16 v197, v86, v86\n"
      "v_dot2c_f32_bf16 v198, v87, v252\n"
      "v_dot2c_f32_bf16 v199, v87, v87\n"
      "s_cmp_lg_u32 s14, 3\n"
      "s_cbranch_scc1 L_ep_1\n"
      "s_mov_b32 exec_hi, 0\n"
      "v_mov_b64 v[28:29], 0\n"
      "buffer_atomic_or_x2 v[28:29], v7, s[4:7], s83 offen sc0\n"
      "s_mov_b64 exec, -1\n"
      "L_ep_1:\n"
      "v_dot2c_f32_bf16 v192, v88, v252\n"
      "v_dot2c_f32_bf16 v193, v88, v88\n"
      "v_dot2c_f32_bf16 v194, v89, v252\n"
      "v_dot2c_f32_bf16 v195, v89, v89\n"
      "v_dot2c_f32_bf16 v196, v90, v252\n"
      "v_dot2c_f32_bf16 v197, v90, v90\n"
      "v_dot2c_f32_bf16 v198, v91, v252\n"
      "v_dot2c_f32_bf16 v199, v91, v91\n"
      "v_dot2c_f32_bf16 v192, v92, v252\n"
      "v_dot2c_f32_bf16 v193, v92, v92\n"
      "v_dot2c_f32_bf16 v194, v93, v252\n"
      "v_dot2c_f32_bf16 v195, v93, v93\n"
      "v_dot2c_f32_bf16 v196, v94, v252\n"
      "v_dot2c_f32_bf16 v197, v94, v94\n"
      "v_dot2c_f32_bf16 v198, v95, v252\n"
      "v_dot2c_f32_bf16 v199, v95, v95\n"
      "v_dot2c_f32_bf16 v192, v96, v252\n"
      "v_dot2c_f32_bf16 v193, v96, v96\n"
      "v_dot2c_f32_bf16 v194, v97, v252\n"
      "v_dot2c_f32_bf16 v195, v97, v97\n"
      "v_dot2c_f32_bf16 v196, v98, v252\n"
      "v_dot2c_f32_bf16 v197, v98, v98\n"
      "v_dot2c_f32_bf16 v198, v99, v252\n"
      "v_dot2c_f32_bf16 v199, v99, v99\n"
      "v_dot2c_f32_bf16 v192, v100, v252\n"
      "v_dot2c_f32_bf16 v193, v100, v100\n"
      "v_dot2c_f32_bf16 v194, v101, v252\n"
      "v_dot2c_f32_bf16 v195, v101, v101\n"
      "v_dot2c_f32_bf16 v196, v102, v252\n"
      "v_dot2c_f32_bf16 v197, v102, v102\n"
      "v_dot2c_f32_bf16 v198, v103, v252\n"
      "v_dot2c_f32_bf16 v199, v103, v103\n"
      "v_dot2c_f32_bf16 v192, v104, v252\n"
      "v_dot2c_f32_bf16 v193, v104, v104\n"
      "v_dot2c_f32_bf16 v194, v105, v252\n"
      "v_dot2c_f32_bf16 v195, v105, v105\n"
      "v_dot2c_f32_bf16 v196, v106, v252\n"
      "v_dot2c_f32_bf16 v197, v106, v106\n"
      "v_dot2c_f32_bf16 v198, v107, v252\n"
      "v_dot2c_f32_bf16 v199, v107, v107\n"
      "v_dot2c_f32_bf16 v192, v108, v252\n"
      "v_dot2c_f32_bf16 v193, v108, v108\n"
      "v_dot2c_f32_bf16 v194, v109, v252\n"
      "v_dot2c_f32_bf16 v195, v109, v109\n"
      "v_dot2c_f32_bf16 v196, v110, v252\n"
      "v_dot2c_f32_bf16 v197, v110, v110\n"
      "v_dot2c_f32_bf16 v198, v111, v252\n"
      "v_dot2c_f32_bf16 v199, v111, v111\n"
      "v_dot2c_f32_bf16 v192, v112, v252\n"
      "v_dot2c_f32_bf16 v193, v112, v112\n"
      "v_dot2c_f32_bf16 v194, v113, v252\n"
      "v_dot2c_f32_bf16 v195, v113, v113\n"
      "v_dot2c_f32_bf16 v196, v114, v252\n"
      "v_dot2c_f32_bf16 v197, v114, v114\n"
      "v_dot2c_f32_bf16 v198, v115, v252\n"
      "v_dot2c_f32_bf16 v199, v115, v115\n"
      "v_dot2c_f32_bf16 v192, v116, v252\n"
      "v_dot2c_f32_bf16 v193, v116, v116\n"
      "v_dot2c_f32_bf16 v194, v117, v252\n"
      "v_dot2c_f32_bf16 v195, v117, v117\n"
      "v_dot2c_f32_bf16 v196, v118, v252\n"
      "v_dot2c_f32_bf16 v197, v118, v118\n"
      "v_dot2c_f32_bf16 v198, v119, v252\n"
      "v_dot2c_f32_bf16 v199, v119, v119\n"
      "v_dot2c_f32_bf16 v192, v120, v252\n"
      "v_dot2c_f32_bf16 v193, v120, v120\n"
      "v_dot2c_f32_bf16 v194, v121, v252\n"
      "v_dot2c_f32_bf16 v195, v121, v121\n"
      "v_dot2c_f32_bf16 v196, v122, v252\n"
      "v_dot2c_f32_bf16 v197, v122, v122\n"
      "v_dot2c_f32_bf16 v198, v123, v252\n"
      "v_dot2c_f32_bf16 v199, v123, v123\n"
      "v_dot2c_f32_bf16 v192, v124, v252\n"
      "v_dot2c_f32_bf16 v193, v124, v124\n"
      "v_dot2c_f32_bf16 v194, v125, v252\n"
      "v_dot2c_f32_bf16 v195, v125, v125\n"
      "v_dot2c_f32_bf16 v196, v126, v252\n"
      "v_dot2c_f32_bf16 v197, v126, v126\n"
      "v_dot2c_f32_bf16 v198, v127, v252\n"
      "v_dot2c_f32_bf16 v199, v127, v127\n"
      "v_dot2c_f32_bf16 v192, v128, v252\n"
      "v_dot2c_f32_bf16 v193, v128, v128\n"
      "v_dot2c_f32_bf16 v194, v129, v252\n"
      "v_dot2c_f32_bf16 v195, v129, v129\n"
      "v_dot2c_f32_bf16 v196, v130, v252\n"
      "v_dot2c_f32_bf16 v197, v130, v130\n"
      "v_dot2c_f32_bf16 v198, v131, v252\n"
      "v_dot2c_f32_bf16 v199, v131, v131\n"
      "; hand-off: wave 3 polls its flags, barrier\n"
      "s_add_u32 s34, s16, 1\n"
      "s_cmp_lg_u32 s14, 3\n"
      "s_cbranch_scc1 L_pd_3\n"
      "s_mov_b32 s51, 0x40000\n"
      "s_mov_b32 exec_hi, 0\n"
      "s_branch L_pc_4\n"
      "L_poll_2:\n"
      "s_mov_b32 exec_hi, 0\n"
      "v_mov_b64 v[28:29], 0\n"
      "buffer_atomic_or_x2 v[28:29], v7, s[4:7], s83 offen sc0\n"
      "s_sub_u32 s51, s51, 1\n"
      "s_cbranch_scc1 L_pd_3\n"
      "L_pc_4:\n"
      "s_waitcnt vmcnt(0)\n"
      "v_subrev_u32 v28, s34, v28\n"
      "v_cmp_eq_u32 s[40:41], s17, v29\n"
      "v_cmp_gt_u32 vcc, 64, v28\n"
      "s_and_b64 vcc, vcc, s[40:41]\n"
      "s_cmp_eq_u64 vcc, exec\n"
      "s_cbranch_scc0 L_poll_2\n"
      "L_pd_3:\n"
      "s_mov_b64 exec, -1\n"
      "s_waitcnt lgkmcnt(0)\n"
      "s_barrier\n"
      "s_add_u32 s36, s50, 0\n"
      "buffer_load_dwordx4 a[24:27], v14, s[4:7], s36 offen sc1\n"
      "s_add_u32 s36, s50, 25088\n"
      "s_add_u32 s37, s36, 4096\n"
      "buffer_load_dwordx4 a[208:211], v13, s[4:7], s36 offen sc1\n"
      "v_dot2c_f32_bf16 v192, v132, v252\n"
      "v_dot2c_f32_bf16 v193, v132, v132\n"
      "v_dot2c_f32_bf16 v194, v133, v252\n"
      "v_dot2c_f32_bf16 v195, v133, v133\n"
      "v_dot2c_f32_bf16 v196, v134, v252\n"
      "v_dot2c_f32_bf16 v197, v134, v134\n"
      "v_dot2c_f32_bf16 v198, v135, v252\n"
      "v_dot2c_f32_bf16 v199, v135, v135\n"
      "v_dot2c_f32_bf16 v192, v136, v252\n"
      "v_dot2c_f32_bf16 v193, v136, v136\n"
      "v_dot2c_f32_bf16 v194, v137, v252\n"
      "v_dot2c_f32_bf16 v195, v137, v137\n"
      "v_dot2c_f32_bf16 v196, v138, v252\n"
      "v_dot2c_f32_bf16 v197, v138, v138\n"
      "buffer_load_dwordx4 a[212:215], v13, s[4:7], s36 offen offset:1024 sc1\n"
      "v_dot2c_f32_bf16 v198, v139, v252\n"
      "v_dot2c_f32_bf16 v199, v139, v139\n"
      "v_dot2c_f32_bf16 v192, v140, v252\n"
      "v_dot2c_f32_bf16 v193, v140, v140\n"
      "v_dot2c_f32_bf16 v194, v141, v252\n"
      "v_dot2c_f32_bf16 v195, v141, v141\n"
      "v_dot2c_f32_bf16 v196, v142, v252\n"
      "v_dot2c_f32_bf16 v197, v142, v142\n"
      "v_dot2c_f32_bf16 v198, v143, v252\n"
      "v_dot2c_f32_bf16 v199, v143, v143\n"
      "v_dot2c_f32_bf16 v192, v144, v252\n"
      "v_dot2c_f32_bf16 v193, v144, v144\n"
      "v_dot2c_f32_bf16 v194, v145, v252\n"
      "v_dot2c_f32_bf16 v195, v145, v145\n"
      "buffer_load_dwordx4 a[216:219], v13, s[4:7], s36 offen offset:2048 sc1\n"
      "v_dot2c_f32_bf16 v196, v146, v252\n"
      "v_dot2c_f32_bf16 v197, v146, v146\n"
      "v_dot2c_f32_bf16 v198, v147, v252\n"
      "v_dot2c_f32_bf16 v199, v147, v147\n"
      "v_dot2c_f32_bf16 v192, v148, v252\n"
      "v_dot2c_f32_bf16 v193, v148, v148\n"
      "v_dot2c_f32_bf16 v194, v149, v252\n"
      "v_dot2c_f32_bf16 v195, v149, v149\n"
      "v_dot2c_f32_bf16 v196, v150, v252\n"
      "v_dot2c_f32_bf16 v197, v150, v150\n"
      "v_dot2c_f32_bf16 v198, v151, v252\n"
      "v_dot2c_f32_bf16 v199, v151, v151\n"
      "v_dot2c_f32_bf16 v192, v152, v252\n"
      "v_dot2c_f32_bf16 v193, v152, v152\n"
      "buffer_load_dwordx4 a[220:223], v13, s[4:7], s36 offen offset:3072 sc1\n"
      "v_dot2c_f32_bf16 v194, v153, v252\n"
      "v_dot2c_f32_bf16 v195, v153, v153\n"
      "v_dot2c_f32_bf16 v196, v154, v252\n"
      "v_dot2c_f32_bf16 v197, v154, v154\n"
      "v_dot2c_f32_bf16 v198, v155, v252\n"
      "v_dot2c_f32_bf16 v199, v155, v155\n"
      "v_dot2c_f32_bf16 v192, v156, v252\n"
      "v_dot2c_f32_bf16 v193, v156, v156\n"
      "v_dot2c_f32_bf16 v194, v157, v252\n"
      "v_dot2c_f32_bf16 v195, v157, v157\n"
      "v_dot2c_f32_bf16 v196, v158, v252\n"
      "v_dot2c_f32_bf16 v197, v158, v158\n"
      "v_dot2c_f32_bf16 v198, v159, v252\n"
      "v_dot2c_f32_bf16 v199, v159, v159\n"
      "buffer_load_dwordx4 a[224:227], v13, s[4:7], s37 offen sc1\n"
      "v_lshlrev_b32 v32, 16, v160\n"
      "v_and_b32 v33, 0xffff0000, v160\n"
      "v_lshlrev_b32 v34, 16, v161\n"
      "v_and_b32 v35, 0xffff0000, v161\n"
      "v_lshlrev_b32 v36, 16, v162\n"
      "v_and_b32 v37, 0xffff0000, v162\n"
      "v_lshlrev_b32 v38, 16, v163\n"
      "v_and_b32 v39, 0xffff0000, v163\n"
      "v_lshlrev_b32 v40, 16, v164\n"
      "v_and_b32 v41, 0xffff0000, v164\n"
      "v_lshlrev_b32 v42, 16, v165\n"
      "v_and_b32 v43, 0xffff0000, v165\n"
      "v_lshlrev_b32 v44, 16, v166\n"
      "v_and_b32 v45, 0xffff0000, v166\n"
      "v_lshlrev_b32 v46, 16, v167\n"
      "v_and_b32 v47, 0xffff0000, v167\n"
      "buffer_load_dwordx4 a[228:231], v13, s[4:7], s37 offen offset:1024 sc1\n"
      "v_mov_b64 v[240:241], v[192:193]\n"
      "v_mov_b64 v[242:243], v[194:195]\n"
      "v_mov_b64 v[244:245], v[196:197]\n"
      "v_mov_b64 v[246:247], v[198:199]\n"
      "v_add_f32 v240, v240, v244\n"
      "v_add_f32 v241, v241, v245\n"
      "v_add_f32 v242, v242, v246\n"
      "v_add_f32 v243, v243, v247\n"
      "v_add_f32 v240, v240, v242\n"
      "v_add_f32 v241, v241, v243\n"
      "s_add_u32 s36, s50, 0\n"
      "s_add_u32 s37, s36, 4096\n"
      "buffer_load_dwordx4 a[232:235], v13, s[4:7], s36 offen sc1\n"
      "v_mov_b32 v242, v240\n"
      "v_mov_b32 v243, v241\n"
      "v_mov_b64 v[208:209], 0\n"
      "v_mov_b64 v[212:213], 0\n"
      "v_permlane32_swap_b32 v240, v242\n"
      "v_permlane32_swap_b32 v241, v243\n"
      "v_add_f32 v240, v240, v242\n"
      "v_add_f32 v241, v241, v243\n"
      "v_mul_f32 v240, 0xbb124925, v240\n"
      "v_mul_f32 v241, 0x3b124925, v241\n"
      "v_fma_f32 v240, v241, 0, v240\n"
      "v_fma_f32 v241, -v240, v240, v241\n"
      "buffer_load_dwordx4 a[236:239], v13, s[4:7], s36 offen offset:1024 sc1\n"
      "v_max_f32 v241, 0, v241\n"
      "v_add_f32 v241, 0x3727c5ac, v241\n"
      "v_rsq_f32 v24, v241\n"
      "v_cvt_pk_bf16_f32 v242, v240, v240\n"
      "ds_write_b32 v11, v24\n"
      "v_and_b32 v242, 0xffff0000, v242\n"
      "v_sub_f32 v242, v240, v242\n"
      "v_cvt_pk_bf16_f32 v210, v240, v242\n"
      "v_mov_b32 v215, v210\n"
      "v_mov_b32 v211, 0\n"
      "v_mov_b32 v214, 0\n"
      "ds_read_b128 v[192:195], v12 offset:0\n"
      "buffer_load_dwordx4 a[240:243], v13, s[4:7], s36 offen offset:2048 sc1\n"
      "ds_read_b128 v[196:199], v12 offset:32\n"
      "ds_read_b128 v[200:203], v12 offset:64\n"
      "ds_read_b128 v[204:207], v12 offset:96\n"
      "buffer_load_dwordx4 a[244:247], v13, s[4:7], s36 offen offset:3072 sc1\n"
      "s_waitcnt vmcnt(10)\n"
      "s_mov_b64 exec, 0xffff\n"
      "ds_write_b128 v14, a[24:27] offset:0\n"
      "s_mov_b64 exec, -1\n"
      "buffer_load_dwordx4 a[248:251], v13, s[4:7], s37 offen sc1\n"
      "s_waitcnt vmcnt(10)\n"
      "ds_write_b128 v13, a[208:211] offset:25088\n"
      "buffer_load_dwordx4 a[252:255], v13, s[4:7], s37 offen offset:1024 sc1\n"
      "s_waitcnt vmcnt(10)\n"
      "ds_write_b128 v13, a[212:215] offset:26112\n"
      "s_lshl_b32 s38, s13, 14\n"
      "s_lshl_b32 s39, s14, 11\n"
      "s_add_u32 s38, s38, s39\n"
      "s_add_u32 s38, s38, 262144\n"
      "s_add_u32 s39, s38, 8192\n"
      "buffer_load_dwordx4 v[224:227], v0, s[4:7], s38 offen sc1\n"
      "s_waitcnt vmcnt(10)\n"
      "ds_write_b128 v13, a[216:219] offset:27136\n"
      "buffer_load_dwordx4 v[228:231], v0, s[4:7], s38 offen offset:1024 sc1\n"
      "s_waitcnt vmcnt(10)\n"
      "ds_write_b128 v13, a[220:223] offset:28160\n"
      "buffer_load_dwordx4 v[232:235], v0, s[4:7], s39 offen sc1\n"
      "s_waitcnt vmcnt(10)\n"
      "ds_write_b128 v13, a[224:227] offset:29184\n"
      "buffer_load_dwordx4 v[236:239], v0, s[4:7], s39 offen offset:1024 sc1\n"
      "s_waitcnt vmcnt(10)\n"
      "ds_write_b128 v13, a[228:231] offset:30208\n"
      "s_waitcnt lgkmcnt(0)\n"
      "s_barrier\n"
      "; K, then V; the column sums are finished beside them\n"
      "ds_read_b64_tr_b16 a[176:177], v2 offset:25088\n"
      "ds_read_b64_tr_b16 a[178:179], v2 offset:25312\n"
      "ds_read_b64_tr_b16 a[180:181], v2 offset:25984\n"
      "ds_read_b64_tr_b16 a[182:183], v2 offset:26208\n"
      "ds_read_b64_tr_b16 a[184:185], v2 offset:26880\n"
      "ds_read_b64_tr_b16 a[186:187], v2 offset:27104\n"
      "ds_read_b64_tr_b16 a[188:189], v2 offset:27776\n"
      "ds_read_b64_tr_b16 a[190:191], v2 offset:28000\n"
      "ds_read_b64_tr_b16 a[192:193], v2 offset:28672\n"
      "ds_read_b64_tr_b16 a[194:195], v2 offset:28896\n"
      "ds_read_b64_tr_b16 a[196:197], v2 offset:29568\n"
      "ds_read_b64_tr_b16 a[198:199], v2 offset:29792\n"
      "ds_read_b64_tr_b16 a[200:201], v2 offset:30464\n"
      "ds_read_b64_tr_b16 a[202:203], v2 offset:30688\n"
      "ds_read_b64_tr_b16 a[204:205], v2 offset:31360\n"
      "ds_read_b64_tr_b16 a[206:207], v2 offset:31584\n"
      "s_waitcnt lgkmcnt(12)\n"
      "v_mfma_f32_32x32x16_bf16 v[160:175], v[48:51], a[176:179], 0\n"
      "ds_read_b64_tr_b16 a[208:209], v2 offset:32256\n"
      "ds_read_b64_tr_b16 a[210:211], v2 offset:32480\n"
      "s_waitcnt vmcnt(9)\n"
      "ds_write_b128 v13, a[232:235] offset:0\n"
      "v_mfma_f32_32x32x16_bf16 v[160:175], v[52:55], a[180:183], v[160:175]\n"
      "ds_read_b64_tr_b16 a[212:213], v2 offset:33152\n"
      "ds_read_b64_tr_b16 a[214:215], v2 offset:33376\n"
      "s_waitcnt vmcnt(8)\n"
      "ds_write_b128 v13, a[236:239] offset:1024\n"
      "s_waitcnt lgkmcnt(14)\n"
      "v_mfma_f32_32x32x16_bf16 v[160:175], v[56:59], a[184:187], v[160:175]\n"
      "ds_read_b64_tr_b16 a[216:217], v2 offset:34048\n"
      "ds_read_b64_tr_b16 a[218:219], v2 offset:34272\n"
      "s_waitcnt vmcnt(7)\n"
      "ds_write_b128 v13, a[240:243] offset:2048\n"
      "v_mfma_f32_32x32x16_bf16 v[160:175], v[60:63], a[188:191], v[160:175]\n"
      "ds_read_b64_tr_b16 a[220:221], v2 offset:34944\n"
      "ds_read_b64_tr_b16 a[222:223], v2 offset:35168\n"
      "s_waitcnt vmcnt(6)\n"
      "ds_write_b128 v13, a[244:247] offset:3072\n"
      "s_waitcnt lgkmcnt(15)\n"
      "v_mfma_f32_32x32x16_bf16 v[160:175], v[64:67], a[192:195], v[160:175]\n"
      "ds_read_b64_tr_b16 a[224:225], v2 offset:35840\n"
      "ds_read_b64_tr_b16 a[226:227], v2 offset:36064\n"
      "s_waitcnt vmcnt(5)\n"
      "ds_write_b128 v13, a[248:251] offset:4096\n"
      "v_mfma_f32_32x32x16_bf16 v[160:175], v[68:71], a[196:199], v[160:175]\n"
      "ds_read_b64_tr_b16 a[228:229], v2 offset:36736\n"
      "ds_read_b64_tr_b16 a[230:231], v2 offset:36960\n"
      "s_waitcnt vmcnt(4)\n"
      "ds_write_b128 v13, a[252:255] offset:5120\n"
      "s_waitcnt lgkmcnt(15)\n"
      "v_mfma_f32_32x32x16_bf16 v[160:175], v[72:75], a[200:203], v[160:175]\n"
      "ds_read_b64_tr_b16 a[232:233], v2 offset:37632\n"
      "ds_read_b64_tr_b16 a[234:235], v2 offset:37856\n"
      "s_add_u32 s36, s50, 50176\n"
      "s_add_u32 s37, s36, 4096\n"
      "buffer_load_dwordx4 a[0:3], v13, s[4:7], s36 offen sc1\n"
      "v_mfma_f32_32x32x16_bf16 v[160:175], v[76:79], a[204:207], v[160:175]\n"
      "ds_read_b64_tr_b16 a[236:237], v2 offset:38528\n"
      "ds_read_b64_tr_b16 a[238:239], v2 offset:38752\n"
      "s_waitcnt vmcnt(1)\n"
      "v_mfma_f32_32x32x16_bf16 v[160:175], v[80:83], a[208:211], v[160:175]\n"
      "ds_read_b64_tr_b16 a[240:241], v2 offset:39424\n"
      "ds_read_b64_tr_b16 a[242:243], v2 offset:39648\n"
      "buffer_load_dwordx4 a[4:7], v13, s[4:7], s36 offen offset:1024 sc1\n"
      "v_add_f32 v224, v224, v228\n"
      "v_add_f32 v226, v226, v230\n"
      "s_waitcnt lgkmcnt(15)\n"
      "v_mfma_f32_32x32x16_bf16 v[160:175], v[84:87], a[212:215], v[160:175]\n"
      "ds_read_b64_tr_b16 a[244:245], v2 offset:40320\n"
      "ds_read_b64_tr_b16 a[246:247], v2 offset:40544\n"
      "v_add_f32 v227, v227, v231\n"
      "v_add_f32 v232, v232, v236\n"
      "v_add_f32 v234, v234, v238\n"
      "v_mfma_f32_32x32x16_bf16 v[160:175], v[88:91], a[216:219], v[160:175]\n"
      "ds_read_b64_tr_b16 a[248:249], v2 offset:41216\n"
      "ds_read_b64_tr_b16 a[250:251], v2 offset:41440\n"
      "buffer_load_dwordx4 a[8:11], v13, s[4:7], s36 offen offset:2048 sc1\n"
      "v_add_f32 v235, v235, v239\n"
      "s_nop 0\n"
      "s_waitcnt lgkmcnt(14)\n"
      "v_mfma_f32_32x32x16_bf16 v[160:175], v[92:95], a[220:223], v[160:175]\n"
      "ds_read_b64_tr_b16 a[252:253], v2 offset:42112\n"
      "ds_read_b64_tr_b16 a[254:255], v2 offset:42336\n"
      "v_permlane32_swap_b32 v224, v232\n"
      "v_permlane32_swap_b32 v226, v234\n"
      "v_permlane32_swap_b32 v227, v235\n"
      "v_mfma_f32_32x32x16_bf16 v[160:175], v[96:99], a[224:227], v[160:175]\n"
      "ds_read_b64_tr_b16 a[176:177], v2 offset:43008\n"
      "ds_read_b64_tr_b16 a[178:179], v2 offset:43232\n"
      "buffer_load_dwordx4 a[12:15], v13, s[4:7], s36 offen offset:3072 sc1\n"
      "v_add_f32 v224, v224, v232\n"
      "v_add_f32 v226, v226, v234\n"
      "s_waitcnt lgkmcnt(12)\n"
      "v_mfma_f32_32x32x16_bf16 v[160:175], v[100:103], a[228:231], v[160:175]\n"
      "ds_read_b64_tr_b16 a[180:181], v2 offset:43904\n"
      "ds_read_b64_tr_b16 a[182:183], v2 offset:44128\n"
      "v_add_f32 v227, v227, v235\n"
      "ds_write_b128 v26, v[224:227] offset:16384\n"
      "v_mfma_f32_32x32x16_bf16 v[160:175], v[104:107], a[232:235], v[160:175]\n"
      "ds_read_b64_tr_b16 a[184:185], v2 offset:44800\n"
      "ds_read_b64_tr_b16 a[186:187], v2 offset:45024\n"
      "buffer_load_dwordx4 a[16:19], v13, s[4:7], s37 offen sc1\n"
      "s_waitcnt lgkmcnt(13)\n"
      "v_mfma_f32_32x32x16_bf16 v[160:175], v[108:111], a[236:239], v[160:175]\n"
      "ds_read_b64_tr_b16 a[188:189], v2 offset:45696\n"
      "ds_read_b64_tr_b16 a[190:191], v2 offset:45920\n"
      "v_mfma_f32_32x32x16_bf16 v[160:175], v[112:115], a[240:243], v[160:175]\n"
      "ds_read_b64_tr_b16 a[192:193], v2 offset:46592\n"
      "ds_read_b64_tr_b16 a[194:195], v2 offset:46816\n"
      "buffer_load_dwordx4 a[20:23], v13, s[4:7], s37 offen offset:1024 sc1\n"
      "s_waitcnt lgkmcnt(13)\n"
      "v_mfma_f32_32x32x16_bf16 v[160:175], v[116:119], a[244:247], v[160:175]\n"
      "ds_read_b64_tr_b16 a[196:197], v2 offset:47488\n"
      "ds_read_b64_tr_b16 a[198:199], v2 offset:47712\n"
      "v_mfma_f32_32x32x16_bf16 v[160:175], v[120:123], a[248:251], v[160:175]\n"
      "ds_read_b64_tr_b16 a[200:201], v2 offset:48384\n"
      "ds_read_b64_tr_b16 a[202:203], v2 offset:48608\n"
      "s_add_u32 s36, s50, 75264\n"
      "buffer_load_dwordx4 a[52:55], v14, s[4:7], s36 offen sc1\n"
      "s_waitcnt lgkmcnt(13)\n"
      "v_mfma_f32_32x32x16_bf16 v[160:175], v[124:127], a[252:255], v[160:175]\n"
      "ds_read_b64_tr_b16 a[204:205], v2 offset:49280\n"
      "ds_read_b64_tr_b16 a[206:207], v2 offset:49504\n"
      "s_waitcnt lgkmcnt(0)\n"
      "s_barrier\n"
      "v_mfma_f32_32x32x16_bf16 v[160:175], v[128:131], a[176:179], v[160:175]\n"
      "ds_read_b64_tr_b16 a[208:209], v2 offset:0\n"
      "ds_read_b64_tr_b16 a[210:211], v2 offset:224\n"
      "s_add_u32 s36, s50, 75264\n"
      "s_add_u32 s37, s36, 4096\n"
      "buffer_load_dwordx4 a[112:115], v13, s[4:7], s36 offen sc1\n"
      "v_add_u32 v222, 141824, v0\n"
      "ds_read_b128 v[224:227], v222 offset:0\n"
      "v_mfma_f32_32x32x16_bf16 v[160:175], v[132:135], a[180:183], v[160:175]\n"
      "ds_read_b64_tr_b16 a[212:213], v2 offset:896\n"
      "ds_read_b64_tr_b16 a[214:215], v2 offset:1120\n"
      "s_mov_b64 exec, 0x00ff0000\n"
      "ds_write_b128 v14, a[24:27] offset:50176\n"
      "s_mov_b64 exec, -1\n"
      "ds_read_b128 v[228:231], v222 offset:1024\n"
      "ds_read_b128 v[232:235], v222 offset:2048\n"
      "v_mfma_f32_32x32x16_bf16 v[160:175], v[136:139], a[184:187], v[160:175]\n"
      "ds_read_b64_tr_b16 a[216:217], v2 offset:1792\n"
      "ds_read_b64_tr_b16 a[218:219], v2 offset:2016\n"
      "buffer_load_dwordx4 a[116:119], v13, s[4:7], s36 offen offset:1024 sc1\n"
      "ds_read_b128 v[236:239], v222 offset:3072\n"
      "v_mfma_f32_32x32x16_bf16 v[160:175], v[140:143], a[188:191], v[160:175]\n"
      "ds_read_b64_tr_b16 a[220:221], v2 offset:2688\n"
      "ds_read_b64_tr_b16 a[222:223], v2 offset:2912\n"
      "v_add_u32 v223, 100352, v13\n"
      "s_waitcnt vmcnt(8)\n"
      "ds_write_b128 v223, a[0:3] offset:0\n"
      "v_mfma_f32_32x32x16_bf16 v[160:175], v[144:147], a[192:195], v[160:175]\n"
      "ds_read_b64_tr_b16 a[224:225], v2 offset:3584\n"
      "ds_read_b64_tr_b16 a[226:227], v2 offset:3808\n"
      "buffer_load_dwordx4 a[120:123], v13, s[4:7], s36 offen offset:2048 sc1\n"
      "s_waitcnt lgkmcnt(5)\n"
      "v_mfma_f32_32x32x16_bf16 v[160:175], v[148:151], a[196:199], v[160:175]\n"
      "ds_read_b64_tr_b16 a[228:229], v2 offset:4480\n"
      "ds_read_b64_tr_b16 a[230:231], v2 offset:4704\n"
      "s_waitcnt vmcnt(8)\n"
      "ds_write_b128 v223, a[4:7] offset:1024\n"
      "v_add_f32 v224, v224, v228\n"
      "v_add_f32 v232, v232, v236\n"
      "v_mfma_f32_32x32x16_bf16 v[160:175], v[152:155], a[200:203], v[160:175]\n"
      "ds_read_b64_tr_b16 a[232:233], v2 offset:5376\n"
      "ds_read_b64_tr_b16 a[234:235], v2 offset:5600\n"
      "buffer_load_dwordx4 a[124:127], v13, s[4:7], s36 offen offset:3072 sc1\n"
      "v_add_f32 v224, v224, v232\n"
      "v_add_f32 v226, v226, v230\n"
      "v_mfma_f32_32x32x16_bf16 v[160:175], v[156:159], a[204:207], v[160:175]\n"
      "ds_read_b64_tr_b16 a[236:237], v2 offset:6272\n"
      "ds_read_b64_tr_b16 a[238:239], v2 offset:6496\n"
      "s_waitcnt vmcnt(8)\n"
      "ds_write_b128 v223, a[8:11] offset:2048\n"
      "v_add_f32 v234, v234, v238\n"
      "v_add_f32 v226, v226, v234\n"
      "v_mfma_f32_32x32x16_bf16 v[176:191], v[48:51], a[208:211], 0\n"
      "ds_read_b64_tr_b16 a[240:241], v2 offset:7168\n"
      "ds_read_b64_tr_b16 a[242:243], v2 offset:7392\n"
      "buffer_load_dwordx4 a[128:131], v13, s[4:7], s37 offen sc1\n"
      "v_add_f32 v227, v227, v231\n"
      "v_add_f32 v235, v235, v239\n"
      "v_mfma_f32_32x32x16_bf16 v[176:191], v[52:55], a[212:215], v[176:191]\n"
      "ds_read_b64_tr_b16 a[244:245], v2 offset:8064\n"
      "ds_read_b64_tr_b16 a[246:247], v2 offset:8288\n"
      "s_waitcnt vmcnt(8)\n"
      "ds_write_b128 v223, a[12:15] offset:3072\n"
      "v_add_f32 v227, v227, v235\n"
      "v_cvt_pk_bf16_f32 v228, v224, v224\n"
      "v_mfma_f32_32x32x16_bf16 v[176:191], v[56:59], a[216:219], v[176:191]\n"
      "ds_read_b64_tr_b16 a[248:249], v2 offset:8960\n"
      "ds_read_b64_tr_b16 a[250:251], v2 offset:9184\n"
      "buffer_load_dwordx4 a[132:135], v13, s[4:7], s37 offen offset:1024 sc1\n"
      "v_and_b32 v229, 0xffff0000, v228\n"
      "v_sub_f32 v229, v224, v229\n"
      "s_waitcnt lgkmcnt(15)\n"
      "v_mfma_f32_32x32x16_bf16 v[176:191], v[60:63], a[220:223], v[176:191]\n"
      "ds_read_b64_tr_b16 a[252:253], v2 offset:9856\n"
      "ds_read_b64_tr_b16 a[254:255], v2 offset:10080\n"
      "s_waitcnt vmcnt(8)\n"
      "ds_write_b128 v223, a[16:19] offset:4096\n"
      "v_cvt_pk_bf16_f32 v229, v229, v229\n"
      "s_nop 1\n"
      "v_mfma_f32_32x32x16_bf16 v[176:191], v[64:67], a[224:227], v[176:191]\n"
      "ds_read_b64_tr_b16 a[176:177], v2 offset:10752\n"
      "ds_read_b64_tr_b16 a[178:179], v2 offset:10976\n"
      "s_add_u32 s36, s50, 100352\n"
      "s_add_u32 s37, s36, 4096\n"
      "buffer_load_dwordx4 a[136:139], v13, s[4:7], s36 offen sc1\n"
      "v_permlane32_swap_b32 v228, v229\n"
      "v_accvgpr_write_b32 a168, v228\n"
      "s_waitcnt lgkmcnt(15)\n"
      "v_mfma_f32_32x32x16_bf16 v[176:191], v[68:71], a[228:231], v[176:191]\n"
      "ds_read_b64_tr_b16 a[180:181], v2 offset:11648\n"
      "ds_read_b64_tr_b16 a[182:183], v2 offset:11872\n"
      "s_waitcnt vmcnt(8)\n"
      "ds_write_b128 v223, a[20:23] offset:5120\n"
      "v_accvgpr_write_b32 a172, v229\n"
      "v_cvt_pk_bf16_f32 v228, v226, v226\n"
      "v_mfma_f32_32x32x16_bf16 v[176:191], v[72:75], a[232:235], v[176:191]\n"
      "ds_read_b64_tr_b16 a[184:185], v2 offset:12544\n"
      "ds_read_b64_tr_b16 a[186:187], v2 offset:12768\n"
      "buffer_load_dwordx4 a[140:143], v13, s[4:7], s36 offen offset:1024 sc1\n"
      "v_and_b32 v229, 0xffff0000, v228\n"
      "v_sub_f32 v229, v226, v229\n"
      "s_waitcnt lgkmcnt(15)\n"
      "v_mfma_f32_32x32x16_bf16 v[176:191], v[76:79], a[236:239], v[176:191]\n"
      "ds_read_b64_tr_b16 a[188:189], v2 offset:13440\n"
      "ds_read_b64_tr_b16 a[190:191], v2 offset:13664\n"
      "v_cvt_pk_bf16_f32 v229, v229, v229\n"
      "s_nop 1\n"
      "v_permlane32_swap_b32 v228, v229\n"
      "v_mfma_f32_32x32x16_bf16 v[176:191], v[80:83], a[240:243], v[176:191]\n"
      "ds_read_b64_tr_b16 a[192:193], v2 offset:14336\n"
      "ds_read_b64_tr_b16 a[194:195], v2 offset:14560\n"
      "buffer_load_dwordx4 a[144:147], v13, s[4:7], s36 offen offset:2048 sc1\n"
      "v_accvgpr_write_b32 a170, v228\n"
      "v_accvgpr_write_b32 a174, v229\n"
      "s_waitcnt lgkmcnt(14)\n"
      "v_mfma_f32_32x32x16_bf16 v[176:191], v[84:87], a[244:247], v[176:191]\n"
      "ds_read_b64_tr_b16 a[196:197], v2 offset:15232\n"
      "ds_read_b64_tr_b16 a[198:199], v2 offset:15456\n"
      "v_cvt_pk_bf16_f32 v228, v227, v227\n"
      "v_and_b32 v229, 0xffff0000, v228\n"
      "v_sub_f32 v229, v227, v229\n"
      "v_cvt_pk_bf16_f32 v229, v229, v229\n"
      "s_nop 1\n"
      "v_permlane32_swap_b32 v228, v229\n"
      "v_accvgpr_write_b32 a171, v228\n"
      "v_accvgpr_write_b32 a175, v229\n"
      "s_nop 1\n"
      "v_mfma_f32_32x32x16_bf16 v[160:175], v[210:213], a[168:171], v[160:175]\n"
      "v_mfma_f32_32x32x16_bf16 v[176:191], v[212:215], a[168:171], v[176:191]\n"
      "v_mfma_f32_32x32x16_bf16 v[176:191], v[88:91], a[248:251], v[176:191]\n"
      "ds_read_b64_tr_b16 a[200:201], v2 offset:16128\n"
      "ds_read_b64_tr_b16 a[202:203], v2 offset:16352\n"
      "buffer_load_dwordx4 a[148:151], v13, s[4:7], s36 offen offset:3072 sc1\n"
      "v_fma_f32 v160, v160, v192, v22\n"
      "v_fma_f32 v161, v161, v193, v22\n"
      "s_waitcnt lgkmcnt(13)\n"
      "v_mfma_f32_32x32x16_bf16 v[176:191], v[92:95], a[252:255], v[176:191]\n"
      "ds_read_b64_tr_b16 a[204:205], v2 offset:17024\n"
      "ds_read_b64_tr_b16 a[206:207], v2 offset:17248\n"
      "buffer_load_dwordx4 a[152:155], v13, s[4:7], s37 offen sc1\n"
      "v_fma_f32 v162, v162, v194, v22\n"
      "v_fma_f32 v163, v163, v195, v22\n"
      "v_mfma_f32_32x32x16_bf16 v[176:191], v[96:99], a[176:179], v[176:191]\n"
      "ds_read_b64_tr_b16 a[208:209], v2 offset:17920\n"
      "ds_read_b64_tr_b16 a[210:211], v2 offset:18144\n"
      "buffer_load_dwordx4 a[156:159], v13, s[4:7], s37 offen offset:1024 sc1\n"
      "v_fma_f32 v164, v164, v196, v22\n"
      "v_fma_f32 v165, v165, v197, v22\n"
      "s_waitcnt lgkmcnt(12)\n"
      "v_mfma_f32_32x32x16_bf16 v[176:191], v[100:103], a[180:183], v[176:191]\n"
      "ds_read_b64_tr_b16 a[212:213], v2 offset:18816\n"
      "ds_read_b64_tr_b16 a[214:215], v2 offset:19040\n"
      "s_add_u32 s36, s50, 125440\n"
      "s_add_u32 s37, s36, 4096\n"
      "buffer_load_dwordx4 a[28:31], v13, s[4:7], s36 offen sc1\n"
      "v_fma_f32 v166, v166, v198, v22\n"
      "v_fma_f32 v167, v167, v199, v22\n"
      "v_mfma_f32_32x32x16_bf16 v[176:191], v[104:107], a[184:187], v[176:191]\n"
      "ds_read_b64_tr_b16 a[216:217], v2 offset:19712\n"
      "ds_read_b64_tr_b16 a[218:219], v2 offset:19936\n"
      "buffer_load_dwordx4 a[32:35], v13, s[4:7], s36 offen offset:1024 sc1\n"
      "v_fma_f32 v168, v168, v200, v22\n"
      "v_fma_f32 v169, v169, v201, v22\n"
      "s_waitcnt lgkmcnt(12)\n"
      "v_mfma_f32_32x32x16_bf16 v[176:191], v[108:111], a[188:191], v[176:191]\n"
      "ds_read_b64_tr_b16 a[220:221], v2 offset:20608\n"
      "ds_read_b64_tr_b16 a[222:223], v2 offset:20832\n"
      "buffer_load_dwordx4 a[36:39], v13, s[4:7], s36 offen offset:2048 sc1\n"
      "v_fma_f32 v170, v170, v202, v22\n"
      "v_fma_f32 v171, v171, v203, v22\n"
      "v_mfma_f32_32x32x16_bf16 v[176:191], v[112:115], a[192:195], v[176:191]\n"
      "ds_read_b64_tr_b16 a[224:225], v2 offset:21504\n"
      "ds_read_b64_tr_b16 a[226:227], v2 offset:21728\n"
      "buffer_load_dwordx4 a[40:43], v13, s[4:7], s36 offen offset:3072 sc1\n"
      "v_fma_f32 v172, v172, v204, v22\n"
      "v_fma_f32 v173, v173, v205, v22\n"
      "s_waitcnt lgkmcnt(12)\n"
      "v_mfma_f32_32x32x16_bf16 v[176:191], v[116:119], a[196:199], v[176:191]\n"
      "ds_read_b64_tr_b16 a[228:229], v2 offset:22400\n"
      "ds_read_b64_tr_b16 a[230:231], v2 offset:22624\n"
      "buffer_load_dwordx4 a[44:47], v13, s[4:7], s37 offen sc1\n"
      "v_fma_f32 v174, v174, v206, v22\n"
      "v_fma_f32 v175, v175, v207, v22\n"
      "v_mfma_f32_32x32x16_bf16 v[176:191], v[120:123], a[200:203], v[176:191]\n"
      "ds_read_b64_tr_b16 a[232:233], v2 offset:23296\n"
      "ds_read_b64_tr_b16 a[234:235], v2 offset:23520\n"
      "buffer_load_dwordx4 a[48:51], v13, s[4:7], s37 offen offset:1024 sc1\n"
      "v_mul_f32 v224, s63, v160\n"
      "v_mul_f32 v225, s63, v161\n"
      "s_waitcnt lgkmcnt(12)\n"
      "v_mfma_f32_32x32x16_bf16 v[176:191], v[124:127], a[204:207], v[176:191]\n"
      "ds_read_b64_tr_b16 a[236:237], v2 offset:24192\n"
      "ds_read_b64_tr_b16 a[238:239], v2 offset:24416\n"
      "buffer_store_dwordx4 v[248:251], v0, s[4:7], s45 offen offset:0 sc1\n"
      "v_mul_f32 v226, s63, v162\n"
      "v_mul_f32 v227, s63, v163\n"
      "v_mfma_f32_32x32x16_bf16 v[176:191], v[128:131], a[208:211], v[176:191]\n"
      "buffer_store_dwordx4 v[248:251], v0, s[4:7], s45 offen offset:1024 sc1\n"
      "v_mul_f32 v228, s63, v164\n"
      "v_mul_f32 v229, s63, v165\n"
      "v_mul_f32 v230, s63, v166\n"
      "v_mul_f32 v231, s63, v167\n"
      "s_waitcnt lgkmcnt(10)\n"
      "v_mfma_f32_32x32x16_bf16 v[176:191], v[132:135], a[212:215], v[176:191]\n"
      "buffer_store_dwordx4 v[248:251], v3, s[4:7], s43 offen offset:0 sc1\n"
      "v_mul_f32 v232, s63, v168\n"
      "v_mul_f32 v233, s63, v169\n"
      "v_mul_f32 v234, s63, v170\n"
      "v_mul_f32 v235, s63, v171\n"
      "v_mfma_f32_32x32x16_bf16 v[176:191], v[136:139], a[216:219], v[176:191]\n"
      "buffer_store_dwordx4 v[248:251], v3, s[4:7], s43 offen offset:1024 sc1\n"
      "v_mul_f32 v236, s63, v172\n"
      "v_mul_f32 v237, s63, v173\n"
      "v_mul_f32 v238, s63, v174\n"
      "v_mul_f32 v239, s63, v175\n"
      "s_waitcnt lgkmcnt(6)\n"
      "v_mfma_f32_32x32x16_bf16 v[176:191], v[140:143], a[220:223], v[176:191]\n"
      "buffer_store_dword v248, v9, s[4:7], s47 offen offset:0 sc1\n"
      "v_exp_f32_e64 v224, v224 clamp\n"
      "v_exp_f32_e64 v225, v225 clamp\n"
      "v_exp_f32_e64 v226, v226 clamp\n"
      "v_exp_f32_e64 v227, v227 clamp\n"
      "v_mfma_f32_32x32x16_bf16 v[176:191], v[144:147], a[224:227], v[176:191]\n"
      "s_add_u32 s35, s48, s79\n"
      "v_exp_f32_e64 v228, v228 clamp\n"
      "v_exp_f32_e64 v229, v229 clamp\n"
      "v_exp_f32_e64 v230, v230 clamp\n"
      "v_exp_f32_e64 v231, v231 clamp\n"
      "s_waitcnt lgkmcnt(2)\n"
      "v_mfma_f32_32x32x16_bf16 v[176:191], v[148:151], a[228:231], v[176:191]\n"
      "buffer_store_dwordx4 v[248:251], v0, s[4:7], s35 offen offset:0 sc1\n"
      "v_exp_f32_e64 v232, v232 clamp\n"
      "v_exp_f32_e64 v233, v233 clamp\n"
      "v_exp_f32_e64 v234, v234 clamp\n"
      "v_exp_f32_e64 v235, v235 clamp\n"
      "v_mfma_f32_32x32x16_bf16 v[176:191], v[152:155], a[232:235], v[176:191]\n"
      "buffer_store_dwordx4 v[248:251], v0, s[4:7], s35 offen offset:1024 sc1\n"
      "v_exp_f32_e64 v236, v236 clamp\n"
      "v_exp_f32_e64 v237, v237 clamp\n"
      "v_exp_f32_e64 v238, v238 clamp\n"
      "v_exp_f32_e64 v239, v239 clamp\n"
      "s_waitcnt lgkmcnt(0)\n"
      "v_mfma_f32_32x32x16_bf16 v[176:191], v[156:159], a[236:239], v[176:191]\n"
      "buffer_store_dwordx4 v[248:251], v0, s[4:7], s35 offen offset:2048 sc1\n"
      "v_max_f32 v160, v22, v160\n"
      "v_max_f32 v161, v22, v161\n"
      "v_max_f32 v162, v22, v162\n"
      "v_max_f32 v163, v22, v163\n"
      "v_max_f32 v164, v22, v164\n"
      "v_max_f32 v165, v22, v165\n"
      "v_max_f32 v166, v22, v166\n"
      "v_max_f32 v167, v22, v167\n"
      "v_max_f32 v168, v22, v168\n"
      "v_max_f32 v169, v22, v169\n"
      "v_max_f32 v170, v22, v170\n"
      "v_max_f32 v171, v22, v171\n"
      "buffer_store_dwordx4 v[248:251], v0, s[4:7], s35 offen offset:3072 sc1\n"
      "v_max_f32 v172, v22, v172\n"
      "v_max_f32 v173, v22, v173\n"
      "v_max_f32 v174, v22, v174\n"
      "v_max_f32 v175, v22, v175\n"
      "v_pk_add_f32 v[160:161], v[160:161], v[224:225]\n"
      "v_pk_add_f32 v[162:163], v[162:163], v[226:227]\n"
      "v_pk_add_f32 v[164:165], v[164:165], v[228:229]\n"
      "v_pk_add_f32 v[166:167], v[166:167], v[230:231]\n"
      "v_pk_add_f32 v[168:169], v[168:169], v[232:233]\n"
      "v_pk_add_f32 v[170:171], v[170:171], v[234:235]\n"
      "v_pk_add_f32 v[172:173], v[172:173], v[236:237]\n"
      "v_pk_add_f32 v[174:175], v[174:175], v[238:239]\n"
      "v_cvt_pk_bf16_f32 v240, v160, v161\n"
      "v_cvt_pk_bf16_f32 v241, v162, v163\n"
      "v_cvt_pk_bf16_f32 v242, v164, v165\n"
      "v_cvt_pk_bf16_f32 v243, v166, v167\n"
      "v_cvt_pk_bf16_f32 v244, v168, v169\n"
      "v_cvt_pk_bf16_f32 v245, v170, v171\n"
      "v_cvt_pk_bf16_f32 v246, v172, v173\n"
      "v_cvt_pk_bf16_f32 v247, v174, v175\n"
      "s_waitcnt lgkmcnt(0)\n"
      "s_nop 7\n"
      "s_branch L_A_EPI\n"
      "; ================ linear attention layer ================\n"
      "L_LOOP:\n"
      "s_add_u32 s52, s42, 0\n"
      "s_add_u32 s53, s42, 4096\n"
      "s_add_u32 s54, s42, 8192\n"
      "s_add_u32 s55, s42, 12288\n"
      "s_add_u32 s56, s42, 16384\n"
      "s_add_u32 s57, s42, 20480\n"
      "s_add_u32 s58, s42, 24576\n"
      "s_add_u32 s42, s42, 1835008\n"
      "s_mov_b32 s1, 0\n"
      "L_XA_RE:\n"
      "ds_read_b64_tr_b16 a[176:177], v2 offset:25088\n"
      "ds_read_b64_tr_b16 a[178:179], v2 offset:25312\n"
      "ds_read_b64_tr_b16 a[180:181], v2 offset:25984\n"
      "ds_read_b64_tr_b16 a[182:183], v2 offset:26208\n"
      "ds_read_b64_tr_b16 a[184:185], v2 offset:26880\n"
      "ds_read_b64_tr_b16 a[186:187], v2 offset:27104\n"
      "ds_read_b64_tr_b16 a[188:189], v2 offset:27776\n"
      "ds_read_b64_tr_b16 a[190:191], v2 offset:28000\n"
      "ds_read_b64_tr_b16 a[192:193], v2 offset:28672\n"
      "ds_read_b64_tr_b16 a[194:195], v2 offset:28896\n"
      "ds_read_b64_tr_b16 a[196:197], v2 offset:29568\n"
      "ds_read_b64_tr_b16 a[198:199], v2 offset:29792\n"
      "ds_read_b64_tr_b16 a[200:201], v2 offset:30464\n"
      "ds_read_b64_tr_b16 a[202:203], v2 offset:30688\n"
      "ds_read_b64_tr_b16 a[204:205], v2 offset:31360\n"
      "ds_read_b64_tr_b16 a[206:207], v2 offset:31584\n"
      "ds_read_b64_tr_b16 a[208:209], v2 offset:32256\n"
      "ds_read_b64_tr_b16 a[210:211], v2 offset:32480\n"
      "ds_read_b64_tr_b16 a[212:213], v2 offset:0\n"
      "ds_read_b64_tr_b16 a[214:215], v2 offset:224\n"
      "ds_read_b64_tr_b16 a[216:217], v2 offset:33152\n"
      "ds_read_b64_tr_b16 a[218:219], v2 offset:33376\n"
      "ds_read_b64_tr_b16 a[220:221], v2 offset:896\n"
      "ds_read_b64_tr_b16 a[222:223], v2 offset:1120\n"
      "ds_read_b64_tr_b16 a[224:225], v2 offset:34048\n"
      "ds_read_b64_tr_b16 a[226:227], v2 offset:34272\n"
      "ds_read_b64_tr_b16 a[228:229], v2 offset:1792\n"
      "ds_read_b64_tr_b16 a[230:231], v2 offset:2016\n"
      "ds_read_b64_tr_b16 a[232:233], v2 offset:34944\n"
      "ds_read_b64_tr_b16 a[234:235], v2 offset:35168\n"
      "ds_read_b64_tr_b16 a[236:237], v2 offset:2688\n"
      "ds_read_b64_tr_b16 a[238:239], v2 offset:2912\n"
      "ds_read_b64_tr_b16 a[240:241], v2 offset:35840\n"
      "ds_read_b64_tr_b16 a[242:243], v2 offset:36064\n"
      "ds_read_b64_tr_b16 a[244:245], v2 offset:3584\n"
      "ds_read_b64_tr_b16 a[246:247], v2 offset:3808\n"
      "s_cmp_lg_u32 s1, 0\n"
      "s_waitcnt lgkmcnt(0)\n"
      "s_cbranch_scc1 L_XA_GO\n"
      "; hand-off: wave 3 polls its flags, barrier\n"
      "s_add_u32 s34, s16, 1\n"
      "s_cmp_lg_u32 s14, 3\n"
      "s_cbranch_scc1 L_pd_6\n"
      "s_mov_b32 s51, 0x40000\n"
      "L_poll_5:\n"
      "s_mov_b64 exec, 0xffff\n"
      "v_mov_b64 v[28:29], 0\n"
      "buffer_atomic_or_x2 v[28:29], v7, s[4:7], s82 offen sc0\n"
      "s_sub_u32 s51, s51, 1\n"
      "s_cbranch_scc1 L_pd_6\n"
      "L_pc_7:\n"
      "s_waitcnt vmcnt(0)\n"
      "v_subrev_u32 v28, s34, v28\n"
      "v_cmp_eq_u32 s[40:41], s17, v29\n"
      "v_cmp_gt_u32 vcc, 64, v28\n"
      "s_and_b64 vcc, vcc, s[40:41]\n"
      "s_cmp_eq_u64 vcc, exec\n"
      "s_cbranch_scc0 L_poll_5\n"
      "L_pd_6:\n"
      "s_mov_b64 exec, -1\n"
      "s_waitcnt vmcnt(0) lgkmcnt(0)\n"
      "s_barrier\n"
      "L_XA_GO:\n"
      "; first requests, paced by the LDS stores of the Wq image so that the four waves interleave\n"
      "buffer_load_dwordx4 v[48:51], v0, s[4:7], s52 offen sc1\n"
      "buffer_load_dwordx4 v[52:55], v0, s[4:7], s52 offen offset:1024 sc1\n"
      "v_add_u32 v223, 100352, v13\n"
      "ds_write_b128 v223, a[0:3] offset:0\n"
      "buffer_load_dwordx4 v[56:59], v0, s[4:7], s52 offen offset:2048 sc1\n"
      "buffer_load_dwordx4 v[60:63], v0, s[4:7], s52 offen offset:3072 sc1\n"
      "ds_write_b128 v223, a[4:7] offset:1024\n"
      "buffer_load_dwordx4 v[64:67], v0, s[4:7], s53 offen sc1\n"
      "buffer_load_dwordx4 v[68:71], v0, s[4:7], s53 offen offset:1024 sc1\n"
      "ds_write_b128 v223, a[8:11] offset:2048\n"
      "buffer_load_dwordx4 v[72:75], v0, s[4:7], s53 offen offset:2048 sc1\n"
      "buffer_load_dwordx4 v[76:79], v0, s[4:7], s53 offen offset:3072 sc1\n"
      "ds_write_b128 v223, a[12:15] offset:3072\n"
      "buffer_load_dwordx4 v[80:83], v0, s[4:7], s54 offen sc1\n"
      "buffer_load_dwordx4 v[84:87], v0, s[4:7], s54 offen offset:1024 sc1\n"
      "ds_write_b128 v223, a[16:19] offset:4096\n"
      "buffer_load_dwordx4 v[88:91], v0, s[4:7], s54 offen offset:2048 sc1\n"
      "buffer_load_dwordx4 v[92:95], v0, s[4:7], s54 offen offset:3072 sc1\n"
      "ds_write_b128 v223, a[20:23] offset:5120\n"
      "s_mov_b64 exec, 0x00ff0000\n"
      "ds_write_b128 v14, a[24:27] offset:50176\n"
      "s_mov_b64 exec, -1\n"
      "s_waitcnt vmcnt(11)\n"
      "v_mfma_f32_32x32x16_bf16 v[160:175], v[48:51], a[176:179], 0\n"
      "ds_read_b64_tr_b16 a[248:249], v2 offset:36736\n"
      "ds_read_b64_tr_b16 a[250:251], v2 offset:36960\n"
      "buffer_load_dwordx4 v[96:99], v0, s[4:7], s55 offen sc1\n"
      "s_waitcnt vmcnt(11)\n"
      "v_mfma_f32_32x32x16_bf16 v[160:175], v[52:55], a[180:183], v[160:175]\n"
      "ds_read_b64_tr_b16 a[252:253], v2 offset:4480\n"
      "ds_read_b64_tr_b16 a[254:255], v2 offset:4704\n"
      "buffer_load_dwordx4 v[100:103], v0, s[4:7], s55 offen offset:1024 sc1\n"
      "s_waitcnt vmcnt(11)\n"
      "v_mfma_f32_32x32x16_bf16 v[160:175], v[56:59], a[184:187], v[160:175]\n"
      "ds_read_b64_tr_b16 a[176:177], v2 offset:37632\n"
      "ds_read_b64_tr_b16 a[178:179], v2 offset:37856\n"
      "buffer_load_dwordx4 v[104:107], v0, s[4:7], s55 offen offset:2048 sc1\n"
      "s_waitcnt vmcnt(11)\n"
      "v_mfma_f32_32x32x16_bf16 v[160:175], v[60:63], a[188:191], v[160:175]\n"
      "ds_read_b64_tr_b16 a[180:181], v2 offset:5376\n"
      "ds_read_b64_tr_b16 a[182:183], v2 offset:5600\n"
      "buffer_load_dwordx4 v[108:111], v0, s[4:7], s55 offen offset:3072 sc1\n"
      "s_waitcnt vmcnt(11)\n"
      "v_mfma_f32_32x32x16_bf16 v[160:175], v[64:67], a[192:195], v[160:175]\n"
      "ds_read_b64_tr_b16 a[184:185], v2 offset:38528\n"
      "ds_read_b64_tr_b16 a[186:187], v2 offset:38752\n"
      "buffer_load_dwordx4 v[240:243], v0, s[4:7], s46 offen sc1\n"
      "s_waitcnt vmcnt(11)\n"
      "v_mfma_f32_32x32x16_bf16 v[160:175], v[68:71], a[196:199], v[160:175]\n"
      "ds_read_b64_tr_b16 a[188:189], v2 offset:6272\n"
      "ds_read_b64_tr_b16 a[190:191], v2 offset:6496\n"
      "buffer_load_dwordx4 v[244:247], v0, s[4:7], s46 offen offset:1024 sc1\n"
      "s_waitcnt vmcnt(11)\n"
      "v_mfma_f32_32x32x16_bf16 v[160:175], v[72:75], a[200:203], v[160:175]\n"
      "ds_read_b64_tr_b16 a[192:193], v2 offset:39424\n"
      "ds_read_b64_tr_b16 a[194:195], v2 offset:39648\n"
      "buffer_load_dwordx4 v[248:251], v0, s[4:7], s46 offen offset:2048 sc1\n"
      "s_waitcnt vmcnt(11)\n"
      "v_mfma_f32_32x32x16_bf16 v[160:175], v[76:79], a[204:207], v[160:175]\n"
      "ds_read_b64_tr_b16 a[196:197], v2 offset:7168\n"
      "ds_read_b64_tr_b16 a[198:199], v2 offset:7392\n"
      "buffer_load_dwordx4 v[252:255], v216, s[4:7], s46 offen offset:3072 sc1\n"
      "s_waitcnt vmcnt(11)\n"
      "v_mfma_f32_32x32x16_bf16 v[160:175], v[80:83], a[208:211], v[160:175]\n"
      "ds_read_b64_tr_b16 a[200:201], v2 offset:40320\n"
      "ds_read_b64_tr_b16 a[202:203], v2 offset:40544\n"
      "buffer_load_dwordx4 v[112:115], v0, s[4:7], s56 offen sc1\n"
      "v_mfma_f32_32x32x16_bf16 v[176:191], v[48:51], a[212:215], 0\n"
      "ds_read_b64_tr_b16 a[204:205], v2 offset:8064\n"
      "ds_read_b64_tr_b16 a[206:207], v2 offset:8288\n"
      "s_waitcnt vmcnt(11)\n"
      "v_mfma_f32_32x32x16_bf16 v[160:175], v[84:87], a[216:219], v[160:175]\n"
      "ds_read_b64_tr_b16 a[208:209], v2 offset:41216\n"
      "ds_read_b64_tr_b16 a[210:211], v2 offset:41440\n"
      "buffer_load_dwordx4 v[116:119], v0, s[4:7], s56 offen offset:1024 sc1\n"
      "v_mfma_f32_32x32x16_bf16 v[176:191], v[52:55], a[220:223], v[176:191]\n"
      "ds_read_b64_tr_b16 a[212:213], v2 offset:8960\n"
      "ds_read_b64_tr_b16 a[214:215], v2 offset:9184\n"
      "s_waitcnt vmcnt(11)\n"
      "v_mfma_f32_32x32x16_bf16 v[160:175], v[88:91], a[224:227], v[160:175]\n"
      "ds_read_b64_tr_b16 a[216:217], v2 offset:42112\n"
      "ds_read_b64_tr_b16 a[218:219], v2 offset:42336\n"
      "buffer_load_dwordx4 v[120:123], v0, s[4:7], s56 offen offset:2048 sc1\n"
      "v_mfma_f32_32x32x16_bf16 v[176:191], v[56:59], a[228:231], v[176:191]\n"
      "ds_read_b64_tr_b16 a[220:221], v2 offset:9856\n"
      "ds_read_b64_tr_b16 a[222:223], v2 offset:10080\n"
      "s_waitcnt vmcnt(11)\n"
      "v_mfma_f32_32x32x16_bf16 v[160:175], v[92:95], a[232:235], v[160:175]\n"
      "ds_read_b64_tr_b16 a[224:225], v2 offset:43008\n"
      "ds_read_b64_tr_b16 a[226:227], v2 offset:43232\n"
      "buffer_load_dwordx4 v[124:127], v0, s[4:7], s56 offen offset:3072 sc1\n"
      "v_mfma_f32_32x32x16_bf16 v[176:191], v[60:63], a[236:239], v[176:191]\n"
      "ds_read_b64_tr_b16 a[228:229], v2 offset:10752\n"
      "ds_read_b64_tr_b16 a[230:231], v2 offset:10976\n"
      "s_waitcnt vmcnt(11)\n"
      "v_mfma_f32_32x32x16_bf16 v[160:175], v[96:99], a[240:243], v[160:175]\n"
      "ds_read_b64_tr_b16 a[232:233], v2 offset:43904\n"
      "ds_read_b64_tr_b16 a[234:235], v2 offset:44128\n"
      "buffer_load_dwordx4 v[128:131], v0, s[4:7], s57 offen sc1\n"
      "v_mfma_f32_32x32x16_bf16 v[176:191], v[64:67], a[244:247], v[176:191]\n"
      "ds_read_b64_tr_b16 a[236:237], v2 offset:11648\n"
      "ds_read_b64_tr_b16 a[238:239], v2 offset:11872\n"
      "s_waitcnt vmcnt(11) lgkmcnt(15)\n"
      "v_mfma_f32_32x32x16_bf16 v[160:175], v[100:103], a[248:251], v[160:175]\n"
      "ds_read_b64_tr_b16 a[240:241], v2 offset:44800\n"
      "ds_read_b64_tr_b16 a[242:243], v2 offset:45024\n"
      "buffer_load_dwordx4 v[132:135], v0, s[4:7], s57 offen offset:1024 sc1\n"
      "v_mfma_f32_32x32x16_bf16 v[176:191], v[68:71], a[252:255], v[176:191]\n"
      "ds_read_b64_tr_b16 a[244:245], v2 offset:12544\n"
      "ds_read_b64_tr_b16 a[246:247], v2 offset:12768\n"
      "s_waitcnt vmcnt(11)\n"
      "v_mfma_f32_32x32x16_bf16 v[160:175], v[104:107], a[176:179], v[160:175]\n"
      "ds_read_b64_tr_b16 a[248:249], v2 offset:45696\n"
      "ds_read_b64_tr_b16 a[250:251], v2 offset:45920\n"
      "buffer_load_dwordx4 v[136:139], v0, s[4:7], s57 offen offset:2048 sc1\n"
      "v_mfma_f32_32x32x16_bf16 v[176:191], v[72:75], a[180:183], v[176:191]\n"
      "ds_read_b64_tr_b16 a[252:253], v2 offset:13440\n"
      "ds_read_b64_tr_b16 a[254:255], v2 offset:13664\n"
      "s_waitcnt vmcnt(11)\n"
      "v_mfma_f32_32x32x16_bf16 v[160:175], v[108:111], a[184:187], v[160:175]\n"
      "ds_read_b64_tr_b16 a[176:177], v2 offset:46592\n"
      "ds_read_b64_tr_b16 a[178:179], v2 offset:46816\n"
      "buffer_load_dwordx4 v[140:143], v0, s[4:7], s57 offen offset:3072 sc1\n"
      "v_mfma_f32_32x32x16_bf16 v[176:191], v[76:79], a[188:191], v[176:191]\n"
      "ds_read_b64_tr_b16 a[180:181], v2 offset:14336\n"
      "ds_read_b64_tr_b16 a[182:183], v2 offset:14560\n"
      "s_waitcnt vmcnt(7)\n"
      "v_mfma_f32_32x32x16_bf16 v[160:175], v[112:115], a[192:195], v[160:175]\n"
      "ds_read_b64_tr_b16 a[184:185], v2 offset:47488\n"
      "ds_read_b64_tr_b16 a[186:187], v2 offset:47712\n"
      "buffer_load_dwordx4 v[144:147], v0, s[4:7], s58 offen sc1\n"
      "v_mfma_f32_32x32x16_bf16 v[176:191], v[80:83], a[196:199], v[176:191]\n"
      "ds_read_b64_tr_b16 a[188:189], v2 offset:15232\n"
      "ds_read_b64_tr_b16 a[190:191], v2 offset:15456\n"
      "s_waitcnt vmcnt(7)\n"
      "v_mfma_f32_32x32x16_bf16 v[160:175], v[116:119], a[200:203], v[160:175]\n"
      "ds_read_b64_tr_b16 a[192:193], v2 offset:48384\n"
      "ds_read_b64_tr_b16 a[194:195], v2 offset:48608\n"
      "buffer_load_dwordx4 v[148:151], v0, s[4:7], s58 offen offset:1024 sc1\n"
      "v_mfma_f32_32x32x16_bf16 v[176:191], v[84:87], a[204:207], v[176:191]\n"
      "ds_read_b64_tr_b16 a[196:197], v2 offset:16128\n"
      "ds_read_b64_tr_b16 a[198:199], v2 offset:16352\n"
      "s_waitcnt vmcnt(7) lgkmcnt(15)\n"
      "v_mfma_f32_32x32x16_bf16 v[160:175], v[120:123], a[208:211], v[160:175]\n"
      "ds_read_b64_tr_b16 a[200:201], v2 offset:49280\n"
      "ds_read_b64_tr_b16 a[202:203], v2 offset:49504\n"
      "buffer_load_dwordx4 v[152:155], v0, s[4:7], s58 offen offset:2048 sc1\n"
      "v_mfma_f32_32x32x16_bf16 v[176:191], v[88:91], a[212:215], v[176:191]\n"
      "ds_read_b64_tr_b16 a[204:205], v2 offset:17024\n"
      "ds_read_b64_tr_b16 a[206:207], v2 offset:17248\n"
      "v_add_f32 v240, v240, v248\n"
      "s_waitcnt vmcnt(7)\n"
      "v_mfma_f32_32x32x16_bf16 v[160:175], v[124:127], a[216:219], v[160:175]\n"
      "ds_read_b64_tr_b16 a[208:209], v2 offset:17920\n"
      "ds_read_b64_tr_b16 a[210:211], v2 offset:18144\n"
      "buffer_load_dwordx4 v[156:159], v0, s[4:7], s58 offen offset:3072 sc1\n"
      "v_mfma_f32_32x32x16_bf16 v[176:191], v[92:95], a[220:223], v[176:191]\n"
      "ds_read_b64_tr_b16 a[212:213], v2 offset:18816\n"
      "ds_read_b64_tr_b16 a[214:215], v2 offset:19040\n"
      "v_add_f32 v241, v241, v249\n"
      "v_add_f32 v242, v242, v250\n"
      "s_waitcnt vmcnt(7)\n"
      "v_mfma_f32_32x32x16_bf16 v[160:175], v[128:131], a[224:227], v[160:175]\n"
      "ds_read_b64_tr_b16 a[216:217], v2 offset:19712\n"
      "ds_read_b64_tr_b16 a[218:219], v2 offset:19936\n"
      "v_add_f32 v243, v243, v251\n"
      "v_add_f32 v244, v244, v252\n"
      "v_mfma_f32_32x32x16_bf16 v[176:191], v[96:99], a[228:231], v[176:191]\n"
      "ds_read_b64_tr_b16 a[220:221], v2 offset:20608\n"
      "ds_read_b64_tr_b16 a[222:223], v2 offset:20832\n"
      "v_add_f32 v245, v245, v253\n"
      "v_add_f32 v246, v246, v254\n"
      "s_waitcnt vmcnt(6)\n"
      "v_mfma_f32_32x32x16_bf16 v[160:175], v[132:135], a[232:235], v[160:175]\n"
      "ds_read_b64_tr_b16 a[224:225], v2 offset:21504\n"
      "ds_read_b64_tr_b16 a[226:227], v2 offset:21728\n"
      "v_add_f32 v247, v247, v255\n"
      "v_add_f32 v240, v240, v244\n"
      "v_mfma_f32_32x32x16_bf16 v[176:191], v[100:103], a[236:239], v[176:191]\n"
      "ds_read_b64_tr_b16 a[228:229], v2 offset:22400\n"
      "ds_read_b64_tr_b16 a[230:231], v2 offset:22624\n"
      "v_add_f32 v241, v241, v245\n"
      "v_add_f32 v242, v242, v246\n"
      "s_waitcnt vmcnt(5)\n"
      "v_mfma_f32_32x32x16_bf16 v[160:175], v[136:139], a[240:243], v[160:175]\n"
      "ds_read_b64_tr_b16 a[232:233], v2 offset:23296\n"
      "ds_read_b64_tr_b16 a[234:235], v2 offset:23520\n"
      "v_add_f32 v243, v243, v247\n"
      "v_mov_b64 v[208:209], 0\n"
      "v_mfma_f32_32x32x16_bf16 v[176:191], v[104:107], a[244:247], v[176:191]\n"
      "ds_read_b64_tr_b16 a[236:237], v2 offset:24192\n"
      "ds_read_b64_tr_b16 a[238:239], v2 offset:24416\n"
      "v_mov_b64 v[212:213], 0\n"
      "v_permlane32_swap_b32 v240, v242\n"
      "s_waitcnt vmcnt(4) lgkmcnt(15)\n"
      "v_mfma_f32_32x32x16_bf16 v[160:175], v[140:143], a[248:251], v[160:175]\n"
      "v_permlane32_swap_b32 v241, v243\n"
      "v_add_f32 v240, v240, v242\n"
      "v_add_f32 v241, v241, v243\n"
      "v_mov_b32 v211, 0\n"
      "v_mfma_f32_32x32x16_bf16 v[176:191], v[108:111], a[252:255], v[176:191]\n"
      "v_mov_b32 v214, 0\n"
      "v_permlane16_swap_b32 v240, v241\n"
      "v_add_f32 v240, v240, v241\n"
      "ds_write_b32 v8, v240\n"
      "s_waitcnt vmcnt(3)\n"
      "v_mfma_f32_32x32x16_bf16 v[160:175], v[144:147], a[176:179], v[160:175]\n"
      "ds_read2_b32 v[240:241], v11 offset0:32 offset1:64\n"
      "v_mfma_f32_32x32x16_bf16 v[176:191], v[112:115], a[180:183], v[176:191]\n"
      "s_waitcnt vmcnt(2)\n"
      "v_mfma_f32_32x32x16_bf16 v[160:175], v[148:151], a[184:187], v[160:175]\n"
      "s_waitcnt lgkmcnt(0)\n"
      "v_mfma_f32_32x32x16_bf16 v[176:191], v[116:119], a[188:191], v[176:191]\n"
      "v_mul_f32 v240, 0xbb124925, v240\n"
      "v_mul_f32 v241, 0x3b124925, v241\n"
      "v_fma_f32 v240, v241, 0, v240\n"
      "v_fma_f32 v241, -v240, v240, v241\n"
      "s_waitcnt vmcnt(1)\n"
      "v_mfma_f32_32x32x16_bf16 v[160:175], v[152:155], a[192:195], v[160:175]\n"
      "v_max_f32 v241, 0, v241\n"
      "v_add_f32 v241, 0x3727c5ac, v241\n"
      "v_rsq_f32 v24, v241\n"
      "v_cvt_pk_bf16_f32 v242, v240, v240\n"
      "v_mfma_f32_32x32x16_bf16 v[176:191], v[120:123], a[196:199], v[176:191]\n"
      "ds_write_b32 v11, v24\n"
      "v_and_b32 v242, 0xffff0000, v242\n"
      "v_sub_f32 v242, v240, v242\n"
      "v_cvt_pk_bf16_f32 v210, v240, v242\n"
      "s_waitcnt vmcnt(0)\n"
      "v_mfma_f32_32x32x16_bf16 v[160:175], v[156:159], a[200:203], v[160:175]\n"
      "v_mov_b32 v215, v210\n"
      "ds_read_b128 v[192:195], v12 offset:0\n"
      "ds_read_b128 v[196:199], v12 offset:32\n"
      "ds_read_b128 v[200:203], v12 offset:64\n"
      "v_mfma_f32_32x32x16_bf16 v[176:191], v[124:127], a[204:207], v[176:191]\n"
      "ds_read_b128 v[204:207], v12 offset:96\n"
      "s_add_u32 s46, s46, 262144\n"
      "; LATE flag: every wave drained its stores in front of the barrier just passed\n"
      "s_cmp_lg_u32 s14, 1\n"
      "s_cbranch_scc1 L_late_8\n"
      "s_add_u32 s34, s16, 1\n"
      "v_mov_b32 v28, s34\n"
      "v_mov_b32 v29, s17\n"
      "buffer_atomic_swap_x2 v[28:29], v6, s[4:7], s80 offen offset:128\n"
      "L_late_8:\n"
      "s_waitcnt lgkmcnt(0)\n"
      "v_mfma_f32_32x32x16_bf16 v[160:175], v[210:213], a[168:171], v[160:175]\n"
      "v_mfma_f32_32x32x16_bf16 v[176:191], v[212:215], a[168:171], v[176:191]\n"
      "; phi(k) beside the last V steps\n"
      "v_mfma_f32_32x32x16_bf16 v[176:191], v[128:131], a[208:211], v[176:191]\n"
      "v_fma_f32 v160, v160, v192, v22\n"
      "v_fma_f32 v161, v161, v193, v22\n"
      "v_fma_f32 v162, v162, v194, v22\n"
      "v_fma_f32 v163, v163, v195, v22\n"
      "v_fma_f32 v164, v164, v196, v22\n"
      "v_fma_f32 v165, v165, v197, v22\n"
      "v_mfma_f32_32x32x16_bf16 v[176:191], v[132:135], a[212:215], v[176:191]\n"
      "v_fma_f32 v166, v166, v198, v22\n"
      "v_fma_f32 v167, v167, v199, v22\n"
      "v_fma_f32 v168, v168, v200, v22\n"
      "v_fma_f32 v169, v169, v201, v22\n"
      "v_fma_f32 v170, v170, v202, v22\n"
      "v_fma_f32 v171, v171, v203, v22\n"
      "v_mfma_f32_32x32x16_bf16 v[176:191], v[136:139], a[216:219], v[176:191]\n"
      "v_fma_f32 v172, v172, v204, v22\n"
      "v_fma_f32 v173, v173, v205, v22\n"
      "v_fma_f32 v174, v174, v206, v22\n"
      "v_fma_f32 v175, v175, v207, v22\n"
      "v_mul_f32 v224, s63, v160\n"
      "v_mul_f32 v225, s63, v161\n"
      "v_mfma_f32_32x32x16_bf16 v[176:191], v[140:143], a[220:223], v[176:191]\n"
      "v_mul_f32 v226, s63, v162\n"
      "v_mul_f32 v227, s63, v163\n"
      "v_mul_f32 v228, s63, v164\n"
      "v_mul_f32 v229, s63, v165\n"
      "v_mul_f32 v230, s63, v166\n"
      "v_mul_f32 v231, s63, v167\n"
      "v_mfma_f32_32x32x16_bf16 v[176:191], v[144:147], a[224:227], v[176:191]\n"
      "v_mul_f32 v232, s63, v168\n"
      "v_mul_f32 v233, s63, v169\n"
      "v_mul_f32 v234, s63, v170\n"
      "v_mul_f32 v235, s63, v171\n"
      "v_mul_f32 v236, s63, v172\n"
      "v_mul_f32 v237, s63, v173\n"
      "v_mfma_f32_32x32x16_bf16 v[176:191], v[148:151], a[228:231], v[176:191]\n"
      "v_mul_f32 v238, s63, v174\n"
      "v_mul_f32 v239, s63, v175\n"
      "v_exp_f32_e64 v224, v224 clamp\n"
      "v_exp_f32_e64 v225, v225 clamp\n"
      "v_exp_f32_e64 v226, v226 clamp\n"
      "v_exp_f32_e64 v227, v227 clamp\n"
      "v_mfma_f32_32x32x16_bf16 v[176:191], v[152:155], a[232:235], v[176:191]\n"
      "v_exp_f32_e64 v228, v228 clamp\n"
      "v_exp_f32_e64 v229, v229 clamp\n"
      "v_exp_f32_e64 v230, v230 clamp\n"
      "v_exp_f32_e64 v231, v231 clamp\n"
      "v_exp_f32_e64 v232, v232 clamp\n"
      "v_exp_f32_e64 v233, v233 clamp\n"
      "v_mfma_f32_32x32x16_bf16 v[176:191], v[156:159], a[236:239], v[176:191]\n"
      "v_exp_f32_e64 v234, v234 clamp\n"
      "v_exp_f32_e64 v235, v235 clamp\n"
      "v_exp_f32_e64 v236, v236 clamp\n"
      "v_exp_f32_e64 v237, v237 clamp\n"
      "v_exp_f32_e64 v238, v238 clamp\n"
      "v_exp_f32_e64 v239, v239 clamp\n"
      "v_max_f32 v160, v22, v160\n"
      "v_max_f32 v161, v22, v161\n"
      "v_max_f32 v162, v22, v162\n"
      "v_max_f32 v163, v22, v163\n"
      "v_max_f32 v164, v22, v164\n"
      "v_max_f32 v165, v22, v165\n"
      "v_max_f32 v166, v22, v166\n"
      "v_max_f32 v167, v22, v167\n"
      "v_max_f32 v168, v22, v168\n"
      "v_max_f32 v169, v22, v169\n"
      "v_max_f32 v170, v22, v170\n"
      "v_max_f32 v171, v22, v171\n"
      "v_max_f32 v172, v22, v172\n"
      "v_max_f32 v173, v22, v173\n"
      "v_max_f32 v174, v22, v174\n"
      "v_max_f32 v175, v22, v175\n"
      "v_pk_add_f32 v[160:161], v[160:161], v[224:225]\n"
      "v_pk_add_f32 v[162:163], v[162:163], v[226:227]\n"
      "v_pk_add_f32 v[164:165], v[164:165], v[228:229]\n"
      "v_pk_add_f32 v[166:167], v[166:167], v[230:231]\n"
      "v_pk_add_f32 v[168:169], v[168:169], v[232:233]\n"
      "v_pk_add_f32 v[170:171], v[170:171], v[234:235]\n"
      "v_pk_add_f32 v[172:173], v[172:173], v[236:237]\n"
      "v_pk_add_f32 v[174:175], v[174:175], v[238:239]\n"
      "v_cvt_pk_bf16_f32 v240, v160, v161\n"
      "v_cvt_pk_bf16_f32 v241, v162, v163\n"
      "v_cvt_pk_bf16_f32 v242, v164, v165\n"
      "v_cvt_pk_bf16_f32 v243, v166, v167\n"
      "v_cvt_pk_bf16_f32 v244, v168, v169\n"
      "v_cvt_pk_bf16_f32 v245, v170, v171\n"
      "v_cvt_pk_bf16_f32 v246, v172, v173\n"
      "v_cvt_pk_bf16_f32 v247, v174, v175\n"
      "L_A_EPI:\n"
      "v_pk_fma_f32 v[176:177], v[176:177], v[192:193], v[20:21] op_sel_hi:[1,1,0]\n"
      "v_pk_fma_f32 v[178:179], v[178:179], v[194:195], v[20:21] op_sel_hi:[1,1,0]\n"
      "v_pk_fma_f32 v[180:181], v[180:181], v[196:197], v[20:21] op_sel_hi:[1,1,0]\n"
      "v_pk_fma_f32 v[182:183], v[182:183], v[198:199], v[20:21] op_sel_hi:[1,1,0]\n"
      "v_pk_fma_f32 v[184:185], v[184:185], v[200:201], v[20:21] op_sel_hi:[1,1,0]\n"
      "v_pk_fma_f32 v[186:187], v[186:187], v[202:203], v[20:21] op_sel_hi:[1,1,0]\n"
      "v_pk_fma_f32 v[188:189], v[188:189], v[204:205], v[20:21] op_sel_hi:[1,1,0]\n"
      "v_pk_fma_f32 v[190:191], v[190:191], v[206:207], v[20:21] op_sel_hi:[1,1,0]\n"
      "v_cvt_pk_bf16_f32 v248, v176, v177\n"
      "v_cvt_pk_bf16_f32 v249, v178, v179\n"
      "v_cvt_pk_bf16_f32 v250, v180, v181\n"
      "v_cvt_pk_bf16_f32 v251, v182, v183\n"
      "v_cvt_pk_bf16_f32 v252, v184, v185\n"
      "v_cvt_pk_bf16_f32 v253, v186, v187\n"
      "v_cvt_pk_bf16_f32 v254, v188, v189\n"
      "v_cvt_pk_bf16_f32 v255, v190, v191\n"
      "s_lshl_b32 s35, s12, 11\n"
      "v_mfma_f32_32x32x16_bf16 v[224:239], v[240:243], v[248:251], 0\n"
      "v_mfma_f32_32x32x16_bf16 v[224:239], v[244:247], v[252:255], v[224:239]\n"
      "s_add_u32 s35, s35, s49\n"
      "s_nop 10\n"
      "v_cmp_u_f32 vcc, v224, v224\n"
      "s_cbranch_vccnz L_XA_BAD\n"
      "L_XA_OK:\n"
      "ds_write_b128 v26, v[224:227] offset:0\n"
      "ds_write_b128 v26, v[228:231] offset:4096\n"
      "ds_write_b128 v26, v[232:235] offset:8192\n"
      "ds_write_b128 v26, v[236:239] offset:12288\n"
      "s_waitcnt lgkmcnt(0)\n"
      "s_barrier\n"
      "ds_read_b128 v[192:195], v27 offset:0\n"
      "ds_read_b128 v[196:199], v27 offset:1024\n"
      "ds_read_b128 v[200:203], v27 offset:2048\n"
      "ds_read_b128 v[204:207], v27 offset:3072\n"
      "s_waitcnt lgkmcnt(0)\n"
      "v_pk_add_f32 v[192:193], v[192:193], v[196:197]\n"
      "v_pk_add_f32 v[194:195], v[194:195], v[198:199]\n"
      "v_pk_add_f32 v[200:201], v[200:201], v[204:205]\n"
      "v_pk_add_f32 v[202:203], v[202:203], v[206:207]\n"
      "v_pk_add_f32 v[192:193], v[192:193], v[200:201]\n"
      "v_pk_add_f32 v[194:195], v[194:195], v[202:203]\n"
      "v_cvt_pk_bf16_f32 v204, v192, v193\n"
      "v_cvt_pk_bf16_f32 v205, v194, v195\n"
      "buffer_store_dwordx2 v[204:205], v7, s[4:7], s35 offen sc1\n"
      "s_cmp_eq_u32 s15, 0\n"
      "s_cbranch_scc1 L_S1\n"
      "; EARLY flag: every wave has issued its stores; wave 0 pushes the tag into the 16 consumers' lines (consumers validate what they read)\n"
      "s_add_u32 s34, s16, 2\n"
      "v_mov_b32 v28, s34\n"
      "v_mov_b32 v29, s17\n"
      "s_cmp_lg_u32 s14, 0\n"
      "s_barrier\n"
      "s_cbranch_scc1 L_sig_9\n"
      "buffer_atomic_swap_x2 v[28:29], v6, s[4:7], s81 offen\n"
      "L_sig_9:\n"
      "L_S1:\n"
      "ds_read_b64_tr_b16 a[176:177], v218 offset:0\n"
      "ds_read_b64_tr_b16 a[178:179], v218 offset:224\n"
      "ds_read_b64_tr_b16 a[180:181], v218 offset:896\n"
      "ds_read_b64_tr_b16 a[182:183], v218 offset:1120\n"
      "ds_read_b64_tr_b16 a[184:185], v218 offset:1792\n"
      "ds_read_b64_tr_b16 a[186:187], v218 offset:2016\n"
      "ds_read_b64_tr_b16 a[188:189], v218 offset:2688\n"
      "ds_read_b64_tr_b16 a[190:191], v218 offset:2912\n"
      "ds_read_b64_tr_b16 a[192:193], v218 offset:3584\n"
      "ds_read_b64_tr_b16 a[194:195], v218 offset:3808\n"
      "ds_read_b64_tr_b16 a[196:197], v218 offset:4480\n"
      "ds_read_b64_tr_b16 a[198:199], v218 offset:4704\n"
      "ds_read_b64_tr_b16 a[200:201], v218 offset:5376\n"
      "ds_read_b64_tr_b16 a[202:203], v218 offset:5600\n"
      "ds_read_b64_tr_b16 a[204:205], v218 offset:6272\n"
      "ds_read_b64_tr_b16 a[206:207], v218 offset:6496\n"
      "ds_read_b64_tr_b16 a[208:209], v218 offset:7168\n"
      "ds_read_b64_tr_b16 a[210:211], v218 offset:7392\n"
      "ds_read_b64_tr_b16 a[212:213], v218 offset:8064\n"
      "ds_read_b64_tr_b16 a[214:215], v218 offset:8288\n"
      "; Q projection in the shadow of the hand-off\n"
      "s_waitcnt lgkmcnt(15)\n"
      "v_mfma_f32_32x32x16_bf16 v[160:175], a[176:179], v[48:51], 0\n"
      "ds_read_b64_tr_b16 a[216:217], v218 offset:8960\n"
      "ds_read_b64_tr_b16 a[218:219], v218 offset:9184\n"
      "s_bfe_u32 s52, s64, 0x2000e\n"
      "v_mfma_f32_32x32x16_bf16 v[160:175], a[180:183], v[52:55], v[160:175]\n"
      "ds_read_b64_tr_b16 a[220:221], v218 offset:9856\n"
      "ds_read_b64_tr_b16 a[222:223], v218 offset:10080\n"
      "s_lshl_b32 s52, s52, 13\n"
      "s_waitcnt lgkmcnt(15)\n"
      "v_mfma_f32_32x32x16_bf16 v[160:175], a[184:187], v[56:59], v[160:175]\n"
      "ds_read_b64_tr_b16 a[224:225], v218 offset:10752\n"
      "ds_read_b64_tr_b16 a[226:227], v218 offset:10976\n"
      "s_add_u32 s52, s52, s49\n"
      "v_mfma_f32_32x32x16_bf16 v[160:175], a[188:191], v[60:63], v[160:175]\n"
      "ds_read_b64_tr_b16 a[228:229], v218 offset:11648\n"
      "ds_read_b64_tr_b16 a[230:231], v218 offset:11872\n"
      "s_bfe_u32 s53, s65, 0x2000e\n"
      "s_waitcnt lgkmcnt(15)\n"
      "v_mfma_f32_32x32x16_bf16 v[160:175], a[192:195], v[64:67], v[160:175]\n"
      "ds_read_b64_tr_b16 a[232:233], v218 offset:12544\n"
      "ds_read_b64_tr_b16 a[234:235], v218 offset:12768\n"
      "s_lshl_b32 s53, s53, 13\n"
      "v_mfma_f32_32x32x16_bf16 v[160:175], a[196:199], v[68:71], v[160:175]\n"
      "ds_read_b64_tr_b16 a[236:237], v218 offset:13440\n"
      "ds_read_b64_tr_b16 a[238:239], v218 offset:13664\n"
      "s_add_u32 s53, s53, s49\n"
      "s_waitcnt lgkmcnt(15)\n"
      "v_mfma_f32_32x32x16_bf16 v[160:175], a[200:203], v[72:75], v[160:175]\n"
      "ds_read_b64_tr_b16 a[240:241], v218 offset:14336\n"
      "ds_read_b64_tr_b16 a[242:243], v218 offset:14560\n"
      "s_bfe_u32 s54, s66, 0x2000e\n"
      "v_mfma_f32_32x32x16_bf16 v[160:175], a[204:207], v[76:79], v[160:175]\n"
      "ds_read_b64_tr_b16 a[244:245], v218 offset:15232\n"
      "ds_read_b64_tr_b16 a[246:247], v218 offset:15456\n"
      "s_lshl_b32 s54, s54, 13\n"
      "s_waitcnt lgkmcnt(15)\n"
      "v_mfma_f32_32x32x16_bf16 v[160:175], a[208:211], v[80:83], v[160:175]\n"
      "ds_read_b64_tr_b16 a[248:249], v218 offset:16128\n"
      "ds_read_b64_tr_b16 a[250:251], v218 offset:16352\n"
      "s_add_u32 s54, s54, s49\n"
      "v_mfma_f32_32x32x16_bf16 v[160:175], a[212:215], v[84:87], v[160:175]\n"
      "ds_read_b64_tr_b16 a[252:253], v218 offset:17024\n"
      "ds_read_b64_tr_b16 a[254:255], v218 offset:17248\n"
      "s_bfe_u32 s55, s67, 0x2000e\n"
      "s_waitcnt lgkmcnt(15)\n"
      "v_mfma_f32_32x32x16_bf16 v[160:175], a[216:219], v[88:91], v[160:175]\n"
      "ds_read_b64_tr_b16 a[176:177], v218 offset:17920\n"
      "ds_read_b64_tr_b16 a[178:179], v218 offset:18144\n"
      "s_lshl_b32 s55, s55, 13\n"
      "v_mfma_f32_32x32x16_bf16 v[160:175], a[220:223], v[92:95], v[160:175]\n"
      "ds_read_b64_tr_b16 a[180:181], v218 offset:18816\n"
      "ds_read_b64_tr_b16 a[182:183], v218 offset:19040\n"
      "s_add_u32 s55, s55, s49\n"
      "s_cmp_lg_u32 s15, 0\n"
      "s_cbranch_scc1 L_S2\n"
      "; first pass (buffer not poisoned): drained flag\n"
      "; EARLY flag: every wave has issued its stores; wave 0 pushes the tag into the 16 consumers' lines (consumers validate what they read)\n"
      "s_add_u32 s34, s16, 2\n"
      "v_mov_b32 v28, s34\n"
      "v_mov_b32 v29, s17\n"
      "s_cmp_lg_u32 s14, 0\n"
      "s_waitcnt vmcnt(0)\n"
      "s_barrier\n"
      "s_cbranch_scc1 L_sig_10\n"
      "buffer_atomic_swap_x2 v[28:29], v6, s[4:7], s81 offen\n"
      "L_sig_10:\n"
      "; first pass: the softmax layer's Wv, Wk images -> LDS\n"
      "s_mov_b64 exec, 0xffff\n"
      "ds_write_b128 v14, a[52:55] offset:50176\n"
      "s_mov_b64 exec, -1\n"
      "ds_write_b128 v13, a[112:115] offset:50176\n"
      "ds_write_b128 v13, a[116:119] offset:51200\n"
      "ds_write_b128 v13, a[120:123] offset:52224\n"
      "ds_write_b128 v13, a[124:127] offset:53248\n"
      "ds_write_b128 v13, a[128:131] offset:54272\n"
      "ds_write_b128 v13, a[132:135] offset:55296\n"
      "v_add_u32 v223, 75264, v13\n"
      "ds_write_b128 v223, a[136:139] offset:0\n"
      "ds_write_b128 v223, a[140:143] offset:1024\n"
      "ds_write_b128 v223, a[144:147] offset:2048\n"
      "ds_write_b128 v223, a[148:151] offset:3072\n"
      "ds_write_b128 v223, a[152:155] offset:4096\n"
      "ds_write_b128 v223, a[156:159] offset:5120\n"
      "s_waitcnt lgkmcnt(0)\n"
      "L_S2:\n"
      "s_waitcnt lgkmcnt(15)\n"
      "v_mfma_f32_32x32x16_bf16 v[160:175], a[224:227], v[96:99], v[160:175]\n"
      "ds_read_b64_tr_b16 a[184:185], v218 offset:19712\n"
      "ds_read_b64_tr_b16 a[186:187], v218 offset:19936\n"
      "s_add_u32 s49, s49, 524288\n"
      "v_mfma_f32_32x32x16_bf16 v[160:175], a[228:231], v[100:103], v[160:175]\n"
      "ds_read_b64_tr_b16 a[188:189], v218 offset:20608\n"
      "ds_read_b64_tr_b16 a[190:191], v218 offset:20832\n"
      "v_add_u32 v30, 4096, v219\n"
      "s_waitcnt lgkmcnt(15)\n"
      "v_mfma_f32_32x32x16_bf16 v[160:175], a[232:235], v[104:107], v[160:175]\n"
      "ds_read_b64_tr_b16 a[192:193], v218 offset:21504\n"
      "ds_read_b64_tr_b16 a[194:195], v218 offset:21728\n"
      "v_mov_b32 v200, 0x3f80\n"
      "v_mfma_f32_32x32x16_bf16 v[160:175], a[236:239], v[108:111], v[160:175]\n"
      "ds_read_b64_tr_b16 a[196:197], v218 offset:22400\n"
      "ds_read_b64_tr_b16 a[198:199], v218 offset:22624\n"
      "v_mov_b32 v201, 0x3f800000\n"
      "s_waitcnt lgkmcnt(15)\n"
      "v_mfma_f32_32x32x16_bf16 v[160:175], a[240:243], v[112:115], v[160:175]\n"
      "ds_read_b64_tr_b16 a[200:201], v218 offset:23296\n"
      "ds_read_b64_tr_b16 a[202:203], v218 offset:23520\n"
      "v_mov_b64 v[192:193], 0\n"
      "v_mfma_f32_32x32x16_bf16 v[160:175], a[244:247], v[116:119], v[160:175]\n"
      "ds_read_b64_tr_b16 a[204:205], v218 offset:24192\n"
      "ds_read_b64_tr_b16 a[206:207], v218 offset:24416\n"
      "v_mov_b64 v[194:195], 0\n"
      "s_waitcnt lgkmcnt(15)\n"
      "v_mfma_f32_32x32x16_bf16 v[160:175], a[248:251], v[120:123], v[160:175]\n"
      "v_mov_b64 v[196:197], 0\n"
      "v_mov_b64 v[198:199], 0\n"
      "s_mov_b32 s1, 0\n"
      "v_mfma_f32_32x32x16_bf16 v[160:175], a[252:255], v[124:127], v[160:175]\n"
      "s_waitcnt lgkmcnt(12)\n"
      "v_mfma_f32_32x32x16_bf16 v[160:175], a[176:179], v[128:131], v[160:175]\n"
      "v_mfma_f32_32x32x16_bf16 v[160:175], a[180:183], v[132:135], v[160:175]\n"
      "s_waitcnt lgkmcnt(8)\n"
      "v_mfma_f32_32x32x16_bf16 v[160:175], a[184:187], v[136:139], v[160:175]\n"
      "v_mfma_f32_32x32x16_bf16 v[160:175], a[188:191], v[140:143], v[160:175]\n"
      "s_waitcnt lgkmcnt(4)\n"
      "v_mfma_f32_32x32x16_bf16 v[160:175], a[192:195], v[144:147], v[160:175]\n"
      "v_mfma_f32_32x32x16_bf16 v[160:175], a[196:199], v[148:151], v[160:175]\n"
      "s_waitcnt lgkmcnt(0)\n"
      "v_mfma_f32_32x32x16_bf16 v[160:175], a[200:203], v[152:155], v[160:175]\n"
      "v_mfma_f32_32x32x16_bf16 v[160:175], a[204:207], v[156:159], v[160:175]\n"
      "v_mfma_f32_32x32x16_bf16 v[160:175], a[168:171], v[208:211], v[160:175]\n"
      "s_cmp_lg_u32 s14, 3\n"
      "s_cbranch_scc1 L_ep_11\n"
      "s_mov_b64 exec, 0xffff\n"
      "v_mov_b64 v[28:29], 0\n"
      "buffer_atomic_or_x2 v[28:29], v7, s[4:7], s83 offen sc0\n"
      "s_mov_b64 exec, -1\n"
      "L_ep_11:\n"
      "s_nop 9\n"
      "v_pk_fma_f32 v[160:161], v[160:161], v[24:25], v[22:23] op_sel_hi:[1,0,0]\n"
      "v_pk_fma_f32 v[162:163], v[162:163], v[24:25], v[22:23] op_sel_hi:[1,0,0]\n"
      "v_pk_fma_f32 v[164:165], v[164:165], v[24:25], v[22:23] op_sel_hi:[1,0,0]\n"
      "v_pk_fma_f32 v[166:167], v[166:167], v[24:25], v[22:23] op_sel_hi:[1,0,0]\n"
      "v_pk_fma_f32 v[168:169], v[168:169], v[24:25], v[22:23] op_sel_hi:[1,0,0]\n"
      "v_pk_fma_f32 v[170:171], v[170:171], v[24:25], v[22:23] op_sel_hi:[1,0,0]\n"
      "v_pk_fma_f32 v[172:173], v[172:173], v[24:25], v[22:23] op_sel_hi:[1,0,0]\n"
      "v_pk_fma_f32 v[174:175], v[174:175], v[24:25], v[22:23] op_sel_hi:[1,0,0]\n"
      "v_pk_mul_f32 v[224:225], v[160:161], s[62:63] op_sel:[0,1] op_sel_hi:[1,1]\n"
      "v_pk_mul_f32 v[226:227], v[162:163], s[62:63] op_sel:[0,1] op_sel_hi:[1,1]\n"
      "v_pk_mul_f32 v[228:229], v[164:165], s[62:63] op_sel:[0,1] op_sel_hi:[1,1]\n"
      "v_pk_mul_f32 v[230:231], v[166:167], s[62:63] op_sel:[0,1] op_sel_hi:[1,1]\n"
      "v_pk_mul_f32 v[232:233], v[168:169], s[62:63] op_sel:[0,1] op_sel_hi:[1,1]\n"
      "v_pk_mul_f32 v[234:235], v[170:171], s[62:63] op_sel:[0,1] op_sel_hi:[1,1]\n"
      "v_pk_mul_f32 v[236:237], v[172:173], s[62:63] op_sel:[0,1] op_sel_hi:[1,1]\n"
      "v_pk_mul_f32 v[238:239], v[174:175], s[62:63] op_sel:[0,1] op_sel_hi:[1,1]\n"
      "v_exp_f32_e64 v224, v224 clamp\n"
      "v_exp_f32_e64 v225, v225 clamp\n"
      "v_exp_f32_e64 v226, v226 clamp\n"
      "v_exp_f32_e64 v227, v227 clamp\n"
      "v_exp_f32_e64 v228, v228 clamp\n"
      "v_exp_f32_e64 v229, v229 clamp\n"
      "v_exp_f32_e64 v230, v230 clamp\n"
      "v_exp_f32_e64 v231, v231 clamp\n"
      "v_exp_f32_e64 v232, v232 clamp\n"
      "v_exp_f32_e64 v233, v233 clamp\n"
      "v_exp_f32_e64 v234, v234 clamp\n"
      "v_exp_f32_e64 v235, v235 clamp\n"
      "v_exp_f32_e64 v236, v236 clamp\n"
      "v_exp_f32_e64 v237, v237 clamp\n"
      "v_exp_f32_e64 v238, v238 clamp\n"
      "v_exp_f32_e64 v239, v239 clamp\n"
      "v_max_f32 v160, v22, v160\n"
      "v_max_f32 v161, v22, v161\n"
      "v_max_f32 v162, v22, v162\n"
      "v_max_f32 v163, v22, v163\n"
      "v_max_f32 v164, v22, v164\n"
      "v_max_f32 v165, v22, v165\n"
      "v_max_f32 v166, v22, v166\n"
      "v_max_f32 v167, v22, v167\n"
      "v_max_f32 v168, v22, v168\n"
      "v_max_f32 v169, v22, v169\n"
      "v_max_f32 v170, v22, v170\n"
      "v_max_f32 v171, v22, v171\n"
      "v_max_f32 v172, v22, v172\n"
      "v_max_f32 v173, v22, v173\n"
      "v_max_f32 v174, v22, v174\n"
      "v_max_f32 v175, v22, v175\n"
      "v_pk_add_f32 v[160:161], v[160:161], v[224:225]\n"
      "v_pk_add_f32 v[162:163], v[162:163], v[226:227]\n"
      "v_pk_add_f32 v[164:165], v[164:165], v[228:229]\n"
      "v_pk_add_f32 v[166:167], v[166:167], v[230:231]\n"
      "v_pk_add_f32 v[168:169], v[168:169], v[232:233]\n"
      "v_pk_add_f32 v[170:171], v[170:171], v[234:235]\n"
      "v_pk_add_f32 v[172:173], v[172:173], v[236:237]\n"
      "v_pk_add_f32 v[174:175], v[174:175], v[238:239]\n"
      "v_cvt_pk_bf16_f32 v240, v160, v161\n"
      "v_cvt_pk_bf16_f32 v241, v162, v163\n"
      "v_cvt_pk_bf16_f32 v242, v164, v165\n"
      "v_cvt_pk_bf16_f32 v243, v166, v167\n"
      "v_cvt_pk_bf16_f32 v244, v168, v169\n"
      "v_cvt_pk_bf16_f32 v245, v170, v171\n"
      "v_cvt_pk_bf16_f32 v246, v172, v173\n"
      "v_cvt_pk_bf16_f32 v247, v174, v175\n"
      "v_cndmask_b32 v246, v246, 0, s[60:61]\n"
      "v_cndmask_b32 v247, v247, 0, s[60:61]\n"
      "; hand-off: wave 3 polls its flags, barrier\n"
      "s_add_u32 s34, s16, 2\n"
      "s_cmp_lg_u32 s14, 3\n"
      "s_cbranch_scc1 L_pd_13\n"
      "s_mov_b32 s51, 0x40000\n"
      "s_mov_b64 exec, 0xffff\n"
      "s_branch L_pc_14\n"
      "L_poll_12:\n"
      "s_mov_b64 exec, 0xffff\n"
      "v_mov_b64 v[28:29], 0\n"
      "buffer_atomic_or_x2 v[28:29], v7, s[4:7], s83 offen sc0\n"
      "s_sub_u32 s51, s51, 1\n"
      "s_cbranch_scc1 L_pd_13\n"
      "L_pc_14:\n"
      "s_waitcnt vmcnt(0)\n"
      "v_subrev_u32 v28, s34, v28\n"
      "v_cmp_eq_u32 s[40:41], s17, v29\n"
      "v_cmp_gt_u32 vcc, 64, v28\n"
      "s_and_b64 vcc, vcc, s[40:41]\n"
      "s_cmp_eq_u64 vcc, exec\n"
      "s_cbranch_scc0 L_poll_12\n"
      "L_pd_13:\n"
      "s_mov_b64 exec, -1\n"
      "s_waitcnt vmcnt(0) lgkmcnt(0)\n"
      "s_barrier\n"
      "; sum the 16 row blocks' partial k^T v (this wave: one quarter of the registers; lower / upper lanes: even / odd row blocks)\n"
      "buffer_load_dwordx4 v[48:51], v219, s[4:7], s52 offen sc1\n"
      "buffer_load_dwordx4 v[52:55], v30, s[4:7], s52 offen sc1\n"
      "buffer_load_dwordx4 v[56:59], v219, s[4:7], s53 offen sc1\n"
      "buffer_load_dwordx4 v[60:63], v30, s[4:7], s53 offen sc1\n"
      "buffer_load_dwordx4 v[64:67], v219, s[4:7], s54 offen sc1\n"
      "buffer_load_dwordx4 v[68:71], v30, s[4:7], s54 offen sc1\n"
      "buffer_load_dwordx4 v[72:75], v219, s[4:7], s55 offen sc1\n"
      "buffer_load_dwordx4 v[76:79], v30, s[4:7], s55 offen sc1\n"
      "; meanwhile: this layer's Wo columns -> LDS\n"
      "v_add_u32 v223, s85, v0\n"
      "ds_write_b128 v223, a[56:59] offset:0\n"
      "ds_write_b128 v223, a[60:63] offset:1024\n"
      "ds_write_b128 v223, a[64:67] offset:2048\n"
      "ds_write_b128 v223, a[68:71] offset:3072\n"
      "ds_write_b128 v223, a[72:75] offset:4096\n"
      "ds_write_b128 v223, a[76:79] offset:5120\n"
      "ds_write_b128 v223, a[80:83] offset:6144\n"
      "ds_write_b128 v223, a[84:87] offset:7168\n"
      "ds_write_b128 v223, a[88:91] offset:8192\n"
      "ds_write_b128 v223, a[92:95] offset:9216\n"
      "s_waitcnt vmcnt(7)\n"
      "v_dot2c_f32_bf16 v192, v48, v200\n"
      "v_dot2c_f32_bf16 v193, v48, v201\n"
      "v_dot2c_f32_bf16 v194, v49, v200\n"
      "v_dot2c_f32_bf16 v195, v49, v201\n"
      "v_dot2c_f32_bf16 v196, v50, v200\n"
      "v_dot2c_f32_bf16 v197, v50, v201\n"
      "v_dot2c_f32_bf16 v198, v51, v200\n"
      "v_dot2c_f32_bf16 v199, v51, v201\n"
      "s_waitcnt vmcnt(6)\n"
      "ds_write_b128 v223, a[96:99] offset:10240\n"
      "v_dot2c_f32_bf16 v192, v52, v200\n"
      "v_dot2c_f32_bf16 v193, v52, v201\n"
      "v_dot2c_f32_bf16 v194, v53, v200\n"
      "v_dot2c_f32_bf16 v195, v53, v201\n"
      "v_dot2c_f32_bf16 v196, v54, v200\n"
      "v_dot2c_f32_bf16 v197, v54, v201\n"
      "v_dot2c_f32_bf16 v198, v55, v200\n"
      "v_dot2c_f32_bf16 v199, v55, v201\n"
      "s_waitcnt vmcnt(5)\n"
      "v_dot2c_f32_bf16 v192, v56, v200\n"
      "v_dot2c_f32_bf16 v193, v56, v201\n"
      "v_dot2c_f32_bf16 v194, v57, v200\n"
      "v_dot2c_f32_bf16 v195, v57, v201\n"
      "v_dot2c_f32_bf16 v196, v58, v200\n"
      "v_dot2c_f32_bf16 v197, v58, v201\n"
      "v_dot2c_f32_bf16 v198, v59, v200\n"
      "v_dot2c_f32_bf16 v199, v59, v201\n"
      "s_waitcnt vmcnt(4)\n"
      "ds_write_b128 v223, a[100:103] offset:11264\n"
      "v_dot2c_f32_bf16 v192, v60, v200\n"
      "v_dot2c_f32_bf16 v193, v60, v201\n"
      "v_dot2c_f32_bf16 v194, v61, v200\n"
      "v_dot2c_f32_bf16 v195, v61, v201\n"
      "v_dot2c_f32_bf16 v196, v62, v200\n"
      "v_dot2c_f32_bf16 v197, v62, v201\n"
      "v_dot2c_f32_bf16 v198, v63, v200\n"
      "v_dot2c_f32_bf16 v199, v63, v201\n"
      "s_waitcnt vmcnt(3)\n"
      "v_dot2c_f32_bf16 v192, v64, v200\n"
      "v_dot2c_f32_bf16 v193, v64, v201\n"
      "v_dot2c_f32_bf16 v194, v65, v200\n"
      "v_dot2c_f32_bf16 v195, v65, v201\n"
      "v_dot2c_f32_bf16 v196, v66, v200\n"
      "v_dot2c_f32_bf16 v197, v66, v201\n"
      "v_dot2c_f32_bf16 v198, v67, v200\n"
      "v_dot2c_f32_bf16 v199, v67, v201\n"
      "s_waitcnt vmcnt(2)\n"
      "ds_write_b128 v223, a[104:107] offset:12288\n"
      "v_dot2c_f32_bf16 v192, v68, v200\n"
      "v_dot2c_f32_bf16 v193, v68, v201\n"
      "v_dot2c_f32_bf16 v194, v69, v200\n"
      "v_dot2c_f32_bf16 v195, v69, v201\n"
      "v_dot2c_f32_bf16 v196, v70, v200\n"
      "v_dot2c_f32_bf16 v197, v70, v201\n"
      "v_dot2c_f32_bf16 v198, v71, v200\n"
      "v_dot2c_f32_bf16 v199, v71, v201\n"
      "s_waitcnt vmcnt(1)\n"
      "v_dot2c_f32_bf16 v192, v72, v200\n"
      "v_dot2c_f32_bf16 v193, v72, v201\n"
      "v_dot2c_f32_bf16 v194, v73, v200\n"
      "v_dot2c_f32_bf16 v195, v73, v201\n"
      "v_dot2c_f32_bf16 v196, v74, v200\n"
      "v_dot2c_f32_bf16 v197, v74, v201\n"
      "v_dot2c_f32_bf16 v198, v75, v200\n"
      "v_dot2c_f32_bf16 v199, v75, v201\n"
      "s_waitcnt vmcnt(0)\n"
      "ds_write_b128 v223, a[108:111] offset:13312\n"
      "v_dot2c_f32_bf16 v192, v76, v200\n"
      "v_dot2c_f32_bf16 v193, v76, v201\n"
      "v_dot2c_f32_bf16 v194, v77, v200\n"
      "v_dot2c_f32_bf16 v195, v77, v201\n"
      "v_dot2c_f32_bf16 v196, v78, v200\n"
      "v_dot2c_f32_bf16 v197, v78, v201\n"
      "v_dot2c_f32_bf16 v198, v79, v200\n"
      "v_dot2c_f32_bf16 v199, v79, v201\n"
      "; LATE flag: every wave drained its stores in front of the barrier just passed\n"
      "s_cmp_lg_u32 s14, 1\n"
      "s_cbranch_scc1 L_late_15\n"
      "s_add_u32 s34, s16, 2\n"
      "v_mov_b32 v28, s34\n"
      "v_mov_b32 v29, s17\n"
      "buffer_atomic_swap_x2 v[28:29], v6, s[4:7], s81 offen offset:128\n"
      "L_late_15:\n"
      "L_P_RE:\n"
      "s_nop 2\n"
      "v_permlane32_swap_b32 v192, v196\n"
      "v_permlane32_swap_b32 v193, v197\n"
      "v_permlane32_swap_b32 v194, v198\n"
      "v_permlane32_swap_b32 v195, v199\n"
      "v_pk_add_f32 v[192:193], v[192:193], v[196:197]\n"
      "v_pk_add_f32 v[194:195], v[194:195], v[198:199]\n"
      "v_cmp_u_f32 vcc, v192, v194\n"
      "s_cbranch_vccnz L_P_BAD\n"
      "L_P_OK:\n"
      "v_cvt_pk_bf16_f32 v192, v192, v193\n"
      "v_cvt_pk_bf16_f32 v193, v194, v195\n"
      "ds_write_b64 v220, v[192:193]\n"
      "s_waitcnt lgkmcnt(0)\n"
      "s_barrier\n"
      "ds_read_b128 v[116:119], v221\n"
      "ds_read_b128 v[120:123], v221 offset:1024\n"
      "s_waitcnt lgkmcnt(1)\n"
      "v_mfma_f32_32x32x16_bf16 v[160:175], v[116:119], v[240:243], 0\n"
      "s_waitcnt lgkmcnt(0)\n"
      "v_mfma_f32_32x32x16_bf16 v[160:175], v[120:123], v[244:247], v[160:175]\n"
      "s_nop 11\n"
      "v_add_f32 v28, v22, v172\n"
      "v_rcp_f32 v29, v28\n"
      "s_nop 0\n"
      "v_mov_b32 v28, v29\n"
      "s_nop 1\n"
      "v_permlane32_swap_b32 v29, v28\n"
      "v_mul_f32 v29, v28, v23\n"
      "v_pk_mul_f32 v[112:113], v[160:161], v[28:29] op_sel_hi:[1,0]\n"
      "v_pk_mul_f32 v[114:115], v[162:163], v[28:29] op_sel_hi:[1,0]\n"
      "v_pk_mul_f32 v[116:117], v[164:165], v[28:29] op_sel_hi:[1,0]\n"
      "v_pk_mul_f32 v[118:119], v[166:167], v[28:29] op_sel_hi:[1,0]\n"
      "v_cvt_pk_bf16_f32 v128, v112, v113\n"
      "v_cvt_pk_bf16_f32 v129, v114, v115\n"
      "v_cvt_pk_bf16_f32 v130, v116, v117\n"
      "v_cvt_pk_bf16_f32 v131, v118, v119\n"
      "buffer_store_dwordx4 v[128:131], v0, s[4:7], s45 offen sc1\n"
      "v_pk_mul_f32 v[120:121], v[168:169], v[28:29] op_sel_hi:[1,0]\n"
      "v_pk_mul_f32 v[122:123], v[170:171], v[28:29] op_sel_hi:[1,0]\n"
      "v_pk_mul_f32 v[124:125], v[172:173], v[28:29] op_sel:[0,1] op_sel_hi:[1,1]\n"
      "v_pk_mul_f32 v[126:127], v[174:175], v[28:29] op_sel:[0,1] op_sel_hi:[1,1]\n"
      "v_cvt_pk_bf16_f32 v132, v120, v121\n"
      "v_cvt_pk_bf16_f32 v133, v122, v123\n"
      "v_cvt_pk_bf16_f32 v134, v124, v125\n"
      "v_cvt_pk_bf16_f32 v135, v126, v127\n"
      "buffer_store_dwordx4 v[132:135], v0, s[4:7], s45 offen offset:1024 sc1\n"
      "; EARLY flag: every wave has issued its stores; wave 0 pushes the tag into the 16 consumers' lines (consumers validate what they read)\n"
      "s_add_u32 s34, s16, 3\n"
      "v_mov_b32 v28, s34\n"
      "v_mov_b32 v29, s17\n"
      "s_cmp_lg_u32 s14, 0\n"
      "s_barrier\n"
      "s_cbranch_scc1 L_sig_16\n"
      "buffer_atomic_swap_x2 v[28:29], v6, s[4:7], s80 offen\n"
      "L_sig_16:\n"
      "s_add_u32 s52, s44, 0\n"
      "s_add_u32 s53, s44, 4096\n"
      "s_add_u32 s54, s44, 8192\n"
      "s_add_u32 s55, s44, 12288\n"
      "s_add_u32 s44, s44, 2097152\n"
      "s_add_u32 s45, s45, 2097152\n"
      "s_mov_b32 s1, 0\n"
      "v_mov_b32 v222, 0x207\n"
      "L_OA_RE:\n"
      "ds_read_b64_tr_b16 a[176:177], v5 offset:0\n"
      "ds_read_b64_tr_b16 a[178:179], v5 offset:1024\n"
      "ds_read_b64_tr_b16 a[180:181], v5 offset:2048\n"
      "ds_read_b64_tr_b16 a[182:183], v5 offset:3072\n"
      "ds_read_b64_tr_b16 a[184:185], v5 offset:3584\n"
      "ds_read_b64_tr_b16 a[186:187], v5 offset:4608\n"
      "ds_read_b64_tr_b16 a[188:189], v5 offset:5632\n"
      "ds_read_b64_tr_b16 a[190:191], v5 offset:6656\n"
      "ds_read_b64_tr_b16 a[192:193], v5 offset:7168\n"
      "ds_read_b64_tr_b16 a[194:195], v5 offset:8192\n"
      "ds_read_b64_tr_b16 a[196:197], v4 offset:0\n"
      "ds_read_b64_tr_b16 a[198:199], v4 offset:1024\n"
      "ds_read_b64_tr_b16 a[200:201], v5 offset:9216\n"
      "ds_read_b64_tr_b16 a[202:203], v5 offset:10240\n"
      "ds_read_b64_tr_b16 a[204:205], v4 offset:2048\n"
      "ds_read_b64_tr_b16 a[206:207], v4 offset:3072\n"
      "ds_read_b64_tr_b16 a[208:209], v5 offset:10752\n"
      "ds_read_b64_tr_b16 a[210:211], v5 offset:11776\n"
      "ds_read_b64_tr_b16 a[212:213], v4 offset:3584\n"
      "ds_read_b64_tr_b16 a[214:215], v4 offset:4608\n"
      "ds_read_b64_tr_b16 a[216:217], v5 offset:12800\n"
      "ds_read_b64_tr_b16 a[218:219], v5 offset:13824\n"
      "ds_read_b64_tr_b16 a[220:221], v4 offset:5632\n"
      "ds_read_b64_tr_b16 a[222:223], v4 offset:6656\n"
      "ds_read_b64_tr_b16 a[224:225], v5 offset:14336\n"
      "ds_read_b64_tr_b16 a[226:227], v5 offset:15360\n"
      "ds_read_b64_tr_b16 a[228:229], v4 offset:7168\n"
      "ds_read_b64_tr_b16 a[230:231], v4 offset:8192\n"
      "ds_read_b64_tr_b16 a[232:233], v5 offset:16384\n"
      "ds_read_b64_tr_b16 a[234:235], v5 offset:17408\n"
      "ds_read_b64_tr_b16 a[236:237], v4 offset:9216\n"
      "ds_read_b64_tr_b16 a[238:239], v4 offset:10240\n"
      "ds_read_b64_tr_b16 a[240:241], v5 offset:17920\n"
      "ds_read_b64_tr_b16 a[242:243], v5 offset:18944\n"
      "ds_read_b64_tr_b16 a[244:245], v4 offset:10752\n"
      "ds_read_b64_tr_b16 a[246:247], v4 offset:11776\n"
      "s_cmp_lg_u32 s1, 0\n"
      "s_waitcnt lgkmcnt(0)\n"
      "s_cbranch_scc1 L_OA_GO\n"
      "; hand-off: wave 3 polls its flags, barrier\n"
      "s_add_u32 s34, s16, 3\n"
      "s_cmp_lg_u32 s14, 3\n"
      "s_cbranch_scc1 L_pd_18\n"
      "s_mov_b32 s51, 0x40000\n"
      "L_poll_17:\n"
      "s_mov_b64 exec, 0xffff\n"
      "v_mov_b64 v[28:29], 0\n"
      "buffer_atomic_or_x2 v[28:29], v7, s[4:7], s82 offen sc0\n"
      "s_sub_u32 s51, s51, 1\n"
      "s_cbranch_scc1 L_pd_18\n"
      "L_pc_19:\n"
      "s_waitcnt vmcnt(0)\n"
      "v_subrev_u32 v28, s34, v28\n"
      "v_cmp_eq_u32 s[40:41], s17, v29\n"
      "v_cmp_gt_u32 vcc, 64, v28\n"
      "s_and_b64 vcc, vcc, s[40:41]\n"
      "s_cmp_eq_u64 vcc, exec\n"
      "s_cbranch_scc0 L_poll_17\n"
      "L_pd_18:\n"
      "s_mov_b64 exec, -1\n"
      "s_waitcnt vmcnt(0) lgkmcnt(0)\n"
      "s_barrier\n"
      "L_OA_GO:\n"
      "buffer_load_dwordx4 v[48:51], v3, s[4:7], s52 offen sc1\n"
      "buffer_load_dwordx4 v[52:55], v3, s[4:7], s52 offen offset:1024 sc1\n"
      "s_nop 15\n"
      "buffer_load_dwordx4 v[56:59], v3, s[4:7], s52 offen offset:2048 sc1\n"
      "buffer_load_dwordx4 v[60:63], v3, s[4:7], s52 offen offset:3072 sc1\n"
      "s_nop 15\n"
      "buffer_load_dwordx4 v[64:67], v3, s[4:7], s53 offen sc1\n"
      "buffer_load_dwordx4 v[68:71], v3, s[4:7], s53 offen offset:1024 sc1\n"
      "s_nop 15\n"
      "buffer_load_dwordx4 v[72:75], v3, s[4:7], s53 offen offset:2048 sc1\n"
      "buffer_load_dwordx4 v[76:79], v3, s[4:7], s53 offen offset:3072 sc1\n"
      "s_nop 15\n"
      "buffer_load_dwordx4 v[80:83], v3, s[4:7], s54 offen sc1\n"
      "buffer_load_dwordx4 v[84:87], v3, s[4:7], s54 offen offset:1024 sc1\n"
      "s_nop 15\n"
      "v_mov_b64 v[192:193], v[32:33]\n"
      "v_mov_b64 v[194:195], v[34:35]\n"
      "v_mov_b64 v[196:197], v[36:37]\n"
      "v_mov_b64 v[198:199], v[38:39]\n"
      "v_mov_b64 v[200:201], v[40:41]\n"
      "v_mov_b64 v[202:203], v[42:43]\n"
      "v_mov_b64 v[204:205], v[44:45]\n"
      "v_mov_b64 v[206:207], v[46:47]\n"
      "s_waitcnt vmcnt(9)\n"
      "v_mfma_f32_32x32x16_bf16 v[160:175], a[176:179], v[48:51], 0\n"
      "ds_read_b64_tr_b16 a[248:249], v5 offset:19968\n"
      "ds_read_b64_tr_b16 a[250:251], v5 offset:20992\n"
      "buffer_load_dwordx4 v[88:91], v3, s[4:7], s54 offen offset:2048 sc1\n"
      "s_waitcnt vmcnt(9)\n"
      "v_mfma_f32_32x32x16_bf16 v[160:175], a[180:183], v[52:55], v[160:175]\n"
      "ds_read_b64_tr_b16 a[252:253], v4 offset:12800\n"
      "ds_read_b64_tr_b16 a[254:255], v4 offset:13824\n"
      "buffer_load_dwordx4 v[92:95], v3, s[4:7], s54 offen offset:3072 sc1\n"
      "s_waitcnt vmcnt(9)\n"
      "v_mfma_f32_32x32x16_bf16 v[160:175], a[184:187], v[56:59], v[160:175]\n"
      "ds_read_b64_tr_b16 a[176:177], v5 offset:21504\n"
      "ds_read_b64_tr_b16 a[178:179], v5 offset:22528\n"
      "buffer_load_dwordx4 v[96:99], v3, s[4:7], s55 offen sc1\n"
      "s_waitcnt vmcnt(9)\n"
      "v_mfma_f32_32x32x16_bf16 v[160:175], a[188:191], v[60:63], v[160:175]\n"
      "ds_read_b64_tr_b16 a[180:181], v4 offset:14336\n"
      "ds_read_b64_tr_b16 a[182:183], v4 offset:15360\n"
      "buffer_load_dwordx4 v[100:103], v3, s[4:7], s55 offen offset:1024 sc1\n"
      "s_waitcnt vmcnt(9)\n"
      "v_mfma_f32_32x32x16_bf16 v[160:175], a[192:195], v[64:67], v[160:175]\n"
      "ds_read_b64_tr_b16 a[184:185], v5 offset:23552\n"
      "ds_read_b64_tr_b16 a[186:187], v5 offset:24576\n"
      "buffer_load_dwordx4 v[104:107], v3, s[4:7], s55 offen offset:2048 sc1\n"
      "v_mfma_f32_32x32x16_bf16 v[32:47], a[196:199], v[48:51], v[32:47]\n"
      "ds_read_b64_tr_b16 a[188:189], v4 offset:16384\n"
      "ds_read_b64_tr_b16 a[190:191], v4 offset:17408\n"
      "s_waitcnt vmcnt(9)\n"
      "v_mfma_f32_32x32x16_bf16 v[160:175], a[200:203], v[68:71], v[160:175]\n"
      "ds_read_b64_tr_b16 a[192:193], v5 offset:25088\n"
      "ds_read_b64_tr_b16 a[194:195], v5 offset:26112\n"
      "buffer_load_dwordx4 v[108:111], v3, s[4:7], s55 offen offset:3072 sc1\n"
      "v_mfma_f32_32x32x16_bf16 v[32:47], a[204:207], v[52:55], v[32:47]\n"
      "ds_read_b64_tr_b16 a[196:197], v4 offset:17920\n"
      "ds_read_b64_tr_b16 a[198:199], v4 offset:18944\n"
      "s_waitcnt vmcnt(9)\n"
      "v_mfma_f32_32x32x16_bf16 v[160:175], a[208:211], v[72:75], v[160:175]\n"
      "ds_read_b64_tr_b16 a[200:201], v5 offset:27136\n"
      "ds_read_b64_tr_b16 a[202:203], v5 offset:28160\n"
      "v_mfma_f32_32x32x16_bf16 v[32:47], a[212:215], v[56:59], v[32:47]\n"
      "ds_read_b64_tr_b16 a[204:205], v4 offset:19968\n"
      "ds_read_b64_tr_b16 a[206:207], v4 offset:20992\n"
      "s_waitcnt vmcnt(8)\n"
      "v_mfma_f32_32x32x16_bf16 v[160:175], a[216:219], v[76:79], v[160:175]\n"
      "ds_read_b64_tr_b16 a[208:209], v4 offset:21504\n"
      "ds_read_b64_tr_b16 a[210:211], v4 offset:22528\n"
      "v_mfma_f32_32x32x16_bf16 v[32:47], a[220:223], v[60:63], v[32:47]\n"
      "ds_read_b64_tr_b16 a[212:213], v4 offset:23552\n"
      "ds_read_b64_tr_b16 a[214:215], v4 offset:24576\n"
      "s_waitcnt vmcnt(7)\n"
      "v_mfma_f32_32x32x16_bf16 v[160:175], a[224:227], v[80:83], v[160:175]\n"
      "ds_read_b64_tr_b16 a[216:217], v4 offset:25088\n"
      "ds_read_b64_tr_b16 a[218:219], v4 offset:26112\n"
      "v_mfma_f32_32x32x16_bf16 v[32:47], a[228:231], v[64:67], v[32:47]\n"
      "ds_read_b64_tr_b16 a[220:221], v4 offset:27136\n"
      "ds_read_b64_tr_b16 a[222:223], v4 offset:28160\n"
      "s_waitcnt vmcnt(6)\n"
      "v_mfma_f32_32x32x16_bf16 v[160:175], a[232:235], v[84:87], v[160:175]\n"
      "v_mfma_f32_32x32x16_bf16 v[32:47], a[236:239], v[68:71], v[32:47]\n"
      "s_waitcnt vmcnt(5)\n"
      "v_mfma_f32_32x32x16_bf16 v[160:175], a[240:243], v[88:91], v[160:175]\n"
      "v_mfma_f32_32x32x16_bf16 v[32:47], a[244:247], v[72:75], v[32:47]\n"
      "s_waitcnt vmcnt(4) lgkmcnt(15)\n"
      "v_mfma_f32_32x32x16_bf16 v[160:175], a[248:251], v[92:95], v[160:175]\n"
      "v_mfma_f32_32x32x16_bf16 v[32:47], a[252:255], v[76:79], v[32:47]\n"
      "s_waitcnt vmcnt(3)\n"
      "v_mfma_f32_32x32x16_bf16 v[160:175], a[176:179], v[96:99], v[160:175]\n"
      "v_mfma_f32_32x32x16_bf16 v[32:47], a[180:183], v[80:83], v[32:47]\n"
      "s_waitcnt vmcnt(2)\n"
      "v_mfma_f32_32x32x16_bf16 v[160:175], a[184:187], v[100:103], v[160:175]\n"
      "v_mfma_f32_32x32x16_bf16 v[32:47], a[188:191], v[84:87], v[32:47]\n"
      "s_waitcnt vmcnt(1) lgkmcnt(12)\n"
      "v_mfma_f32_32x32x16_bf16 v[160:175], a[192:195], v[104:107], v[160:175]\n"
      "v_mfma_f32_32x32x16_bf16 v[32:47], a[196:199], v[88:91], v[32:47]\n"
      "s_waitcnt vmcnt(0) lgkmcnt(8)\n"
      "v_mfma_f32_32x32x16_bf16 v[160:175], a[200:203], v[108:111], v[160:175]\n"
      "v_mfma_f32_32x32x16_bf16 v[32:47], a[204:207], v[92:95], v[32:47]\n"
      "; LATE flag: every wave drained its stores in front of the barrier just passed\n"
      "s_cmp_lg_u32 s14, 1\n"
      "s_cbranch_scc1 L_late_20\n"
      "s_add_u32 s34, s16, 3\n"
      "v_mov_b32 v28, s34\n"
      "v_mov_b32 v29, s17\n"
      "buffer_atomic_swap_x2 v[28:29], v6, s[4:7], s80 offen offset:128\n"
      "L_late_20:\n"
      "s_waitcnt lgkmcnt(4)\n"
      "v_mfma_f32_32x32x16_bf16 v[32:47], a[208:211], v[96:99], v[32:47]\n"
      "v_cmp_class_f32 vcc, v160, v222\n"
      "s_cbranch_vccnz L_OA_BAD\n"
      "L_OA_OK:\n"
      "s_waitcnt lgkmcnt(0)\n"
      "s_barrier\n"
      "v_mfma_f32_32x32x16_bf16 v[32:47], a[212:215], v[100:103], v[32:47]\n"
      "ds_write_b128 v217, v[160:163] offset:0\n"
      "ds_write_b128 v217, v[164:167] offset:1024\n"
      "ds_write_b128 v217, v[168:171] offset:2048\n"
      "ds_write_b128 v217, v[172:175] offset:3072\n"
      "v_mfma_f32_32x32x16_bf16 v[32:47], a[216:219], v[104:107], v[32:47]\n"
      "v_mfma_f32_32x32x16_bf16 v[32:47], a[220:223], v[108:111], v[32:47]\n"
      "s_waitcnt lgkmcnt(0)\n"
      "s_barrier\n"
      "ds_read_b128 v[176:179], v27 offset:0\n"
      "ds_read_b128 v[180:183], v27 offset:1024\n"
      "ds_read_b128 v[184:187], v27 offset:2048\n"
      "ds_read_b128 v[188:191], v27 offset:3072\n"
      "s_nop 4\n"
      "s_waitcnt lgkmcnt(2)\n"
      "v_pk_add_f32 v[32:33], v[32:33], v[176:177]\n"
      "v_pk_add_f32 v[34:35], v[34:35], v[178:179]\n"
      "v_pk_add_f32 v[36:37], v[36:37], v[180:181]\n"
      "v_pk_add_f32 v[38:39], v[38:39], v[182:183]\n"
      "v_cvt_pk_bf16_f32 v248, v32, v33\n"
      "v_cvt_pk_bf16_f32 v249, v34, v35\n"
      "v_cvt_pk_bf16_f32 v250, v36, v37\n"
      "v_cvt_pk_bf16_f32 v251, v38, v39\n"
      "s_waitcnt lgkmcnt(0)\n"
      "v_pk_add_f32 v[40:41], v[40:41], v[184:185]\n"
      "v_pk_add_f32 v[42:43], v[42:43], v[186:187]\n"
      "v_pk_add_f32 v[44:45], v[44:45], v[188:189]\n"
      "v_pk_add_f32 v[46:47], v[46:47], v[190:191]\n"
      "v_cvt_pk_bf16_f32 v252, v40, v41\n"
      "v_cvt_pk_bf16_f32 v253, v42, v43\n"
      "v_cvt_pk_bf16_f32 v254, v44, v45\n"
      "v_cvt_pk_bf16_f32 v255, v46, v47\n"
      "buffer_store_dwordx4 v[248:251], v3, s[4:7], s43 offen sc1\n"
      "buffer_store_dwordx4 v[252:255], v3, s[4:7], s43 offen offset:1024 sc1\n"
      "; EARLY flag: every wave has issued its stores; wave 0 pushes the tag into the 16 consumers' lines (consumers validate what they read)\n"
      "s_add_u32 s34, s16, 4\n"
      "v_mov_b32 v28, s34\n"
      "v_mov_b32 v29, s17\n"
      "s_cmp_lg_u32 s14, 0\n"
      "s_barrier\n"
      "s_cbranch_scc1 L_sig_21\n"
      "buffer_atomic_swap_x2 v[28:29], v6, s[4:7], s80 offen\n"
      "L_sig_21:\n"
      "; partial row sums of the new x slice\n"
      "v_pk_add_f32 v[160:161], v[32:33], v[40:41]\n"
      "v_pk_add_f32 v[162:163], v[34:35], v[42:43]\n"
      "v_pk_add_f32 v[164:165], v[36:37], v[44:45]\n"
      "v_pk_add_f32 v[166:167], v[38:39], v[46:47]\n"
      "v_pk_mul_f32 v[168:169], v[32:33], v[32:33]\n"
      "v_pk_mul_f32 v[170:171], v[34:35], v[34:35]\n"
      "v_pk_add_f32 v[160:161], v[160:161], v[164:165]\n"
      "v_pk_add_f32 v[162:163], v[162:163], v[166:167]\n"
      "v_pk_fma_f32 v[168:169], v[36:37], v[36:37], v[168:169]\n"
      "v_pk_fma_f32 v[170:171], v[38:39], v[38:39], v[170:171]\n"
      "v_pk_fma_f32 v[168:169], v[40:41], v[40:41], v[168:169]\n"
      "v_pk_fma_f32 v[170:171], v[42:43], v[42:43], v[170:171]\n"
      "v_pk_fma_f32 v[168:169], v[44:45], v[44:45], v[168:169]\n"
      "v_pk_fma_f32 v[170:171], v[46:47], v[46:47], v[170:171]\n"
      "v_pk_add_f32 v[160:161], v[160:161], v[162:163]\n"
      "v_pk_add_f32 v[168:169], v[168:169], v[170:171]\n"
      "v_add_f32 v160, v160, v161\n"
      "v_add_f32 v168, v168, v169\n"
      "s_add_u32 s43, s43, 1835008\n"
      "s_nop 0\n"
      "v_permlane32_swap_b32 v160, v168\n"
      "v_add_f32 v160, v160, v168\n"
      "buffer_store_dword v160, v9, s[4:7], s47 offen sc1\n"
      "s_add_u32 s47, s47, 262144\n"
      "; ================ softmax attention layer ================\n"
      "s_add_u32 s52, s42, 0\n"
      "s_add_u32 s53, s42, 4096\n"
      "s_add_u32 s54, s42, 8192\n"
      "s_add_u32 s55, s42, 12288\n"
      "s_add_u32 s56, s42, 16384\n"
      "s_add_u32 s57, s42, 20480\n"
      "s_add_u32 s58, s42, 24576\n"
      "s_add_u32 s42, s42, 1835008\n"
      "s_mov_b32 s1, 0\n"
      "L_XB_RE:\n"
      "ds_read_b64_tr_b16 a[176:177], v1 offset:25088\n"
      "ds_read_b64_tr_b16 a[178:179], v1 offset:25312\n"
      "ds_read_b64_tr_b16 a[180:181], v1 offset:25984\n"
      "ds_read_b64_tr_b16 a[182:183], v1 offset:26208\n"
      "ds_read_b64_tr_b16 a[184:185], v1 offset:26880\n"
      "ds_read_b64_tr_b16 a[186:187], v1 offset:27104\n"
      "ds_read_b64_tr_b16 a[188:189], v1 offset:27776\n"
      "ds_read_b64_tr_b16 a[190:191], v1 offset:28000\n"
      "ds_read_b64_tr_b16 a[192:193], v1 offset:0\n"
      "ds_read_b64_tr_b16 a[194:195], v1 offset:224\n"
      "ds_read_b64_tr_b16 a[196:197], v1 offset:28672\n"
      "ds_read_b64_tr_b16 a[198:199], v1 offset:28896\n"
      "ds_read_b64_tr_b16 a[200:201], v1 offset:896\n"
      "ds_read_b64_tr_b16 a[202:203], v1 offset:1120\n"
      "ds_read_b64_tr_b16 a[204:205], v1 offset:29568\n"
      "ds_read_b64_tr_b16 a[206:207], v1 offset:29792\n"
      "ds_read_b64_tr_b16 a[208:209], v1 offset:1792\n"
      "ds_read_b64_tr_b16 a[210:211], v1 offset:2016\n"
      "ds_read_b64_tr_b16 a[212:213], v1 offset:30464\n"
      "ds_read_b64_tr_b16 a[214:215], v1 offset:30688\n"
      "ds_read_b64_tr_b16 a[216:217], v1 offset:2688\n"
      "ds_read_b64_tr_b16 a[218:219], v1 offset:2912\n"
      "ds_read_b64_tr_b16 a[220:221], v1 offset:31360\n"
      "ds_read_b64_tr_b16 a[222:223], v1 offset:31584\n"
      "ds_read_b64_tr_b16 a[224:225], v1 offset:3584\n"
      "ds_read_b64_tr_b16 a[226:227], v1 offset:3808\n"
      "ds_read_b64_tr_b16 a[228:229], v1 offset:32256\n"
      "ds_read_b64_tr_b16 a[230:231], v1 offset:32480\n"
      "ds_read_b64_tr_b16 a[232:233], v1 offset:4480\n"
      "ds_read_b64_tr_b16 a[234:235], v1 offset:4704\n"
      "ds_read_b64_tr_b16 a[236:237], v1 offset:33152\n"
      "ds_read_b64_tr_b16 a[238:239], v1 offset:33376\n"
      "ds_read_b64_tr_b16 a[240:241], v1 offset:5376\n"
      "ds_read_b64_tr_b16 a[242:243], v1 offset:5600\n"
      "ds_read_b64_tr_b16 a[244:245], v1 offset:34048\n"
      "ds_read_b64_tr_b16 a[246:247], v1 offset:34272\n"
      "s_cmp_lg_u32 s1, 0\n"
      "s_waitcnt lgkmcnt(0)\n"
      "s_cbranch_scc1 L_XB_GO\n"
      "; hand-off: wave 3 polls its flags, barrier\n"
      "s_add_u32 s34, s16, 4\n"
      "s_cmp_lg_u32 s14, 3\n"
      "s_cbranch_scc1 L_pd_23\n"
      "s_mov_b32 s51, 0x40000\n"
      "L_poll_22:\n"
      "s_mov_b64 exec, 0xffff\n"
      "v_mov_b64 v[28:29], 0\n"
      "buffer_atomic_or_x2 v[28:29], v7, s[4:7], s82 offen sc0\n"
      "s_sub_u32 s51, s51, 1\n"
      "s_cbranch_scc1 L_pd_23\n"
      "L_pc_24:\n"
      "s_waitcnt vmcnt(0)\n"
      "v_subrev_u32 v28, s34, v28\n"
      "v_cmp_eq_u32 s[40:41], s17, v29\n"
      "v_cmp_gt_u32 vcc, 64, v28\n"
      "s_and_b64 vcc, vcc, s[40:41]\n"
      "s_cmp_eq_u64 vcc, exec\n"
      "s_cbranch_scc0 L_poll_22\n"
      "L_pd_23:\n"
      "s_mov_b64 exec, -1\n"
      "s_waitcnt vmcnt(0) lgkmcnt(0)\n"
      "s_barrier\n"
      "L_XB_GO:\n"
      "; first requests, paced by the LDS stores of the Wq image so that the four waves interleave\n"
      "buffer_load_dwordx4 v[48:51], v0, s[4:7], s52 offen sc1\n"
      "buffer_load_dwordx4 v[52:55], v0, s[4:7], s52 offen offset:1024 sc1\n"
      "v_add_u32 v223, 100352, v13\n"
      "ds_write_b128 v223, a[28:31] offset:0\n"
      "buffer_load_dwordx4 v[56:59], v0, s[4:7], s52 offen offset:2048 sc1\n"
      "buffer_load_dwordx4 v[60:63], v0, s[4:7], s52 offen offset:3072 sc1\n"
      "ds_write_b128 v223, a[32:35] offset:1024\n"
      "buffer_load_dwordx4 v[64:67], v0, s[4:7], s53 offen sc1\n"
      "buffer_load_dwordx4 v[68:71], v0, s[4:7], s53 offen offset:1024 sc1\n"
      "ds_write_b128 v223, a[36:39] offset:2048\n"
      "buffer_load_dwordx4 v[72:75], v0, s[4:7], s53 offen offset:2048 sc1\n"
      "buffer_load_dwordx4 v[76:79], v0, s[4:7], s53 offen offset:3072 sc1\n"
      "ds_write_b128 v223, a[40:43] offset:3072\n"
      "buffer_load_dwordx4 v[80:83], v0, s[4:7], s54 offen sc1\n"
      "buffer_load_dwordx4 v[84:87], v0, s[4:7], s54 offen offset:1024 sc1\n"
      "ds_write_b128 v223, a[44:47] offset:4096\n"
      "buffer_load_dwordx4 v[88:91], v0, s[4:7], s54 offen offset:2048 sc1\n"
      "buffer_load_dwordx4 v[92:95], v0, s[4:7], s54 offen offset:3072 sc1\n"
      "ds_write_b128 v223, a[48:51] offset:5120\n"
      "s_mov_b64 exec, 0x00ff0000\n"
      "ds_write_b128 v14, a[52:55] offset:50176\n"
      "s_mov_b64 exec, -1\n"
      "s_waitcnt vmcnt(11)\n"
      "v_mfma_f32_32x32x16_bf16 v[160:175], a[176:179], v[48:51], 0\n"
      "ds_read_b64_tr_b16 a[248:249], v1 offset:6272\n"
      "ds_read_b64_tr_b16 a[250:251], v1 offset:6496\n"
      "buffer_load_dwordx4 v[96:99], v0, s[4:7], s55 offen sc1\n"
      "s_waitcnt vmcnt(11)\n"
      "v_mfma_f32_32x32x16_bf16 v[160:175], a[180:183], v[52:55], v[160:175]\n"
      "ds_read_b64_tr_b16 a[252:253], v1 offset:34944\n"
      "ds_read_b64_tr_b16 a[254:255], v1 offset:35168\n"
      "buffer_load_dwordx4 v[100:103], v0, s[4:7], s55 offen offset:1024 sc1\n"
      "s_waitcnt vmcnt(11)\n"
      "v_mfma_f32_32x32x16_bf16 v[160:175], a[184:187], v[56:59], v[160:175]\n"
      "ds_read_b64_tr_b16 a[176:177], v1 offset:7168\n"
      "ds_read_b64_tr_b16 a[178:179], v1 offset:7392\n"
      "buffer_load_dwordx4 v[104:107], v0, s[4:7], s55 offen offset:2048 sc1\n"
      "s_waitcnt vmcnt(11)\n"
      "v_mfma_f32_32x32x16_bf16 v[160:175], a[188:191], v[60:63], v[160:175]\n"
      "ds_read_b64_tr_b16 a[180:181], v1 offset:35840\n"
      "ds_read_b64_tr_b16 a[182:183], v1 offset:36064\n"
      "buffer_load_dwordx4 v[108:111], v0, s[4:7], s55 offen offset:3072 sc1\n"
      "v_mfma_f32_32x32x16_bf16 v[176:191], v[48:51], a[192:195], 0\n"
      "ds_read_b64_tr_b16 a[184:185], v1 offset:8064\n"
      "ds_read_b64_tr_b16 a[186:187], v1 offset:8288\n"
      "s_waitcnt vmcnt(11)\n"
      "v_mfma_f32_32x32x16_bf16 v[160:175], a[196:199], v[64:67], v[160:175]\n"
      "ds_read_b64_tr_b16 a[188:189], v1 offset:36736\n"
      "ds_read_b64_tr_b16 a[190:191], v1 offset:36960\n"
      "buffer_load_dwordx4 v[240:243], v0, s[4:7], s46 offen sc1\n"
      "v_mfma_f32_32x32x16_bf16 v[176:191], v[52:55], a[200:203], v[176:191]\n"
      "ds_read_b64_tr_b16 a[192:193], v1 offset:8960\n"
      "ds_read_b64_tr_b16 a[194:195], v1 offset:9184\n"
      "s_waitcnt vmcnt(11)\n"
      "v_mfma_f32_32x32x16_bf16 v[160:175], a[204:207], v[68:71], v[160:175]\n"
      "ds_read_b64_tr_b16 a[196:197], v1 offset:37632\n"
      "ds_read_b64_tr_b16 a[198:199], v1 offset:37856\n"
      "buffer_load_dwordx4 v[244:247], v0, s[4:7], s46 offen offset:1024 sc1\n"
      "v_mfma_f32_32x32x16_bf16 v[176:191], v[56:59], a[208:211], v[176:191]\n"
      "ds_read_b64_tr_b16 a[200:201], v1 offset:9856\n"
      "ds_read_b64_tr_b16 a[202:203], v1 offset:10080\n"
      "s_waitcnt vmcnt(11)\n"
      "v_mfma_f32_32x32x16_bf16 v[160:175], a[212:215], v[72:75], v[160:175]\n"
      "ds_read_b64_tr_b16 a[204:205], v1 offset:38528\n"
      "ds_read_b64_tr_b16 a[206:207], v1 offset:38752\n"
      "buffer_load_dwordx4 v[248:251], v0, s[4:7], s46 offen offset:2048 sc1\n"
      "v_mfma_f32_32x32x16_bf16 v[176:191], v[60:63], a[216:219], v[176:191]\n"
      "ds_read_b64_tr_b16 a[208:209], v1 offset:10752\n"
      "ds_read_b64_tr_b16 a[210:211], v1 offset:10976\n"
      "s_waitcnt vmcnt(11)\n"
      "v_mfma_f32_32x32x16_bf16 v[160:175], a[220:223], v[76:79], v[160:175]\n"
      "ds_read_b64_tr_b16 a[212:213], v1 offset:39424\n"
      "ds_read_b64_tr_b16 a[214:215], v1 offset:39648\n"
      "buffer_load_dwordx4 v[252:255], v216, s[4:7], s46 offen offset:3072 sc1\n"
      "v_mfma_f32_32x32x16_bf16 v[176:191], v[64:67], a[224:227], v[176:191]\n"
      "ds_read_b64_tr_b16 a[216:217], v1 offset:11648\n"
      "ds_read_b64_tr_b16 a[218:219], v1 offset:11872\n"
      "s_waitcnt vmcnt(11)\n"
      "v_mfma_f32_32x32x16_bf16 v[160:175], a[228:231], v[80:83], v[160:175]\n"
      "ds_read_b64_tr_b16 a[220:221], v1 offset:40320\n"
      "ds_read_b64_tr_b16 a[222:223], v1 offset:40544\n"
      "buffer_load_dwordx4 v[112:115], v0, s[4:7], s56 offen sc1\n"
      "v_mfma_f32_32x32x16_bf16 v[176:191], v[68:71], a[232:235], v[176:191]\n"
      "ds_read_b64_tr_b16 a[224:225], v1 offset:12544\n"
      "ds_read_b64_tr_b16 a[226:227], v1 offset:12768\n"
      "s_waitcnt vmcnt(11)\n"
      "v_mfma_f32_32x32x16_bf16 v[160:175], a[236:239], v[84:87], v[160:175]\n"
      "ds_read_b64_tr_b16 a[228:229], v1 offset:41216\n"
      "ds_read_b64_tr_b16 a[230:231], v1 offset:41440\n"
      "buffer_load_dwordx4 v[116:119], v0, s[4:7], s56 offen offset:1024 sc1\n"
      "v_mfma_f32_32x32x16_bf16 v[176:191], v[72:75], a[240:243], v[176:191]\n"
      "ds_read_b64_tr_b16 a[232:233], v1 offset:13440\n"
      "ds_read_b64_tr_b16 a[234:235], v1 offset:13664\n"
      "s_waitcnt vmcnt(11) lgkmcnt(15)\n"
      "v_mfma_f32_32x32x16_bf16 v[160:175], a[244:247], v[88:91], v[160:175]\n"
      "ds_read_b64_tr_b16 a[236:237], v1 offset:42112\n"
      "ds_read_b64_tr_b16 a[238:239], v1 offset:42336\n"
      "buffer_load_dwordx4 v[120:123], v0, s[4:7], s56 offen offset:2048 sc1\n"
      "v_mfma_f32_32x32x16_bf16 v[176:191], v[76:79], a[248:251], v[176:191]\n"
      "ds_read_b64_tr_b16 a[240:241], v1 offset:14336\n"
      "ds_read_b64_tr_b16 a[242:243], v1 offset:14560\n"
      "s_waitcnt vmcnt(11)\n"
      "v_mfma_f32_32x32x16_bf16 v[160:175], a[252:255], v[92:95], v[160:175]\n"
      "ds_read_b64_tr_b16 a[244:245], v1 offset:43008\n"
      "ds_read_b64_tr_b16 a[246:247], v1 offset:43232\n"
      "buffer_load_dwordx4 v[124:127], v0, s[4:7], s56 offen offset:3072 sc1\n"
      "v_mfma_f32_32x32x16_bf16 v[176:191], v[80:83], a[176:179], v[176:191]\n"
      "ds_read_b64_tr_b16 a[248:249], v1 offset:15232\n"
      "ds_read_b64_tr_b16 a[250:251], v1 offset:15456\n"
      "s_waitcnt vmcnt(11)\n"
      "v_mfma_f32_32x32x16_bf16 v[160:175], a[180:183], v[96:99], v[160:175]\n"
      "ds_read_b64_tr_b16 a[252:253], v1 offset:43904\n"
      "ds_read_b64_tr_b16 a[254:255], v1 offset:44128\n"
      "buffer_load_dwordx4 v[128:131], v0, s[4:7], s57 offen sc1\n"
      "v_mfma_f32_32x32x16_bf16 v[176:191], v[84:87], a[184:187], v[176:191]\n"
      "ds_read_b64_tr_b16 a[176:177], v1 offset:16128\n"
      "ds_read_b64_tr_b16 a[178:179], v1 offset:16352\n"
      "s_waitcnt vmcnt(11)\n"
      "v_mfma_f32_32x32x16_bf16 v[160:175], a[188:191], v[100:103], v[160:175]\n"
      "ds_read_b64_tr_b16 a[180:181], v1 offset:44800\n"
      "ds_read_b64_tr_b16 a[182:183], v1 offset:45024\n"
      "buffer_load_dwordx4 v[132:135], v0, s[4:7], s57 offen offset:1024 sc1\n"
      "v_mfma_f32_32x32x16_bf16 v[176:191], v[88:91], a[192:195], v[176:191]\n"
      "ds_read_b64_tr_b16 a[184:185], v1 offset:17024\n"
      "ds_read_b64_tr_b16 a[186:187], v1 offset:17248\n"
      "s_waitcnt vmcnt(11)\n"
      "v_mfma_f32_32x32x16_bf16 v[160:175], a[196:199], v[104:107], v[160:175]\n"
      "ds_read_b64_tr_b16 a[188:189], v1 offset:45696\n"
      "ds_read_b64_tr_b16 a[190:191], v1 offset:45920\n"
      "buffer_load_dwordx4 v[136:139], v0, s[4:7], s57 offen offset:2048 sc1\n"
      "v_mfma_f32_32x32x16_bf16 v[176:191], v[92:95], a[200:203], v[176:191]\n"
      "ds_read_b64_tr_b16 a[192:193], v1 offset:17920\n"
      "ds_read_b64_tr_b16 a[194:195], v1 offset:18144\n"
      "s_waitcnt vmcnt(11) lgkmcnt(15)\n"
      "v_mfma_f32_32x32x16_bf16 v[160:175], a[204:207], v[108:111], v[160:175]\n"
      "ds_read_b64_tr_b16 a[196:197], v1 offset:46592\n"
      "ds_read_b64_tr_b16 a[198:199], v1 offset:46816\n"
      "buffer_load_dwordx4 v[140:143], v0, s[4:7], s57 offen offset:3072 sc1\n"
      "v_mfma_f32_32x32x16_bf16 v[176:191], v[96:99], a[208:211], v[176:191]\n"
      "ds_read_b64_tr_b16 a[200:201], v1 offset:18816\n"
      "ds_read_b64_tr_b16 a[202:203], v1 offset:19040\n"
      "s_waitcnt vmcnt(8)\n"
      "v_add_f32 v240, v240, v248\n"
      "s_waitcnt vmcnt(7)\n"
      "v_mfma_f32_32x32x16_bf16 v[160:175], a[212:215], v[112:115], v[160:175]\n"
      "ds_read_b64_tr_b16 a[204:205], v1 offset:47488\n"
      "ds_read_b64_tr_b16 a[206:207], v1 offset:47712\n"
      "buffer_load_dwordx4 v[144:147], v0, s[4:7], s58 offen sc1\n"
      "v_mfma_f32_32x32x16_bf16 v[176:191], v[100:103], a[216:219], v[176:191]\n"
      "ds_read_b64_tr_b16 a[208:209], v1 offset:19712\n"
      "ds_read_b64_tr_b16 a[210:211], v1 offset:19936\n"
      "v_add_f32 v241, v241, v249\n"
      "v_add_f32 v242, v242, v250\n"
      "s_waitcnt vmcnt(7)\n"
      "v_mfma_f32_32x32x16_bf16 v[160:175], a[220:223], v[116:119], v[160:175]\n"
      "ds_read_b64_tr_b16 a[212:213], v1 offset:48384\n"
      "ds_read_b64_tr_b16 a[214:215], v1 offset:48608\n"
      "buffer_load_dwordx4 v[148:151], v0, s[4:7], s58 offen offset:1024 sc1\n"
      "v_mfma_f32_32x32x16_bf16 v[176:191], v[104:107], a[224:227], v[176:191]\n"
      "ds_read_b64_tr_b16 a[216:217], v1 offset:20608\n"
      "ds_read_b64_tr_b16 a[218:219], v1 offset:20832\n"
      "v_add_f32 v243, v243, v251\n"
      "v_add_f32 v244, v244, v252\n"
      "s_waitcnt vmcnt(7)\n"
      "v_mfma_f32_32x32x16_bf16 v[160:175], a[228:231], v[120:123], v[160:175]\n"
      "ds_read_b64_tr_b16 a[220:221], v1 offset:49280\n"
      "ds_read_b64_tr_b16 a[222:223], v1 offset:49504\n"
      "buffer_load_dwordx4 v[152:155], v0, s[4:7], s58 offen offset:2048 sc1\n"
      "v_mfma_f32_32x32x16_bf16 v[176:191], v[108:111], a[232:235], v[176:191]\n"
      "ds_read_b64_tr_b16 a[224:225], v1 offset:21504\n"
      "ds_read_b64_tr_b16 a[226:227], v1 offset:21728\n"
      "v_add_f32 v245, v245, v253\n"
      "v_add_f32 v246, v246, v254\n"
      "s_waitcnt vmcnt(7)\n"
      "v_mfma_f32_32x32x16_bf16 v[160:175], a[236:239], v[124:127], v[160:175]\n"
      "ds_read_b64_tr_b16 a[228:229], v1 offset:22400\n"
      "ds_read_b64_tr_b16 a[230:231], v1 offset:22624\n"
      "buffer_load_dwordx4 v[156:159], v0, s[4:7], s58 offen offset:3072 sc1\n"
      "v_mfma_f32_32x32x16_bf16 v[176:191], v[112:115], a[240:243], v[176:191]\n"
      "ds_read_b64_tr_b16 a[232:233], v1 offset:23296\n"
      "ds_read_b64_tr_b16 a[234:235], v1 offset:23520\n"
      "v_add_f32 v247, v247, v255\n"
      "v_add_f32 v240, v240, v244\n"
      "s_waitcnt vmcnt(7) lgkmcnt(15)\n"
      "v_mfma_f32_32x32x16_bf16 v[160:175], a[244:247], v[128:131], v[160:175]\n"
      "ds_read_b64_tr_b16 a[236:237], v1 offset:24192\n"
      "ds_read_b64_tr_b16 a[238:239], v1 offset:24416\n"
      "v_add_f32 v241, v241, v245\n"
      "v_add_f32 v242, v242, v246\n"
      "v_mfma_f32_32x32x16_bf16 v[176:191], v[116:119], a[248:251], v[176:191]\n"
      "v_add_f32 v243, v243, v247\n"
      "v_mov_b64 v[208:209], 0\n"
      "v_mov_b64 v[212:213], 0\n"
      "v_permlane32_swap_b32 v240, v242\n"
      "s_waitcnt vmcnt(6)\n"
      "v_mfma_f32_32x32x16_bf16 v[160:175], a[252:255], v[132:135], v[160:175]\n"
      "v_permlane32_swap_b32 v241, v243\n"
      "v_add_f32 v240, v240, v242\n"
      "v_add_f32 v241, v241, v243\n"
      "v_mov_b32 v211, 0\n"
      "v_mfma_f32_32x32x16_bf16 v[176:191], v[120:123], a[176:179], v[176:191]\n"
      "v_mov_b32 v214, 0\n"
      "v_permlane16_swap_b32 v240, v241\n"
      "v_add_f32 v240, v240, v241\n"
      "ds_write_b32 v8, v240\n"
      "s_waitcnt vmcnt(5)\n"
      "v_mfma_f32_32x32x16_bf16 v[160:175], a[180:183], v[136:139], v[160:175]\n"
      "ds_read2_b32 v[240:241], v11 offset0:32 offset1:64\n"
      "v_mfma_f32_32x32x16_bf16 v[176:191], v[124:127], a[184:187], v[176:191]\n"
      "s_waitcnt vmcnt(4)\n"
      "v_mfma_f32_32x32x16_bf16 v[160:175], a[188:191], v[140:143], v[160:175]\n"
      "s_waitcnt lgkmcnt(0)\n"
      "v_mfma_f32_32x32x16_bf16 v[176:191], v[128:131], a[192:195], v[176:191]\n"
      "v_mul_f32 v240, 0xbb124925, v240\n"
      "v_mul_f32 v241, 0x3b124925, v241\n"
      "v_fma_f32 v240, v241, 0, v240\n"
      "v_fma_f32 v241, -v240, v240, v241\n"
      "s_waitcnt vmcnt(3)\n"
      "v_mfma_f32_32x32x16_bf16 v[160:175], a[196:199], v[144:147], v[160:175]\n"
      "v_max_f32 v241, 0, v241\n"
      "v_add_f32 v241, 0x3727c5ac, v241\n"
      "v_rsq_f32 v24, v241\n"
      "v_cvt_pk_bf16_f32 v242, v240, v240\n"
      "v_mfma_f32_32x32x16_bf16 v[176:191], v[132:135], a[200:203], v[176:191]\n"
      "ds_write_b32 v11, v24\n"
      "v_and_b32 v242, 0xffff0000, v242\n"
      "v_sub_f32 v242, v240, v242\n"
      "v_cvt_pk_bf16_f32 v210, v240, v242\n"
      "s_waitcnt vmcnt(2)\n"
      "v_mfma_f32_32x32x16_bf16 v[160:175], a[204:207], v[148:151], v[160:175]\n"
      "v_mov_b32 v215, v210\n"
      "ds_read_b128 v[192:195], v12 offset:0\n"
      "ds_read_b128 v[196:199], v12 offset:32\n"
      "ds_read_b128 v[200:203], v12 offset:64\n"
      "v_mfma_f32_32x32x16_bf16 v[176:191], v[136:139], a[208:211], v[176:191]\n"
      "ds_read_b128 v[204:207], v12 offset:96\n"
      "v_mul_f32 v25, v24, v23\n"
      "s_add_u32 s46, s46, 262144\n"
      "s_waitcnt vmcnt(1)\n"
      "v_mfma_f32_32x32x16_bf16 v[160:175], a[212:215], v[152:155], v[160:175]\n"
      "v_mfma_f32_32x32x16_bf16 v[176:191], v[140:143], a[216:219], v[176:191]\n"
      "s_waitcnt vmcnt(0)\n"
      "v_mfma_f32_32x32x16_bf16 v[160:175], a[220:223], v[156:159], v[160:175]\n"
      "v_mfma_f32_32x32x16_bf16 v[176:191], v[144:147], a[224:227], v[176:191]\n"
      "; LATE flag: every wave drained its stores in front of the barrier just passed\n"
      "s_cmp_lg_u32 s14, 1\n"
      "s_cbranch_scc1 L_late_25\n"
      "s_add_u32 s34, s16, 4\n"
      "v_mov_b32 v28, s34\n"
      "v_mov_b32 v29, s17\n"
      "buffer_atomic_swap_x2 v[28:29], v6, s[4:7], s80 offen offset:128\n"
      "L_late_25:\n"
      "v_mfma_f32_32x32x16_bf16 v[160:175], a[172:175], v[210:213], v[160:175]\n"
      "v_mfma_f32_32x32x16_bf16 v[176:191], v[212:215], a[172:175], v[176:191]\n"
      "s_add_u32 s35, s48, s79\n"
      "v_mfma_f32_32x32x16_bf16 v[176:191], v[148:151], a[228:231], v[176:191]\n"
      "v_cmp_u_f32 vcc, v160, v160\n"
      "s_cbranch_vccnz L_XB_BAD\n"
      "L_XB_OK:\n"
      "v_mul_f32 v160, v160, v24\n"
      "v_mul_f32 v161, v161, v24\n"
      "v_mul_f32 v162, v162, v24\n"
      "v_mul_f32 v163, v163, v24\n"
      "v_mul_f32 v164, v164, v24\n"
      "v_mfma_f32_32x32x16_bf16 v[176:191], v[152:155], a[232:235], v[176:191]\n"
      "v_mul_f32 v165, v165, v24\n"
      "v_mul_f32 v166, v166, v24\n"
      "v_mul_f32 v167, v167, v24\n"
      "v_mul_f32 v168, v168, v24\n"
      "v_mul_f32 v169, v169, v24\n"
      "v_mul_f32 v170, v170, v24\n"
      "v_mfma_f32_32x32x16_bf16 v[176:191], v[156:159], a[236:239], v[176:191]\n"
      "v_mul_f32 v171, v171, v24\n"
      "v_fma_f32 v172, v172, v25, v21\n"
      "v_fma_f32 v173, v173, v25, v21\n"
      "v_fma_f32 v174, v174, v25, v21\n"
      "v_fma_f32 v175, v175, v25, v21\n"
      "v_cvt_pk_bf16_f32 v240, v160, v161\n"
      "v_cvt_pk_bf16_f32 v241, v162, v163\n"
      "v_cvt_pk_bf16_f32 v242, v164, v165\n"
      "v_cvt_pk_bf16_f32 v243, v166, v167\n"
      "v_cvt_pk_bf16_f32 v244, v168, v169\n"
      "v_cvt_pk_bf16_f32 v245, v170, v171\n"
      "v_cvt_pk_bf16_f32 v246, v172, v173\n"
      "v_cvt_pk_bf16_f32 v247, v174, v175\n"
      "buffer_store_dwordx4 v[240:243], v10, s[4:7], s35 offen sc1\n"
      "buffer_store_dwordx4 v[244:247], v10, s[4:7], s35 offen offset:1024 sc1\n"
      "s_waitcnt lgkmcnt(0)\n"
      "v_pk_fma_f32 v[176:177], v[176:177], v[192:193], v[20:21] op_sel_hi:[1,1,0]\n"
      "v_pk_fma_f32 v[178:179], v[178:179], v[194:195], v[20:21] op_sel_hi:[1,1,0]\n"
      "v_pk_fma_f32 v[180:181], v[180:181], v[196:197], v[20:21] op_sel_hi:[1,1,0]\n"
      "v_pk_fma_f32 v[182:183], v[182:183], v[198:199], v[20:21] op_sel_hi:[1,1,0]\n"
      "v_pk_fma_f32 v[184:185], v[184:185], v[200:201], v[20:21] op_sel_hi:[1,1,0]\n"
      "v_pk_fma_f32 v[186:187], v[186:187], v[202:203], v[20:21] op_sel_hi:[1,1,0]\n"
      "v_pk_fma_f32 v[188:189], v[188:189], v[204:205], v[20:21] op_sel_hi:[1,1,0]\n"
      "v_pk_fma_f32 v[190:191], v[190:191], v[206:207], v[20:21] op_sel_hi:[1,1,0]\n"
      "v_cvt_pk_bf16_f32 v248, v176, v177\n"
      "v_cvt_pk_bf16_f32 v249, v178, v179\n"
      "v_cvt_pk_bf16_f32 v250, v180, v181\n"
      "v_cvt_pk_bf16_f32 v251, v182, v183\n"
      "buffer_store_dwordx4 v[248:251], v0, s[4:7], s35 offen offset:2048 sc1\n"
      "v_cvt_pk_bf16_f32 v252, v184, v185\n"
      "v_cvt_pk_bf16_f32 v253, v186, v187\n"
      "v_cvt_pk_bf16_f32 v254, v188, v189\n"
      "v_cvt_pk_bf16_f32 v255, v190, v191\n"
      "buffer_store_dwordx4 v[252:255], v0, s[4:7], s35 offen offset:3072 sc1\n"
      "s_waitcnt lgkmcnt(0)\n"
      "s_barrier\n"
      "ds_read_b64_tr_b16 a[176:177], v218 offset:0\n"
      "ds_read_b64_tr_b16 a[178:179], v218 offset:224\n"
      "ds_read_b64_tr_b16 a[180:181], v218 offset:896\n"
      "ds_read_b64_tr_b16 a[182:183], v218 offset:1120\n"
      "ds_read_b64_tr_b16 a[184:185], v218 offset:1792\n"
      "ds_read_b64_tr_b16 a[186:187], v218 offset:2016\n"
      "ds_read_b64_tr_b16 a[188:189], v218 offset:2688\n"
      "ds_read_b64_tr_b16 a[190:191], v218 offset:2912\n"
      "ds_read_b64_tr_b16 a[192:193], v218 offset:3584\n"
      "ds_read_b64_tr_b16 a[194:195], v218 offset:3808\n"
      "ds_read_b64_tr_b16 a[196:197], v218 offset:4480\n"
      "ds_read_b64_tr_b16 a[198:199], v218 offset:4704\n"
      "ds_read_b64_tr_b16 a[200:201], v218 offset:5376\n"
      "ds_read_b64_tr_b16 a[202:203], v218 offset:5600\n"
      "ds_read_b64_tr_b16 a[204:205], v218 offset:6272\n"
      "ds_read_b64_tr_b16 a[206:207], v218 offset:6496\n"
      "ds_read_b64_tr_b16 a[208:209], v218 offset:7168\n"
      "ds_read_b64_tr_b16 a[210:211], v218 offset:7392\n"
      "ds_read_b64_tr_b16 a[212:213], v218 offset:8064\n"
      "ds_read_b64_tr_b16 a[214:215], v218 offset:8288\n"
      "; Q projection in the shadow of the hand-off\n"
      "s_waitcnt lgkmcnt(15)\n"
      "v_mfma_f32_32x32x16_bf16 v[160:175], a[176:179], v[48:51], 0\n"
      "ds_read_b64_tr_b16 a[216:217], v218 offset:8960\n"
      "ds_read_b64_tr_b16 a[218:219], v218 offset:9184\n"
      "v_mul_f32 v24, s62, v24\n"
      "v_permlane32_swap_b32 v248, v250\n"
      "v_mfma_f32_32x32x16_bf16 v[160:175], a[180:183], v[52:55], v[160:175]\n"
      "ds_read_b64_tr_b16 a[220:221], v218 offset:9856\n"
      "ds_read_b64_tr_b16 a[222:223], v218 offset:10080\n"
      "v_permlane32_swap_b32 v249, v251\n"
      "v_permlane32_swap_b32 v252, v254\n"
      "; EARLY flag: every wave has issued its stores; wave 0 pushes the tag into the 16 consumers' lines (consumers validate what they read)\n"
      "s_add_u32 s34, s16, 5\n"
      "v_mov_b32 v28, s34\n"
      "v_mov_b32 v29, s17\n"
      "s_cmp_lg_u32 s14, 0\n"
      "s_barrier\n"
      "s_cbranch_scc1 L_sig_26\n"
      "buffer_atomic_swap_x2 v[28:29], v6, s[4:7], s81 offen\n"
      "L_sig_26:\n"
      "s_waitcnt lgkmcnt(15)\n"
      "v_mfma_f32_32x32x16_bf16 v[160:175], a[184:187], v[56:59], v[160:175]\n"
      "ds_read_b64_tr_b16 a[224:225], v218 offset:10752\n"
      "ds_read_b64_tr_b16 a[226:227], v218 offset:10976\n"
      "v_permlane32_swap_b32 v253, v255\n"
      "v_add_u32 v30, s48, v0\n"
      "v_mfma_f32_32x32x16_bf16 v[160:175], a[188:191], v[60:63], v[160:175]\n"
      "ds_read_b64_tr_b16 a[228:229], v218 offset:11648\n"
      "ds_read_b64_tr_b16 a[230:231], v218 offset:11872\n"
      "s_add_u32 s48, s48, 4194304\n"
      "s_mov_b32 s59, 0\n"
      "s_waitcnt lgkmcnt(15)\n"
      "v_mfma_f32_32x32x16_bf16 v[160:175], a[192:195], v[64:67], v[160:175]\n"
      "ds_read_b64_tr_b16 a[232:233], v218 offset:12544\n"
      "ds_read_b64_tr_b16 a[234:235], v218 offset:12768\n"
      "v_mov_b64 v[48:49], 0\n"
      "v_mov_b64 v[50:51], 0\n"
      "v_mfma_f32_32x32x16_bf16 v[160:175], a[196:199], v[68:71], v[160:175]\n"
      "ds_read_b64_tr_b16 a[236:237], v218 offset:13440\n"
      "ds_read_b64_tr_b16 a[238:239], v218 offset:13664\n"
      "v_mov_b64 v[52:53], 0\n"
      "v_mov_b64 v[54:55], 0\n"
      "s_waitcnt lgkmcnt(15)\n"
      "v_mfma_f32_32x32x16_bf16 v[160:175], a[200:203], v[72:75], v[160:175]\n"
      "ds_read_b64_tr_b16 a[240:241], v218 offset:14336\n"
      "ds_read_b64_tr_b16 a[242:243], v218 offset:14560\n"
      "v_mov_b64 v[56:57], 0\n"
      "v_mov_b64 v[58:59], 0\n"
      "v_mfma_f32_32x32x16_bf16 v[160:175], a[204:207], v[76:79], v[160:175]\n"
      "ds_read_b64_tr_b16 a[244:245], v218 offset:15232\n"
      "ds_read_b64_tr_b16 a[246:247], v218 offset:15456\n"
      "v_mov_b64 v[60:61], 0\n"
      "v_mov_b64 v[62:63], 0\n"
      "s_waitcnt lgkmcnt(15)\n"
      "v_mfma_f32_32x32x16_bf16 v[160:175], a[208:211], v[80:83], v[160:175]\n"
      "ds_read_b64_tr_b16 a[248:249], v218 offset:16128\n"
      "ds_read_b64_tr_b16 a[250:251], v218 offset:16352\n"
      "v_mov_b64 v[64:65], 0\n"
      "v_mov_b64 v[66:67], 0\n"
      "v_mfma_f32_32x32x16_bf16 v[160:175], a[212:215], v[84:87], v[160:175]\n"
      "ds_read_b64_tr_b16 a[252:253], v218 offset:17024\n"
      "ds_read_b64_tr_b16 a[254:255], v218 offset:17248\n"
      "v_mov_b64 v[68:69], 0\n"
      "v_mov_b64 v[70:71], 0\n"
      "s_waitcnt lgkmcnt(15)\n"
      "v_mfma_f32_32x32x16_bf16 v[160:175], a[216:219], v[88:91], v[160:175]\n"
      "ds_read_b64_tr_b16 a[176:177], v218 offset:17920\n"
      "ds_read_b64_tr_b16 a[178:179], v218 offset:18144\n"
      "v_mov_b64 v[72:73], 0\n"
      "v_mov_b64 v[74:75], 0\n"
      "v_mfma_f32_32x32x16_bf16 v[160:175], a[220:223], v[92:95], v[160:175]\n"
      "ds_read_b64_tr_b16 a[180:181], v218 offset:18816\n"
      "ds_read_b64_tr_b16 a[182:183], v218 offset:19040\n"
      "v_mov_b64 v[76:77], 0\n"
      "v_mov_b64 v[78:79], 0\n"
      "s_waitcnt lgkmcnt(15)\n"
      "v_mfma_f32_32x32x16_bf16 v[160:175], a[224:227], v[96:99], v[160:175]\n"
      "ds_read_b64_tr_b16 a[184:185], v218 offset:19712\n"
      "ds_read_b64_tr_b16 a[186:187], v218 offset:19936\n"
      "v_mov_b64 v[80:81], 0\n"
      "v_mov_b64 v[82:83], 0\n"
      "v_mfma_f32_32x32x16_bf16 v[160:175], a[228:231], v[100:103], v[160:175]\n"
      "ds_read_b64_tr_b16 a[188:189], v218 offset:20608\n"
      "ds_read_b64_tr_b16 a[190:191], v218 offset:20832\n"
      "v_mov_b64 v[84:85], 0\n"
      "v_mov_b64 v[86:87], 0\n"
      "s_waitcnt lgkmcnt(15)\n"
      "v_mfma_f32_32x32x16_bf16 v[160:175], a[232:235], v[104:107], v[160:175]\n"
      "ds_read_b64_tr_b16 a[192:193], v218 offset:21504\n"
      "ds_read_b64_tr_b16 a[194:195], v218 offset:21728\n"
      "v_mov_b64 v[88:89], 0\n"
      "v_mov_b64 v[90:91], 0\n"
      "v_mfma_f32_32x32x16_bf16 v[160:175], a[236:239], v[108:111], v[160:175]\n"
      "ds_read_b64_tr_b16 a[196:197], v218 offset:22400\n"
      "ds_read_b64_tr_b16 a[198:199], v218 offset:22624\n"
      "v_mov_b64 v[92:93], 0\n"
      "v_mov_b64 v[94:95], 0\n"
      "s_waitcnt lgkmcnt(15)\n"
      "v_mfma_f32_32x32x16_bf16 v[160:175], a[240:243], v[112:115], v[160:175]\n"
      "ds_read_b64_tr_b16 a[200:201], v218 offset:23296\n"
      "ds_read_b64_tr_b16 a[202:203], v218 offset:23520\n"
      "v_mov_b64 v[96:97], 0\n"
      "v_mov_b64 v[98:99], 0\n"
      "v_mfma_f32_32x32x16_bf16 v[160:175], a[244:247], v[116:119], v[160:175]\n"
      "ds_read_b64_tr_b16 a[204:205], v218 offset:24192\n"
      "ds_read_b64_tr_b16 a[206:207], v218 offset:24416\n"
      "v_mov_b64 v[100:101], 0\n"
      "v_mov_b64 v[102:103], 0\n"
      "s_waitcnt lgkmcnt(15)\n"
      "v_mfma_f32_32x32x16_bf16 v[160:175], a[248:251], v[120:123], v[160:175]\n"
      "v_mov_b64 v[104:105], 0\n"
      "v_mov_b64 v[106:107], 0\n"
      "v_mov_b64 v[108:109], 0\n"
      "v_mov_b64 v[110:111], 0\n"
      "v_mfma_f32_32x32x16_bf16 v[160:175], a[252:255], v[124:127], v[160:175]\n"
      "s_min_u32 s39, s3, 1835004\n"
      "v_mov_b32 v205, s39\n"
      "v_mov_b64 v[200:201], -1\n"
      "v_mov_b64 v[202:203], -1\n"
      "s_waitcnt lgkmcnt(12)\n"
      "v_mfma_f32_32x32x16_bf16 v[160:175], a[176:179], v[128:131], v[160:175]\n"
      "s_lshl_b32 s52, s12, 11\n"
      "s_add_u32 s52, s52, s49\n"
      "s_add_u32 s53, s48, s79\n"
      "s_add_u32 s54, s45, 2097152\n"
      "v_mfma_f32_32x32x16_bf16 v[160:175], a[180:183], v[132:135], v[160:175]\n"
      "s_add_u32 s55, s45, 4194304\n"
      "s_add_u32 s56, s43, 1835008\n"
      "s_add_u32 s57, s47, 262144\n"
      "s_waitcnt lgkmcnt(8)\n"
      "v_mfma_f32_32x32x16_bf16 v[160:175], a[184:187], v[136:139], v[160:175]\n"
      "v_mfma_f32_32x32x16_bf16 v[160:175], a[188:191], v[140:143], v[160:175]\n"
      "s_waitcnt lgkmcnt(4)\n"
      "v_mfma_f32_32x32x16_bf16 v[160:175], a[192:195], v[144:147], v[160:175]\n"
      "v_mfma_f32_32x32x16_bf16 v[160:175], a[196:199], v[148:151], v[160:175]\n"
      "s_waitcnt lgkmcnt(0)\n"
      "v_mfma_f32_32x32x16_bf16 v[160:175], a[200:203], v[152:155], v[160:175]\n"
      "v_mfma_f32_32x32x16_bf16 v[160:175], a[204:207], v[156:159], v[160:175]\n"
      "v_mfma_f32_32x32x16_bf16 v[160:175], a[172:175], v[208:211], v[160:175]\n"
      "s_cmp_lg_u32 s14, 3\n"
      "s_cbranch_scc1 L_ep_27\n"
      "s_mov_b64 exec, 0xffff\n"
      "v_mov_b64 v[28:29], 0\n"
      "buffer_atomic_or_x2 v[28:29], v7, s[4:7], s83 offen sc0\n"
      "s_mov_b64 exec, -1\n"
      "L_ep_27:\n"
      "s_nop 9\n"
      "v_pk_mul_f32 v[160:161], v[160:161], v[24:25] op_sel_hi:[1,0]\n"
      "v_pk_mul_f32 v[162:163], v[162:163], v[24:25] op_sel_hi:[1,0]\n"
      "v_pk_mul_f32 v[164:165], v[164:165], v[24:25] op_sel_hi:[1,0]\n"
      "v_pk_mul_f32 v[166:167], v[166:167], v[24:25] op_sel_hi:[1,0]\n"
      "v_pk_mul_f32 v[168:169], v[168:169], v[24:25] op_sel_hi:[1,0]\n"
      "v_pk_mul_f32 v[170:171], v[170:171], v[24:25] op_sel_hi:[1,0]\n"
      "v_pk_mul_f32 v[172:173], v[172:173], v[24:25] op_sel_hi:[1,0]\n"
      "v_pk_mul_f32 v[174:175], v[174:175], v[24:25] op_sel_hi:[1,0]\n"
      "v_cvt_pk_bf16_f32 v212, v168, v169\n"
      "v_cvt_pk_bf16_f32 v213, v170, v171\n"
      "v_cvt_pk_bf16_f32 v214, v172, v173\n"
      "v_cvt_pk_bf16_f32 v215, v174, v175\n"
      "v_cndmask_b32 v214, v214, 0, s[60:61]\n"
      "v_cndmask_b32 v215, v215, 0, s[60:61]\n"
      "v_cvt_pk_bf16_f32 v208, v160, v161\n"
      "v_cvt_pk_bf16_f32 v209, v162, v163\n"
      "v_cvt_pk_bf16_f32 v210, v164, v165\n"
      "v_cvt_pk_bf16_f32 v211, v166, v167\n"
      "; reference point of the exponent: largest score against the wave's own 32 keys\n"
      "v_mfma_f32_32x32x16_bf16 v[112:127], v[244:247], v[212:215], 0\n"
      "s_nop 0\n"
      "v_mfma_f32_32x32x16_bf16 v[112:127], v[240:243], v[208:211], v[112:127]\n"
      "; hand-off: wave 3 polls its flags, barrier\n"
      "s_add_u32 s34, s16, 5\n"
      "s_cmp_lg_u32 s14, 3\n"
      "s_cbranch_scc1 L_pd_29\n"
      "s_mov_b32 s51, 0x40000\n"
      "s_mov_b64 exec, 0xffff\n"
      "s_branch L_pc_30\n"
      "L_poll_28:\n"
      "s_mov_b64 exec, 0xffff\n"
      "v_mov_b64 v[28:29], 0\n"
      "buffer_atomic_or_x2 v[28:29], v7, s[4:7], s83 offen sc0\n"
      "s_sub_u32 s51, s51, 1\n"
      "s_cbranch_scc1 L_pd_29\n"
      "L_pc_30:\n"
      "s_waitcnt vmcnt(0)\n"
      "v_subrev_u32 v28, s34, v28\n"
      "v_cmp_eq_u32 s[40:41], s17, v29\n"
      "v_cmp_gt_u32 vcc, 64, v28\n"
      "s_and_b64 vcc, vcc, s[40:41]\n"
      "s_cmp_eq_u64 vcc, exec\n"
      "s_cbranch_scc0 L_poll_28\n"
      "L_pd_29:\n"
      "s_mov_b64 exec, -1\n"
      "s_waitcnt vmcnt(0) lgkmcnt(0)\n"
      "s_barrier\n"
      "buffer_load_dwordx4 v[224:227], v30, s[4:7], s64 offen sc1\n"
      "buffer_load_dwordx4 v[228:231], v30, s[4:7], s64 offen offset:1024 sc1\n"
      "buffer_load_dwordx4 v[232:235], v30, s[4:7], s64 offen offset:2048 sc1\n"
      "buffer_load_dwordx4 v[236:239], v30, s[4:7], s64 offen offset:3072 sc1\n"
      "ds_write_b128 v19, v[208:211]\n"
      "v_max3_f32 v128, v112, v113, v114\n"
      "v_max3_f32 v129, v115, v116, v117\n"
      "v_max3_f32 v128, v128, v118, v119\n"
      "v_max3_f32 v129, v129, v120, v121\n"
      "v_max3_f32 v128, v128, v122, v123\n"
      "v_max3_f32 v129, v129, v124, v125\n"
      "v_max3_f32 v128, v128, v126, v127\n"
      "v_max_f32 v128, v128, v129\n"
      "L_PATCH:\n"
      "v_mov_b32 v129, v128\n"
      "v_mov_b32 v132, 159744\n"
      "v_mov_b32 v133, 0\n"
      "v_permlane32_swap_b32 v128, v129\n"
      "v_max_f32 v128, v128, v129\n"
      "; -m as three bf16 terms in the padding of the Q fragment (K carries 1.0 there)\n"
      "v_cvt_pk_bf16_f32 v129, -v128, -v128\n"
      "v_and_b32 v129, 0xffff0000, v129\n"
      "v_sub_f32 v129, -v128, v129\n"
      "v_cvt_pk_bf16_f32 v130, -v128, v129\n"
      "v_and_b32 v131, 0xffff0000, v130\n"
      "v_sub_f32 v131, v129, v131\n"
      "v_cvt_pk_bf16_f32 v131, v131, 0\n"
      "v_cndmask_b32 v214, v214, v130, s[60:61]\n"
      "v_cndmask_b32 v215, v215, v131, s[60:61]\n"
      "ds_write_b128 v19, v[212:215] offset:1024\n"
      "ds_write_b32 v132, v133\n"
      "s_waitcnt lgkmcnt(0)\n"
      "s_barrier\n"
      "ds_read_b128 a[224:227], v16\n"
      "ds_read_b128 a[228:231], v16 offset:1024\n"
      "ds_read_b128 a[232:235], v17\n"
      "ds_read_b128 a[236:239], v17 offset:1024\n"
      "ds_read_b128 a[240:243], v18\n"
      "ds_read_b128 a[244:247], v18 offset:1024\n"
      "ds_read_b128 a[248:251], v19\n"
      "ds_read_b128 a[252:255], v19 offset:1024\n"
      "s_mov_b32 s8, s28\n"
      "s_and_b32 s9, s29, 0xffff\n"
      "s_mov_b32 s10, 401408\n"
      "s_mov_b32 s38, s84\n"
      "v_lshrrev_b32 v206, 7, v0\n"
      "v_mul_u32_u24 v206, 896, v206\n"
      "v_lshrrev_b32 v207, 2, v0\n"
      "v_and_b32 v207, 64, v207\n"
      "v_xor_b32 v207, v207, v3\n"
      "v_and_b32 v207, 0x80000070, v207\n"
      "v_add_u32 v206, v206, v207\n"
      "v_add_u32 v207, 7168, v206\n"
      "v_mov_b32 v222, 0x7149f2ca\n"
      "s_waitcnt lgkmcnt(0)\n"
      "v_mfma_f32_32x32x16_bf16 v[112:127], v[240:243], a[224:227], 0\n"
      "v_mfma_f32_32x32x16_bf16 v[112:127], v[244:247], a[228:231], v[112:127]\n"
      "v_mfma_f32_32x32x16_bf16 v[128:143], v[240:243], a[232:235], 0\n"
      "buffer_load_dwordx4 a[176:179], v30, s[4:7], s65 offen sc1\n"
      "buffer_load_dwordx4 a[180:183], v30, s[4:7], s65 offen offset:1024 sc1\n"
      "buffer_load_dwordx4 a[184:187], v30, s[4:7], s65 offen offset:2048 sc1\n"
      "buffer_load_dwordx4 a[188:191], v30, s[4:7], s65 offen offset:3072 sc1\n"
      "v_exp_f32 v112, v112\n"
      "v_exp_f32 v113, v113\n"
      "v_exp_f32 v114, v114\n"
      "v_exp_f32 v115, v115\n"
      "v_mfma_f32_32x32x16_bf16 v[128:143], v[244:247], a[236:239], v[128:143]\n"
      "v_exp_f32 v116, v116\n"
      "v_exp_f32 v117, v117\n"
      "v_exp_f32 v118, v118\n"
      "v_exp_f32 v119, v119\n"
      "v_exp_f32 v120, v120\n"
      "v_exp_f32 v121, v121\n"
      "v_exp_f32 v122, v122\n"
      "v_exp_f32 v123, v123\n"
      "v_exp_f32 v124, v124\n"
      "v_exp_f32 v125, v125\n"
      "v_exp_f32 v126, v126\n"
      "v_exp_f32 v127, v127\n"
      "v_mfma_f32_32x32x16_bf16 v[144:159], v[240:243], a[240:243], 0\n"
      "v_exp_f32 v128, v128\n"
      "v_exp_f32 v129, v129\n"
      "v_exp_f32 v130, v130\n"
      "v_exp_f32 v131, v131\n"
      "v_mfma_f32_32x32x16_bf16 v[144:159], v[244:247], a[244:247], v[144:159]\n"
      "v_exp_f32 v132, v132\n"
      "v_exp_f32 v133, v133\n"
      "v_exp_f32 v134, v134\n"
      "v_exp_f32 v135, v135\n"
      "v_smfmac_f32_32x32x32_bf16 v[48:63], v[248:251], v[112:119], v15\n"
      "v_exp_f32 v136, v136\n"
      "v_exp_f32 v137, v137\n"
      "v_exp_f32 v138, v138\n"
      "v_exp_f32 v139, v139\n"
      "v_smfmac_f32_32x32x32_bf16 v[48:63], v[252:255], v[120:127], v15\n"
      "v_exp_f32 v140, v140\n"
      "v_exp_f32 v141, v141\n"
      "v_exp_f32 v142, v142\n"
      "v_exp_f32 v143, v143\n"
      "v_mfma_f32_32x32x16_bf16 v[160:175], v[240:243], a[248:251], 0\n"
      "buffer_load_dwordx4 a[192:195], v30, s[4:7], s66 offen sc1\n"
      "buffer_load_dwordx4 a[196:199], v30, s[4:7], s66 offen offset:1024 sc1\n"
      "buffer_load_dwordx4 a[200:203], v30, s[4:7], s66 offen offset:2048 sc1\n"
      "buffer_load_dwordx4 a[204:207], v30, s[4:7], s66 offen offset:3072 sc1\n"
      "v_exp_f32 v144, v144\n"
      "v_exp_f32 v145, v145\n"
      "v_exp_f32 v146, v146\n"
      "v_exp_f32 v147, v147\n"
      "v_mfma_f32_32x32x16_bf16 v[160:175], v[244:247], a[252:255], v[160:175]\n"
      "v_exp_f32 v148, v148\n"
      "v_exp_f32 v149, v149\n"
      "v_exp_f32 v150, v150\n"
      "v_exp_f32 v151, v151\n"
      "v_smfmac_f32_32x32x32_bf16 v[64:79], v[248:251], v[128:135], v15\n"
      "v_exp_f32 v152, v152\n"
      "v_exp_f32 v153, v153\n"
      "v_exp_f32 v154, v154\n"
      "v_exp_f32 v155, v155\n"
      "v_smfmac_f32_32x32x32_bf16 v[64:79], v[252:255], v[136:143], v15\n"
      "v_exp_f32 v156, v156\n"
      "v_exp_f32 v157, v157\n"
      "v_exp_f32 v158, v158\n"
      "v_exp_f32 v159, v159\n"
      "s_waitcnt vmcnt(10)\n"
      "v_mfma_f32_32x32x16_bf16 v[112:127], v[224:227], a[224:227], 0\n"
      "v_exp_f32 v160, v160\n"
      "v_exp_f32 v161, v161\n"
      "v_exp_f32 v162, v162\n"
      "v_exp_f32 v163, v163\n"
      "v_mfma_f32_32x32x16_bf16 v[112:127], v[228:231], a[228:231], v[112:127]\n"
      "v_exp_f32 v164, v164\n"
      "v_exp_f32 v165, v165\n"
      "v_exp_f32 v166, v166\n"
      "v_exp_f32 v167, v167\n"
      "v_smfmac_f32_32x32x32_bf16 v[80:95], v[248:251], v[144:151], v15\n"
      "v_exp_f32 v168, v168\n"
      "v_exp_f32 v169, v169\n"
      "v_exp_f32 v170, v170\n"
      "v_exp_f32 v171, v171\n"
      "v_smfmac_f32_32x32x32_bf16 v[80:95], v[252:255], v[152:159], v15\n"
      "v_exp_f32 v172, v172\n"
      "v_exp_f32 v173, v173\n"
      "v_exp_f32 v174, v174\n"
      "v_exp_f32 v175, v175\n"
      "v_mfma_f32_32x32x16_bf16 v[128:143], v[224:227], a[232:235], 0\n"
      "buffer_load_dwordx4 a[208:211], v30, s[4:7], s67 offen sc1\n"
      "buffer_load_dwordx4 a[212:215], v30, s[4:7], s67 offen offset:1024 sc1\n"
      "buffer_load_dwordx4 a[216:219], v30, s[4:7], s67 offen offset:2048 sc1\n"
      "buffer_load_dwordx4 a[220:223], v30, s[4:7], s67 offen offset:3072 sc1\n"
      "v_exp_f32 v112, v112\n"
      "v_exp_f32 v113, v113\n"
      "v_exp_f32 v114, v114\n"
      "v_exp_f32 v115, v115\n"
      "v_mfma_f32_32x32x16_bf16 v[128:143], v[228:231], a[236:239], v[128:143]\n"
      "v_exp_f32 v116, v116\n"
      "v_exp_f32 v117, v117\n"
      "v_exp_f32 v118, v118\n"
      "v_exp_f32 v119, v119\n"
      "v_smfmac_f32_32x32x32_bf16 v[96:111], v[248:251], v[160:167], v15\n"
      "v_exp_f32 v120, v120\n"
      "v_exp_f32 v121, v121\n"
      "v_exp_f32 v122, v122\n"
      "v_exp_f32 v123, v123\n"
      "v_smfmac_f32_32x32x32_bf16 v[96:111], v[252:255], v[168:175], v15\n"
      "v_exp_f32 v124, v124\n"
      "v_exp_f32 v125, v125\n"
      "v_exp_f32 v126, v126\n"
      "v_exp_f32 v127, v127\n"
      "v_mfma_f32_32x32x16_bf16 v[144:159], v[224:227], a[240:243], 0\n"
      "v_exp_f32 v128, v128\n"
      "v_exp_f32 v129, v129\n"
      "v_exp_f32 v130, v130\n"
      "v_exp_f32 v131, v131\n"
      "v_mfma_f32_32x32x16_bf16 v[144:159], v[228:231], a[244:247], v[144:159]\n"
      "v_exp_f32 v132, v132\n"
      "v_exp_f32 v133, v133\n"
      "v_exp_f32 v134, v134\n"
      "v_exp_f32 v135, v135\n"
      "s_waitcnt vmcnt(12)\n"
      "v_smfmac_f32_32x32x32_bf16 v[48:63], v[232:235], v[112:119], v15\n"
      "v_exp_f32 v136, v136\n"
      "v_exp_f32 v137, v137\n"
      "v_exp_f32 v138, v138\n"
      "v_exp_f32 v139, v139\n"
      "v_smfmac_f32_32x32x32_bf16 v[48:63], v[236:239], v[120:127], v15\n"
      "v_exp_f32 v140, v140\n"
      "v_exp_f32 v141, v141\n"
      "v_exp_f32 v142, v142\n"
      "v_exp_f32 v143, v143\n"
      "v_mfma_f32_32x32x16_bf16 v[160:175], v[224:227], a[248:251], 0\n"
      "v_exp_f32 v144, v144\n"
      "v_exp_f32 v145, v145\n"
      "v_exp_f32 v146, v146\n"
      "v_exp_f32 v147, v147\n"
      "v_mfma_f32_32x32x16_bf16 v[160:175], v[228:231], a[252:255], v[160:175]\n"
      "v_exp_f32 v148, v148\n"
      "v_exp_f32 v149, v149\n"
      "v_exp_f32 v150, v150\n"
      "v_exp_f32 v151, v151\n"
      "v_smfmac_f32_32x32x32_bf16 v[64:79], v[232:235], v[128:135], v15\n"
      "v_exp_f32 v152, v152\n"
      "v_exp_f32 v153, v153\n"
      "v_exp_f32 v154, v154\n"
      "v_exp_f32 v155, v155\n"
      "v_smfmac_f32_32x32x32_bf16 v[64:79], v[236:239], v[136:143], v15\n"
      "v_exp_f32 v156, v156\n"
      "v_exp_f32 v157, v157\n"
      "v_exp_f32 v158, v158\n"
      "v_exp_f32 v159, v159\n"
      "s_waitcnt vmcnt(8)\n"
      "v_mfma_f32_32x32x16_bf16 v[112:127], a[176:179], a[224:227], 0\n"
      "buffer_load_dwordx4 v[240:243], v30, s[4:7], s68 offen sc1\n"
      "buffer_load_dwordx4 v[244:247], v30, s[4:7], s68 offen offset:1024 sc1\n"
      "buffer_load_dwordx4 v[248:251], v30, s[4:7], s68 offen offset:2048 sc1\n"
      "buffer_load_dwordx4 v[252:255], v30, s[4:7], s68 offen offset:3072 sc1\n"
      "v_exp_f32 v160, v160\n"
      "v_exp_f32 v161, v161\n"
      "v_exp_f32 v162, v162\n"
      "v_exp_f32 v163, v163\n"
      "v_mfma_f32_32x32x16_bf16 v[112:127], a[180:183], a[228:231], v[112:127]\n"
      "v_exp_f32 v164, v164\n"
      "v_exp_f32 v165, v165\n"
      "v_exp_f32 v166, v166\n"
      "v_exp_f32 v167, v167\n"
      "v_smfmac_f32_32x32x32_bf16 v[80:95], v[232:235], v[144:151], v15\n"
      "v_exp_f32 v168, v168\n"
      "v_exp_f32 v169, v169\n"
      "v_exp_f32 v170, v170\n"
      "v_exp_f32 v171, v171\n"
      "v_smfmac_f32_32x32x32_bf16 v[80:95], v[236:239], v[152:159], v15\n"
      "v_exp_f32 v172, v172\n"
      "v_exp_f32 v173, v173\n"
      "v_exp_f32 v174, v174\n"
      "v_exp_f32 v175, v175\n"
      "v_mfma_f32_32x32x16_bf16 v[128:143], a[176:179], a[232:235], 0\n"
      "v_exp_f32 v112, v112\n"
      "v_exp_f32 v113, v113\n"
      "v_exp_f32 v114, v114\n"
      "v_exp_f32 v115, v115\n"
      "v_mfma_f32_32x32x16_bf16 v[128:143], a[180:183], a[236:239], v[128:143]\n"
      "v_exp_f32 v116, v116\n"
      "v_exp_f32 v117, v117\n"
      "v_exp_f32 v118, v118\n"
      "v_exp_f32 v119, v119\n"
      "v_smfmac_f32_32x32x32_bf16 v[96:111], v[232:235], v[160:167], v15\n"
      "v_exp_f32 v120, v120\n"
      "v_exp_f32 v121, v121\n"
      "v_exp_f32 v122, v122\n"
      "v_exp_f32 v123, v123\n"
      "v_smfmac_f32_32x32x32_bf16 v[96:111], v[236:239], v[168:175], v15\n"
      "v_exp_f32 v124, v124\n"
      "v_exp_f32 v125, v125\n"
      "v_exp_f32 v126, v126\n"
      "v_exp_f32 v127, v127\n"
      "v_mfma_f32_32x32x16_bf16 v[144:159], a[176:179], a[240:243], 0\n"
      "v_exp_f32 v128, v128\n"
      "v_exp_f32 v129, v129\n"
      "v_exp_f32 v130, v130\n"
      "v_exp_f32 v131, v131\n"
      "v_mfma_f32_32x32x16_bf16 v[144:159], a[180:183], a[244:247], v[144:159]\n"
      "v_exp_f32 v132, v132\n"
      "v_exp_f32 v133, v133\n"
      "v_exp_f32 v134, v134\n"
      "v_exp_f32 v135, v135\n"
      "v_smfmac_f32_32x32x32_bf16 v[48:63], a[184:187], v[112:119], v15\n"
      "v_exp_f32 v136, v136\n"
      "v_exp_f32 v137, v137\n"
      "v_exp_f32 v138, v138\n"
      "v_exp_f32 v139, v139\n"
      "v_smfmac_f32_32x32x32_bf16 v[48:63], a[188:191], v[120:127], v15\n"
      "v_exp_f32 v140, v140\n"
      "v_exp_f32 v141, v141\n"
      "v_exp_f32 v142, v142\n"
      "v_exp_f32 v143, v143\n"
      "v_mfma_f32_32x32x16_bf16 v[160:175], a[176:179], a[248:251], 0\n"
      "buffer_load_dwordx4 v[224:227], v30, s[4:7], s69 offen sc1\n"
      "buffer_load_dwordx4 v[228:231], v30, s[4:7], s69 offen offset:1024 sc1\n"
      "buffer_load_dwordx4 v[232:235], v30, s[4:7], s69 offen offset:2048 sc1\n"
      "buffer_load_dwordx4 v[236:239], v30, s[4:7], s69 offen offset:3072 sc1\n"
      "v_exp_f32 v144, v144\n"
      "v_exp_f32 v145, v145\n"
      "v_exp_f32 v146, v146\n"
      "v_exp_f32 v147, v147\n"
      "v_mfma_f32_32x32x16_bf16 v[160:175], a[180:183], a[252:255], v[160:175]\n"
      "v_exp_f32 v148, v148\n"
      "v_exp_f32 v149, v149\n"
      "v_exp_f32 v150, v150\n"
      "v_exp_f32 v151, v151\n"
      "v_smfmac_f32_32x32x32_bf16 v[64:79], a[184:187], v[128:135], v15\n"
      "v_exp_f32 v152, v152\n"
      "v_exp_f32 v153, v153\n"
      "v_exp_f32 v154, v154\n"
      "v_exp_f32 v155, v155\n"
      "v_smfmac_f32_32x32x32_bf16 v[64:79], a[188:191], v[136:143], v15\n"
      "v_exp_f32 v156, v156\n"
      "v_exp_f32 v157, v157\n"
      "v_exp_f32 v158, v158\n"
      "v_exp_f32 v159, v159\n"
      "s_waitcnt vmcnt(12)\n"
      "v_mfma_f32_32x32x16_bf16 v[112:127], a[192:195], a[224:227], 0\n"
      "v_exp_f32 v160, v160\n"
      "v_exp_f32 v161, v161\n"
      "v_exp_f32 v162, v162\n"
      "v_exp_f32 v163, v163\n"
      "v_mfma_f32_32x32x16_bf16 v[112:127], a[196:199], a[228:231], v[112:127]\n"
      "v_exp_f32 v164, v164\n"
      "v_exp_f32 v165, v165\n"
      "v_exp_f32 v166, v166\n"
      "v_exp_f32 v167, v167\n"
      "v_smfmac_f32_32x32x32_bf16 v[80:95], a[184:187], v[144:151], v15\n"
      "v_exp_f32 v168, v168\n"
      "v_exp_f32 v169, v169\n"
      "v_exp_f32 v170, v170\n"
      "v_exp_f32 v171, v171\n"
      "v_smfmac_f32_32x32x32_bf16 v[80:95], a[188:191], v[152:159], v15\n"
      "v_exp_f32 v172, v172\n"
      "v_exp_f32 v173, v173\n"
      "v_exp_f32 v174, v174\n"
      "v_exp_f32 v175, v175\n"
      "v_mfma_f32_32x32x16_bf16 v[128:143], a[192:195], a[232:235], 0\n"
      "; LATE flag: every wave drained its stores in front of the barrier just passed\n"
      "s_cmp_lg_u32 s14, 1\n"
      "s_cbranch_scc1 L_late_31\n"
      "s_add_u32 s34, s16, 5\n"
      "v_mov_b32 v28, s34\n"
      "v_mov_b32 v29, s17\n"
      "buffer_atomic_swap_x2 v[28:29], v6, s[4:7], s81 offen offset:128\n"
      "L_late_31:\n"
      "s_cmp_lg_u32 s15, 0\n"
      "v_exp_f32 v112, v112\n"
      "v_exp_f32 v113, v113\n"
      "v_exp_f32 v114, v114\n"
      "v_exp_f32 v115, v115\n"
      "v_mfma_f32_32x32x16_bf16 v[128:143], a[196:199], a[236:239], v[128:143]\n"
      "v_exp_f32 v116, v116\n"
      "v_exp_f32 v117, v117\n"
      "v_exp_f32 v118, v118\n"
      "v_exp_f32 v119, v119\n"
      "v_smfmac_f32_32x32x32_bf16 v[96:111], a[184:187], v[160:167], v15\n"
      "v_exp_f32 v120, v120\n"
      "v_exp_f32 v121, v121\n"
      "v_exp_f32 v122, v122\n"
      "v_exp_f32 v123, v123\n"
      "v_smfmac_f32_32x32x32_bf16 v[96:111], a[188:191], v[168:175], v15\n"
      "v_exp_f32 v124, v124\n"
      "v_exp_f32 v125, v125\n"
      "v_exp_f32 v126, v126\n"
      "v_exp_f32 v127, v127\n"
      "v_mfma_f32_32x32x16_bf16 v[144:159], a[192:195], a[240:243], 0\n"
      "buffer_load_dwordx4 a[176:179], v30, s[4:7], s70 offen sc1\n"
      "buffer_load_dwordx4 a[180:183], v30, s[4:7], s70 offen offset:1024 sc1\n"
      "buffer_load_dwordx4 a[184:187], v30, s[4:7], s70 offen offset:2048 sc1\n"
      "buffer_load_dwordx4 a[188:191], v30, s[4:7], s70 offen offset:3072 sc1\n"
      "v_exp_f32 v128, v128\n"
      "v_exp_f32 v129, v129\n"
      "v_exp_f32 v130, v130\n"
      "v_exp_f32 v131, v131\n"
      "v_mfma_f32_32x32x16_bf16 v[144:159], a[196:199], a[244:247], v[144:159]\n"
      "v_exp_f32 v132, v132\n"
      "v_exp_f32 v133, v133\n"
      "v_exp_f32 v134, v134\n"
      "v_exp_f32 v135, v135\n"
      "v_smfmac_f32_32x32x32_bf16 v[48:63], a[200:203], v[112:119], v15\n"
      "v_exp_f32 v136, v136\n"
      "v_exp_f32 v137, v137\n"
      "v_exp_f32 v138, v138\n"
      "v_exp_f32 v139, v139\n"
      "v_smfmac_f32_32x32x32_bf16 v[48:63], a[204:207], v[120:127], v15\n"
      "v_exp_f32 v140, v140\n"
      "v_exp_f32 v141, v141\n"
      "v_exp_f32 v142, v142\n"
      "v_exp_f32 v143, v143\n"
      "v_mfma_f32_32x32x16_bf16 v[160:175], a[192:195], a[248:251], 0\n"
      "v_exp_f32 v144, v144\n"
      "v_exp_f32 v145, v145\n"
      "v_exp_f32 v146, v146\n"
      "v_exp_f32 v147, v147\n"
      "v_mfma_f32_32x32x16_bf16 v[160:175], a[196:199], a[252:255], v[160:175]\n"
      "v_exp_f32 v148, v148\n"
      "v_exp_f32 v149, v149\n"
      "v_exp_f32 v150, v150\n"
      "v_exp_f32 v151, v151\n"
      "v_smfmac_f32_32x32x32_bf16 v[64:79], a[200:203], v[128:135], v15\n"
      "v_exp_f32 v152, v152\n"
      "v_exp_f32 v153, v153\n"
      "v_exp_f32 v154, v154\n"
      "v_exp_f32 v155, v155\n"
      "v_smfmac_f32_32x32x32_bf16 v[64:79], a[204:207], v[136:143], v15\n"
      "v_exp_f32 v156, v156\n"
      "v_exp_f32 v157, v157\n"
      "v_exp_f32 v158, v158\n"
      "v_exp_f32 v159, v159\n"
      "s_waitcnt vmcnt(12)\n"
      "v_mfma_f32_32x32x16_bf16 v[112:127], a[208:211], a[224:227], 0\n"
      "s_cbranch_scc1 L_po_32\n"
      "buffer_load_dwordx4 a[112:115], v206, s[8:11], s38 offen\n"
      "buffer_load_dwordx4 a[116:119], v207, s[8:11], s38 offen\n"
      "s_addk_i32 s38, 14336\n"
      "buffer_store_dwordx4 v[200:203], v0, s[4:7], s45 offen offset:0 sc1\n"
      "buffer_store_dwordx4 v[200:203], v0, s[4:7], s45 offen offset:1024 sc1\n"
      "L_po_32:\n"
      "v_exp_f32 v160, v160\n"
      "v_exp_f32 v161, v161\n"
      "v_exp_f32 v162, v162\n"
      "v_exp_f32 v163, v163\n"
      "v_mfma_f32_32x32x16_bf16 v[112:127], a[212:215], a[228:231], v[112:127]\n"
      "v_exp_f32 v164, v164\n"
      "v_exp_f32 v165, v165\n"
      "v_exp_f32 v166, v166\n"
      "v_exp_f32 v167, v167\n"
      "v_smfmac_f32_32x32x32_bf16 v[80:95], a[200:203], v[144:151], v15\n"
      "v_exp_f32 v168, v168\n"
      "v_exp_f32 v169, v169\n"
      "v_exp_f32 v170, v170\n"
      "v_exp_f32 v171, v171\n"
      "v_smfmac_f32_32x32x32_bf16 v[80:95], a[204:207], v[152:159], v15\n"
      "v_exp_f32 v172, v172\n"
      "v_exp_f32 v173, v173\n"
      "v_exp_f32 v174, v174\n"
      "v_exp_f32 v175, v175\n"
      "v_mfma_f32_32x32x16_bf16 v[128:143], a[208:211], a[232:235], 0\n"
      "v_exp_f32 v112, v112\n"
      "v_exp_f32 v113, v113\n"
      "v_exp_f32 v114, v114\n"
      "v_exp_f32 v115, v115\n"
      "v_mfma_f32_32x32x16_bf16 v[128:143], a[212:215], a[236:239], v[128:143]\n"
      "v_exp_f32 v116, v116\n"
      "v_exp_f32 v117, v117\n"
      "v_exp_f32 v118, v118\n"
      "v_exp_f32 v119, v119\n"
      "v_smfmac_f32_32x32x32_bf16 v[96:111], a[200:203], v[160:167], v15\n"
      "v_exp_f32 v120, v120\n"
      "v_exp_f32 v121, v121\n"
      "v_exp_f32 v122, v122\n"
      "v_exp_f32 v123, v123\n"
      "v_smfmac_f32_32x32x32_bf16 v[96:111], a[204:207], v[168:175], v15\n"
      "v_exp_f32 v124, v124\n"
      "v_exp_f32 v125, v125\n"
      "v_exp_f32 v126, v126\n"
      "v_exp_f32 v127, v127\n"
      "v_mfma_f32_32x32x16_bf16 v[144:159], a[208:211], a[240:243], 0\n"
      "buffer_load_dwordx4 a[192:195], v30, s[4:7], s71 offen sc1\n"
      "buffer_load_dwordx4 a[196:199], v30, s[4:7], s71 offen offset:1024 sc1\n"
      "buffer_load_dwordx4 a[200:203], v30, s[4:7], s71 offen offset:2048 sc1\n"
      "buffer_load_dwordx4 a[204:207], v30, s[4:7], s71 offen offset:3072 sc1\n"
      "v_exp_f32 v128, v128\n"
      "v_exp_f32 v129, v129\n"
      "v_exp_f32 v130, v130\n"
      "v_exp_f32 v131, v131\n"
      "v_mfma_f32_32x32x16_bf16 v[144:159], a[212:215], a[244:247], v[144:159]\n"
      "v_exp_f32 v132, v132\n"
      "v_exp_f32 v133, v133\n"
      "v_exp_f32 v134, v134\n"
      "v_exp_f32 v135, v135\n"
      "v_smfmac_f32_32x32x32_bf16 v[48:63], a[216:219], v[112:119], v15\n"
      "v_exp_f32 v136, v136\n"
      "v_exp_f32 v137, v137\n"
      "v_exp_f32 v138, v138\n"
      "v_exp_f32 v139, v139\n"
      "v_smfmac_f32_32x32x32_bf16 v[48:63], a[220:223], v[120:127], v15\n"
      "v_exp_f32 v140, v140\n"
      "v_exp_f32 v141, v141\n"
      "v_exp_f32 v142, v142\n"
      "v_exp_f32 v143, v143\n"
      "v_mfma_f32_32x32x16_bf16 v[160:175], a[208:211], a[248:251], 0\n"
      "v_exp_f32 v144, v144\n"
      "v_exp_f32 v145, v145\n"
      "v_exp_f32 v146, v146\n"
      "v_exp_f32 v147, v147\n"
      "v_mfma_f32_32x32x16_bf16 v[160:175], a[212:215], a[252:255], v[160:175]\n"
      "v_exp_f32 v148, v148\n"
      "v_exp_f32 v149, v149\n"
      "v_exp_f32 v150, v150\n"
      "v_exp_f32 v151, v151\n"
      "v_smfmac_f32_32x32x32_bf16 v[64:79], a[216:219], v[128:135], v15\n"
      "v_exp_f32 v152, v152\n"
      "v_exp_f32 v153, v153\n"
      "v_exp_f32 v154, v154\n"
      "v_exp_f32 v155, v155\n"
      "v_smfmac_f32_32x32x32_bf16 v[64:79], a[220:223], v[136:143], v15\n"
      "v_exp_f32 v156, v156\n"
      "v_exp_f32 v157, v157\n"
      "v_exp_f32 v158, v158\n"
      "v_exp_f32 v159, v159\n"
      "s_waitcnt vmcnt(12)\n"
      "v_mfma_f32_32x32x16_bf16 v[112:127], v[240:243], a[224:227], 0\n"
      "s_cbranch_scc1 L_po_33\n"
      "buffer_load_dwordx4 a[120:123], v206, s[8:11], s38 offen\n"
      "buffer_load_dwordx4 a[124:127], v207, s[8:11], s38 offen\n"
      "s_addk_i32 s38, 14336\n"
      "buffer_store_dwordx4 v[200:203], v3, s[4:7], s43 offen offset:0 sc1\n"
      "buffer_store_dwordx4 v[200:203], v3, s[4:7], s43 offen offset:1024 sc1\n"
      "L_po_33:\n"
      "v_exp_f32 v160, v160\n"
      "v_exp_f32 v161, v161\n"
      "v_exp_f32 v162, v162\n"
      "v_exp_f32 v163, v163\n"
      "v_mfma_f32_32x32x16_bf16 v[112:127], v[244:247], a[228:231], v[112:127]\n"
      "v_exp_f32 v164, v164\n"
      "v_exp_f32 v165, v165\n"
      "v_exp_f32 v166, v166\n"
      "v_exp_f32 v167, v167\n"
      "v_smfmac_f32_32x32x32_bf16 v[80:95], a[216:219], v[144:151], v15\n"
      "v_exp_f32 v168, v168\n"
      "v_exp_f32 v169, v169\n"
      "v_exp_f32 v170, v170\n"
      "v_exp_f32 v171, v171\n"
      "v_smfmac_f32_32x32x32_bf16 v[80:95], a[220:223], v[152:159], v15\n"
      "v_exp_f32 v172, v172\n"
      "v_exp_f32 v173, v173\n"
      "v_exp_f32 v174, v174\n"
      "v_exp_f32 v175, v175\n"
      "v_mfma_f32_32x32x16_bf16 v[128:143], v[240:243], a[232:235], 0\n"
      "v_exp_f32 v112, v112\n"
      "v_exp_f32 v113, v113\n"
      "v_exp_f32 v114, v114\n"
      "v_exp_f32 v115, v115\n"
      "v_mfma_f32_32x32x16_bf16 v[128:143], v[244:247], a[236:239], v[128:143]\n"
      "v_exp_f32 v116, v116\n"
      "v_exp_f32 v117, v117\n"
      "v_exp_f32 v118, v118\n"
      "v_exp_f32 v119, v119\n"
      "v_smfmac_f32_32x32x32_bf16 v[96:111], a[216:219], v[160:167], v15\n"
      "v_exp_f32 v120, v120\n"
      "v_exp_f32 v121, v121\n"
      "v_exp_f32 v122, v122\n"
      "v_exp_f32 v123, v123\n"
      "v_smfmac_f32_32x32x32_bf16 v[96:111], a[220:223], v[168:175], v15\n"
      "v_exp_f32 v124, v124\n"
      "v_exp_f32 v125, v125\n"
      "v_exp_f32 v126, v126\n"
      "v_exp_f32 v127, v127\n"
      "v_mfma_f32_32x32x16_bf16 v[144:159], v[240:243], a[240:243], 0\n"
      "buffer_load_dwordx4 a[208:211], v30, s[4:7], s72 offen sc1\n"
      "buffer_load_dwordx4 a[212:215], v30, s[4:7], s72 offen offset:1024 sc1\n"
      "buffer_load_dwordx4 a[216:219], v30, s[4:7], s72 offen offset:2048 sc1\n"
      "buffer_load_dwordx4 a[220:223], v30, s[4:7], s72 offen offset:3072 sc1\n"
      "v_exp_f32 v128, v128\n"
      "v_exp_f32 v129, v129\n"
      "v_exp_f32 v130, v130\n"
      "v_exp_f32 v131, v131\n"
      "v_mfma_f32_32x32x16_bf16 v[144:159], v[244:247], a[244:247], v[144:159]\n"
      "v_exp_f32 v132, v132\n"
      "v_exp_f32 v133, v133\n"
      "v_exp_f32 v134, v134\n"
      "v_exp_f32 v135, v135\n"
      "v_smfmac_f32_32x32x32_bf16 v[48:63], v[248:251], v[112:119], v15\n"
      "v_exp_f32 v136, v136\n"
      "v_exp_f32 v137, v137\n"
      "v_exp_f32 v138, v138\n"
      "v_exp_f32 v139, v139\n"
      "v_smfmac_f32_32x32x32_bf16 v[48:63], v[252:255], v[120:127], v15\n"
      "v_exp_f32 v140, v140\n"
      "v_exp_f32 v141, v141\n"
      "v_exp_f32 v142, v142\n"
      "v_exp_f32 v143, v143\n"
      "v_mfma_f32_32x32x16_bf16 v[160:175], v[240:243], a[248:251], 0\n"
      "v_exp_f32 v144, v144\n"
      "v_exp_f32 v145, v145\n"
      "v_exp_f32 v146, v146\n"
      "v_exp_f32 v147, v147\n"
      "v_mfma_f32_32x32x16_bf16 v[160:175], v[244:247], a[252:255], v[160:175]\n"
      "v_exp_f32 v148, v148\n"
      "v_exp_f32 v149, v149\n"
      "v_exp_f32 v150, v150\n"
      "v_exp_f32 v151, v151\n"
      "v_smfmac_f32_32x32x32_bf16 v[64:79], v[248:251], v[128:135], v15\n"
      "v_exp_f32 v152, v152\n"
      "v_exp_f32 v153, v153\n"
      "v_exp_f32 v154, v154\n"
      "v_exp_f32 v155, v155\n"
      "v_smfmac_f32_32x32x32_bf16 v[64:79], v[252:255], v[136:143], v15\n"
      "v_exp_f32 v156, v156\n"
      "v_exp_f32 v157, v157\n"
      "v_exp_f32 v158, v158\n"
      "v_exp_f32 v159, v159\n"
      "s_waitcnt vmcnt(12)\n"
      "v_mfma_f32_32x32x16_bf16 v[112:127], v[224:227], a[224:227], 0\n"
      "s_cbranch_scc1 L_po_34\n"
      "buffer_load_dwordx4 a[128:131], v206, s[8:11], s38 offen\n"
      "buffer_load_dwordx4 a[132:135], v207, s[8:11], s38 offen\n"
      "s_addk_i32 s38, 14336\n"
      "buffer_store_dword v200, v9, s[4:7], s47 offen offset:0 sc1\n"
      "buffer_store_dwordx2 v[200:201], v7, s[4:7], s52 offen offset:0 sc1\n"
      "L_po_34:\n"
      "v_exp_f32 v160, v160\n"
      "v_exp_f32 v161, v161\n"
      "v_exp_f32 v162, v162\n"
      "v_exp_f32 v163, v163\n"
      "v_mfma_f32_32x32x16_bf16 v[112:127], v[228:231], a[228:231], v[112:127]\n"
      "v_exp_f32 v164, v164\n"
      "v_exp_f32 v165, v165\n"
      "v_exp_f32 v166, v166\n"
      "v_exp_f32 v167, v167\n"
      "v_smfmac_f32_32x32x32_bf16 v[80:95], v[248:251], v[144:151], v15\n"
      "v_exp_f32 v168, v168\n"
      "v_exp_f32 v169, v169\n"
      "v_exp_f32 v170, v170\n"
      "v_exp_f32 v171, v171\n"
      "v_smfmac_f32_32x32x32_bf16 v[80:95], v[252:255], v[152:159], v15\n"
      "v_exp_f32 v172, v172\n"
      "v_exp_f32 v173, v173\n"
      "v_exp_f32 v174, v174\n"
      "v_exp_f32 v175, v175\n"
      "v_mfma_f32_32x32x16_bf16 v[128:143], v[224:227], a[232:235], 0\n"
      "v_exp_f32 v112, v112\n"
      "v_exp_f32 v113, v113\n"
      "v_exp_f32 v114, v114\n"
      "v_exp_f32 v115, v115\n"
      "v_mfma_f32_32x32x16_bf16 v[128:143], v[228:231], a[236:239], v[128:143]\n"
      "v_exp_f32 v116, v116\n"
      "v_exp_f32 v117, v117\n"
      "v_exp_f32 v118, v118\n"
      "v_exp_f32 v119, v119\n"
      "v_smfmac_f32_32x32x32_bf16 v[96:111], v[248:251], v[160:167], v15\n"
      "v_exp_f32 v120, v120\n"
      "v_exp_f32 v121, v121\n"
      "v_exp_f32 v122, v122\n"
      "v_exp_f32 v123, v123\n"
      "v_smfmac_f32_32x32x32_bf16 v[96:111], v[252:255], v[168:175], v15\n"
      "v_exp_f32 v124, v124\n"
      "v_exp_f32 v125, v125\n"
      "v_exp_f32 v126, v126\n"
      "v_exp_f32 v127, v127\n"
      "v_mfma_f32_32x32x16_bf16 v[144:159], v[224:227], a[240:243], 0\n"
      "buffer_load_dwordx4 v[240:243], v30, s[4:7], s73 offen sc1\n"
      "buffer_load_dwordx4 v[244:247], v30, s[4:7], s73 offen offset:1024 sc1\n"
      "buffer_load_dwordx4 v[248:251], v30, s[4:7], s73 offen offset:2048 sc1\n"
      "buffer_load_dwordx4 v[252:255], v30, s[4:7], s73 offen offset:3072 sc1\n"
      "v_exp_f32 v128, v128\n"
      "v_exp_f32 v129, v129\n"
      "v_exp_f32 v130, v130\n"
      "v_exp_f32 v131, v131\n"
      "v_mfma_f32_32x32x16_bf16 v[144:159], v[228:231], a[244:247], v[144:159]\n"
      "v_exp_f32 v132, v132\n"
      "v_exp_f32 v133, v133\n"
      "v_exp_f32 v134, v134\n"
      "v_exp_f32 v135, v135\n"
      "v_smfmac_f32_32x32x32_bf16 v[48:63], v[232:235], v[112:119], v15\n"
      "v_exp_f32 v136, v136\n"
      "v_exp_f32 v137, v137\n"
      "v_exp_f32 v138, v138\n"
      "v_exp_f32 v139, v139\n"
      "v_smfmac_f32_32x32x32_bf16 v[48:63], v[236:239], v[120:127], v15\n"
      "v_exp_f32 v140, v140\n"
      "v_exp_f32 v141, v141\n"
      "v_exp_f32 v142, v142\n"
      "v_exp_f32 v143, v143\n"
      "v_mfma_f32_32x32x16_bf16 v[160:175], v[224:227], a[248:251], 0\n"
      "v_exp_f32 v144, v144\n"
      "v_exp_f32 v145, v145\n"
      "v_exp_f32 v146, v146\n"
      "v_exp_f32 v147, v147\n"
      "v_mfma_f32_32x32x16_bf16 v[160:175], v[228:231], a[252:255], v[160:175]\n"
      "v_exp_f32 v148, v148\n"
      "v_exp_f32 v149, v149\n"
      "v_exp_f32 v150, v150\n"
      "v_exp_f32 v151, v151\n"
      "v_smfmac_f32_32x32x32_bf16 v[64:79], v[232:235], v[128:135], v15\n"
      "v_exp_f32 v152, v152\n"
      "v_exp_f32 v153, v153\n"
      "v_exp_f32 v154, v154\n"
      "v_exp_f32 v155, v155\n"
      "v_smfmac_f32_32x32x32_bf16 v[64:79], v[236:239], v[136:143], v15\n"
      "v_exp_f32 v156, v156\n"
      "v_exp_f32 v157, v157\n"
      "v_exp_f32 v158, v158\n"
      "v_exp_f32 v159, v159\n"
      "s_waitcnt vmcnt(12)\n"
      "v_mfma_f32_32x32x16_bf16 v[112:127], a[176:179], a[224:227], 0\n"
      "s_cbranch_scc1 L_po_35\n"
      "buffer_load_dwordx4 a[136:139], v206, s[8:11], s38 offen\n"
      "buffer_load_dwordx4 a[140:143], v207, s[8:11], s38 offen\n"
      "s_addk_i32 s38, 14336\n"
      "buffer_store_dwordx4 v[200:203], v0, s[4:7], s54 offen offset:0 sc1\n"
      "buffer_store_dwordx4 v[200:203], v0, s[4:7], s54 offen offset:1024 sc1\n"
      "L_po_35:\n"
      "v_exp_f32 v160, v160\n"
      "v_exp_f32 v161, v161\n"
      "v_exp_f32 v162, v162\n"
      "v_exp_f32 v163, v163\n"
      "v_mfma_f32_32x32x16_bf16 v[112:127], a[180:183], a[228:231], v[112:127]\n"
      "v_exp_f32 v164, v164\n"
      "v_exp_f32 v165, v165\n"
      "v_exp_f32 v166, v166\n"
      "v_exp_f32 v167, v167\n"
      "v_smfmac_f32_32x32x32_bf16 v[80:95], v[232:235], v[144:151], v15\n"
      "v_exp_f32 v168, v168\n"
      "v_exp_f32 v169, v169\n"
      "v_exp_f32 v170, v170\n"
      "v_exp_f32 v171, v171\n"
      "v_smfmac_f32_32x32x32_bf16 v[80:95], v[236:239], v[152:159], v15\n"
      "v_exp_f32 v172, v172\n"
      "v_exp_f32 v173, v173\n"
      "v_exp_f32 v174, v174\n"
      "v_exp_f32 v175, v175\n"
      "v_mfma_f32_32x32x16_bf16 v[128:143], a[176:179], a[232:235], 0\n"
      "v_exp_f32 v112, v112\n"
      "v_exp_f32 v113, v113\n"
      "v_exp_f32 v114, v114\n"
      "v_exp_f32 v115, v115\n"
      "v_mfma_f32_32x32x16_bf16 v[128:143], a[180:183], a[236:239], v[128:143]\n"
      "v_exp_f32 v116, v116\n"
      "v_exp_f32 v117, v117\n"
      "v_exp_f32 v118, v118\n"
      "v_exp_f32 v119, v119\n"
      "v_smfmac_f32_32x32x32_bf16 v[96:111], v[232:235], v[160:167], v15\n"
      "v_exp_f32 v120, v120\n"
      "v_exp_f32 v121, v121\n"
      "v_exp_f32 v122, v122\n"
      "v_exp_f32 v123, v123\n"
      "v_smfmac_f32_32x32x32_bf16 v[96:111], v[236:239], v[168:175], v15\n"
      "v_exp_f32 v124, v124\n"
      "v_exp_f32 v125, v125\n"
      "v_exp_f32 v126, v126\n"
      "v_exp_f32 v127, v127\n"
      "v_mfma_f32_32x32x16_bf16 v[144:159], a[176:179], a[240:243], 0\n"
      "buffer_load_dwordx4 v[224:227], v30, s[4:7], s74 offen sc1\n"
      "buffer_load_dwordx4 v[228:231], v30, s[4:7], s74 offen offset:1024 sc1\n"
      "buffer_load_dwordx4 v[232:235], v30, s[4:7], s74 offen offset:2048 sc1\n"
      "buffer_load_dwordx4 v[236:239], v30, s[4:7], s74 offen offset:3072 sc1\n"
      "v_exp_f32 v128, v128\n"
      "v_exp_f32 v129, v129\n"
      "v_exp_f32 v130, v130\n"
      "v_exp_f32 v131, v131\n"
      "v_mfma_f32_32x32x16_bf16 v[144:159], a[180:183], a[244:247], v[144:159]\n"
      "v_exp_f32 v132, v132\n"
      "v_exp_f32 v133, v133\n"
      "v_exp_f32 v134, v134\n"
      "v_exp_f32 v135, v135\n"
      "v_smfmac_f32_32x32x32_bf16 v[48:63], a[184:187], v[112:119], v15\n"
      "v_exp_f32 v136, v136\n"
      "v_exp_f32 v137, v137\n"
      "v_exp_f32 v138, v138\n"
      "v_exp_f32 v139, v139\n"
      "v_smfmac_f32_32x32x32_bf16 v[48:63], a[188:191], v[120:127], v15\n"
      "v_exp_f32 v140, v140\n"
      "v_exp_f32 v141, v141\n"
      "v_exp_f32 v142, v142\n"
      "v_exp_f32 v143, v143\n"
      "v_mfma_f32_32x32x16_bf16 v[160:175], a[176:179], a[248:251], 0\n"
      "v_exp_f32 v144, v144\n"
      "v_exp_f32 v145, v145\n"
      "v_exp_f32 v146, v146\n"
      "v_exp_f32 v147, v147\n"
      "v_mfma_f32_32x32x16_bf16 v[160:175], a[180:183], a[252:255], v[160:175]\n"
      "v_exp_f32 v148, v148\n"
      "v_exp_f32 v149, v149\n"
      "v_exp_f32 v150, v150\n"
      "v_exp_f32 v151, v151\n"
      "v_smfmac_f32_32x32x32_bf16 v[64:79], a[184:187], v[128:135], v15\n"
      "v_exp_f32 v152, v152\n"
      "v_exp_f32 v153, v153\n"
      "v_exp_f32 v154, v154\n"
      "v_exp_f32 v155, v155\n"
      "v_smfmac_f32_32x32x32_bf16 v[64:79], a[188:191], v[136:143], v15\n"
      "v_exp_f32 v156, v156\n"
      "v_exp_f32 v157, v157\n"
      "v_exp_f32 v158, v158\n"
      "v_exp_f32 v159, v159\n"
      "s_waitcnt vmcnt(12)\n"
      "v_mfma_f32_32x32x16_bf16 v[112:127], a[192:195], a[224:227], 0\n"
      "s_cbranch_scc1 L_po_36\n"
      "buffer_load_dwordx4 a[144:147], v206, s[8:11], s38 offen\n"
      "buffer_load_dwordx4 a[148:151], v207, s[8:11], s38 offen\n"
      "s_addk_i32 s38, 14336\n"
      "buffer_store_dwordx4 v[200:203], v3, s[4:7], s56 offen offset:0 sc1\n"
      "buffer_store_dwordx4 v[200:203], v3, s[4:7], s56 offen offset:1024 sc1\n"
      "L_po_36:\n"
      "v_exp_f32 v160, v160\n"
      "v_exp_f32 v161, v161\n"
      "v_exp_f32 v162, v162\n"
      "v_exp_f32 v163, v163\n"
      "v_mfma_f32_32x32x16_bf16 v[112:127], a[196:199], a[228:231], v[112:127]\n"
      "v_exp_f32 v164, v164\n"
      "v_exp_f32 v165, v165\n"
      "v_exp_f32 v166, v166\n"
      "v_exp_f32 v167, v167\n"
      "v_smfmac_f32_32x32x32_bf16 v[80:95], a[184:187], v[144:151], v15\n"
      "v_exp_f32 v168, v168\n"
      "v_exp_f32 v169, v169\n"
      "v_exp_f32 v170, v170\n"
      "v_exp_f32 v171, v171\n"
      "v_smfmac_f32_32x32x32_bf16 v[80:95], a[188:191], v[152:159], v15\n"
      "v_exp_f32 v172, v172\n"
      "v_exp_f32 v173, v173\n"
      "v_exp_f32 v174, v174\n"
      "v_exp_f32 v175, v175\n"
      "v_mfma_f32_32x32x16_bf16 v[128:143], a[192:195], a[232:235], 0\n"
      "v_exp_f32 v112, v112\n"
      "v_exp_f32 v113, v113\n"
      "v_exp_f32 v114, v114\n"
      "v_exp_f32 v115, v115\n"
      "v_mfma_f32_32x32x16_bf16 v[128:143], a[196:199], a[236:239], v[128:143]\n"
      "v_exp_f32 v116, v116\n"
      "v_exp_f32 v117, v117\n"
      "v_exp_f32 v118, v118\n"
      "v_exp_f32 v119, v119\n"
      "v_smfmac_f32_32x32x32_bf16 v[96:111], a[184:187], v[160:167], v15\n"
      "v_exp_f32 v120, v120\n"
      "v_exp_f32 v121, v121\n"
      "v_exp_f32 v122, v122\n"
      "v_exp_f32 v123, v123\n"
      "v_smfmac_f32_32x32x32_bf16 v[96:111], a[188:191], v[168:175], v15\n"
      "v_exp_f32 v124, v124\n"
      "v_exp_f32 v125, v125\n"
      "v_exp_f32 v126, v126\n"
      "v_exp_f32 v127, v127\n"
      "v_mfma_f32_32x32x16_bf16 v[144:159], a[192:195], a[240:243], 0\n"
      "buffer_load_dwordx4 a[176:179], v30, s[4:7], s75 offen sc1\n"
      "buffer_load_dwordx4 a[180:183], v30, s[4:7], s75 offen offset:1024 sc1\n"
      "buffer_load_dwordx4 a[184:187], v30, s[4:7], s75 offen offset:2048 sc1\n"
      "buffer_load_dwordx4 a[188:191], v30, s[4:7], s75 offen offset:3072 sc1\n"
      "v_exp_f32 v128, v128\n"
      "v_exp_f32 v129, v129\n"
      "v_exp_f32 v130, v130\n"
      "v_exp_f32 v131, v131\n"
      "v_mfma_f32_32x32x16_bf16 v[144:159], a[196:199], a[244:247], v[144:159]\n"
      "v_exp_f32 v132, v132\n"
      "v_exp_f32 v133, v133\n"
      "v_exp_f32 v134, v134\n"
      "v_exp_f32 v135, v135\n"
      "v_smfmac_f32_32x32x32_bf16 v[48:63], a[200:203], v[112:119], v15\n"
      "v_exp_f32 v136, v136\n"
      "v_exp_f32 v137, v137\n"
      "v_exp_f32 v138, v138\n"
      "v_exp_f32 v139, v139\n"
      "v_smfmac_f32_32x32x32_bf16 v[48:63], a[204:207], v[120:127], v15\n"
      "v_exp_f32 v140, v140\n"
      "v_exp_f32 v141, v141\n"
      "v_exp_f32 v142, v142\n"
      "v_exp_f32 v143, v143\n"
      "v_mfma_f32_32x32x16_bf16 v[160:175], a[192:195], a[248:251], 0\n"
      "v_exp_f32 v144, v144\n"
      "v_exp_f32 v145, v145\n"
      "v_exp_f32 v146, v146\n"
      "v_exp_f32 v147, v147\n"
      "v_mfma_f32_32x32x16_bf16 v[160:175], a[196:199], a[252:255], v[160:175]\n"
      "v_exp_f32 v148, v148\n"
      "v_exp_f32 v149, v149\n"
      "v_exp_f32 v150, v150\n"
      "v_exp_f32 v151, v151\n"
      "v_smfmac_f32_32x32x32_bf16 v[64:79], a[200:203], v[128:135], v15\n"
      "v_exp_f32 v152, v152\n"
      "v_exp_f32 v153, v153\n"
      "v_exp_f32 v154, v154\n"
      "v_exp_f32 v155, v155\n"
      "v_smfmac_f32_32x32x32_bf16 v[64:79], a[204:207], v[136:143], v15\n"
      "v_exp_f32 v156, v156\n"
      "v_exp_f32 v157, v157\n"
      "v_exp_f32 v158, v158\n"
      "v_exp_f32 v159, v159\n"
      "s_waitcnt vmcnt(12)\n"
      "v_mfma_f32_32x32x16_bf16 v[112:127], a[208:211], a[224:227], 0\n"
      "s_cbranch_scc1 L_po_37\n"
      "buffer_load_dwordx4 a[152:155], v206, s[8:11], s38 offen\n"
      "buffer_load_dwordx4 a[156:159], v207, s[8:11], s38 offen\n"
      "s_addk_i32 s38, 14336\n"
      "buffer_store_dword v200, v9, s[4:7], s57 offen offset:0 sc1\n"
      "buffer_store_dwordx4 v[200:203], v0, s[4:7], s55 offen offset:0 sc1\n"
      "L_po_37:\n"
      "v_exp_f32 v160, v160\n"
      "v_exp_f32 v161, v161\n"
      "v_exp_f32 v162, v162\n"
      "v_exp_f32 v163, v163\n"
      "v_mfma_f32_32x32x16_bf16 v[112:127], a[212:215], a[228:231], v[112:127]\n"
      "v_exp_f32 v164, v164\n"
      "v_exp_f32 v165, v165\n"
      "v_exp_f32 v166, v166\n"
      "v_exp_f32 v167, v167\n"
      "v_smfmac_f32_32x32x32_bf16 v[80:95], a[200:203], v[144:151], v15\n"
      "v_exp_f32 v168, v168\n"
      "v_exp_f32 v169, v169\n"
      "v_exp_f32 v170, v170\n"
      "v_exp_f32 v171, v171\n"
      "v_smfmac_f32_32x32x32_bf16 v[80:95], a[204:207], v[152:159], v15\n"
      "v_exp_f32 v172, v172\n"
      "v_exp_f32 v173, v173\n"
      "v_exp_f32 v174, v174\n"
      "v_exp_f32 v175, v175\n"
      "v_mfma_f32_32x32x16_bf16 v[128:143], a[208:211], a[232:235], 0\n"
      "v_exp_f32 v112, v112\n"
      "v_exp_f32 v113, v113\n"
      "v_exp_f32 v114, v114\n"
      "v_exp_f32 v115, v115\n"
      "v_mfma_f32_32x32x16_bf16 v[128:143], a[212:215], a[236:239], v[128:143]\n"
      "v_exp_f32 v116, v116\n"
      "v_exp_f32 v117, v117\n"
      "v_exp_f32 v118, v118\n"
      "v_exp_f32 v119, v119\n"
      "v_smfmac_f32_32x32x32_bf16 v[96:111], a[200:203], v[160:167], v15\n"
      "v_exp_f32 v120, v120\n"
      "v_exp_f32 v121, v121\n"
      "v_exp_f32 v122, v122\n"
      "v_exp_f32 v123, v123\n"
      "v_smfmac_f32_32x32x32_bf16 v[96:111], a[204:207], v[168:175], v15\n"
      "v_exp_f32 v124, v124\n"
      "v_exp_f32 v125, v125\n"
      "v_exp_f32 v126, v126\n"
      "v_exp_f32 v127, v127\n"
      "v_mfma_f32_32x32x16_bf16 v[144:159], a[208:211], a[240:243], 0\n"
      "buffer_load_dwordx4 a[192:195], v30, s[4:7], s76 offen sc1\n"
      "buffer_load_dwordx4 a[196:199], v30, s[4:7], s76 offen offset:1024 sc1\n"
      "buffer_load_dwordx4 a[200:203], v30, s[4:7], s76 offen offset:2048 sc1\n"
      "buffer_load_dwordx4 a[204:207], v30, s[4:7], s76 offen offset:3072 sc1\n"
      "v_exp_f32 v128, v128\n"
      "v_exp_f32 v129, v129\n"
      "v_exp_f32 v130, v130\n"
      "v_exp_f32 v131, v131\n"
      "v_mfma_f32_32x32x16_bf16 v[144:159], a[212:215], a[244:247], v[144:159]\n"
      "v_exp_f32 v132, v132\n"
      "v_exp_f32 v133, v133\n"
      "v_exp_f32 v134, v134\n"
      "v_exp_f32 v135, v135\n"
      "v_smfmac_f32_32x32x32_bf16 v[48:63], a[216:219], v[112:119], v15\n"
      "v_exp_f32 v136, v136\n"
      "v_exp_f32 v137, v137\n"
      "v_exp_f32 v138, v138\n"
      "v_exp_f32 v139, v139\n"
      "v_smfmac_f32_32x32x32_bf16 v[48:63], a[220:223], v[120:127], v15\n"
      "v_exp_f32 v140, v140\n"
      "v_exp_f32 v141, v141\n"
      "v_exp_f32 v142, v142\n"
      "v_exp_f32 v143, v143\n"
      "v_mfma_f32_32x32x16_bf16 v[160:175], a[208:211], a[248:251], 0\n"
      "v_exp_f32 v144, v144\n"
      "v_exp_f32 v145, v145\n"
      "v_exp_f32 v146, v146\n"
      "v_exp_f32 v147, v147\n"
      "v_mfma_f32_32x32x16_bf16 v[160:175], a[212:215], a[252:255], v[160:175]\n"
      "v_exp_f32 v148, v148\n"
      "v_exp_f32 v149, v149\n"
      "v_exp_f32 v150, v150\n"
      "v_exp_f32 v151, v151\n"
      "v_smfmac_f32_32x32x32_bf16 v[64:79], a[216:219], v[128:135], v15\n"
      "v_exp_f32 v152, v152\n"
      "v_exp_f32 v153, v153\n"
      "v_exp_f32 v154, v154\n"
      "v_exp_f32 v155, v155\n"
      "v_smfmac_f32_32x32x32_bf16 v[64:79], a[220:223], v[136:143], v15\n"
      "v_exp_f32 v156, v156\n"
      "v_exp_f32 v157, v157\n"
      "v_exp_f32 v158, v158\n"
      "v_exp_f32 v159, v159\n"
      "s_waitcnt vmcnt(12)\n"
      "v_mfma_f32_32x32x16_bf16 v[112:127], v[240:243], a[224:227], 0\n"
      "s_cbranch_scc1 L_po_38\n"
      "buffer_load_dwordx4 a[160:163], v206, s[8:11], s38 offen\n"
      "buffer_load_dwordx4 a[164:167], v207, s[8:11], s38 offen\n"
      "s_addk_i32 s38, 14336\n"
      "buffer_store_dwordx4 v[200:203], v0, s[4:7], s55 offen offset:1024 sc1\n"
      "buffer_store_dwordx4 v[200:203], v0, s[4:7], s53 offen offset:0 sc1\n"
      "L_po_38:\n"
      "v_exp_f32 v160, v160\n"
      "v_exp_f32 v161, v161\n"
      "v_exp_f32 v162, v162\n"
      "v_exp_f32 v163, v163\n"
      "v_mfma_f32_32x32x16_bf16 v[112:127], v[244:247], a[228:231], v[112:127]\n"
      "v_exp_f32 v164, v164\n"
      "v_exp_f32 v165, v165\n"
      "v_exp_f32 v166, v166\n"
      "v_exp_f32 v167, v167\n"
      "v_smfmac_f32_32x32x32_bf16 v[80:95], a[216:219], v[144:151], v15\n"
      "v_exp_f32 v168, v168\n"
      "v_exp_f32 v169, v169\n"
      "v_exp_f32 v170, v170\n"
      "v_exp_f32 v171, v171\n"
      "v_smfmac_f32_32x32x32_bf16 v[80:95], a[220:223], v[152:159], v15\n"
      "v_exp_f32 v172, v172\n"
      "v_exp_f32 v173, v173\n"
      "v_exp_f32 v174, v174\n"
      "v_exp_f32 v175, v175\n"
      "v_mfma_f32_32x32x16_bf16 v[128:143], v[240:243], a[232:235], 0\n"
      "v_exp_f32 v112, v112\n"
      "v_exp_f32 v113, v113\n"
      "v_exp_f32 v114, v114\n"
      "v_exp_f32 v115, v115\n"
      "v_mfma_f32_32x32x16_bf16 v[128:143], v[244:247], a[236:239], v[128:143]\n"
      "v_exp_f32 v116, v116\n"
      "v_exp_f32 v117, v117\n"
      "v_exp_f32 v118, v118\n"
      "v_exp_f32 v119, v119\n"
      "v_smfmac_f32_32x32x32_bf16 v[96:111], a[216:219], v[160:167], v15\n"
      "v_exp_f32 v120, v120\n"
      "v_exp_f32 v121, v121\n"
      "v_exp_f32 v122, v122\n"
      "v_exp_f32 v123, v123\n"
      "v_smfmac_f32_32x32x32_bf16 v[96:111], a[220:223], v[168:175], v15\n"
      "v_exp_f32 v124, v124\n"
      "v_exp_f32 v125, v125\n"
      "v_exp_f32 v126, v126\n"
      "v_exp_f32 v127, v127\n"
      "v_mfma_f32_32x32x16_bf16 v[144:159], v[240:243], a[240:243], 0\n"
      "buffer_load_dwordx4 a[208:211], v30, s[4:7], s77 offen sc1\n"
      "buffer_load_dwordx4 a[212:215], v30, s[4:7], s77 offen offset:1024 sc1\n"
      "buffer_load_dwordx4 a[216:219], v30, s[4:7], s77 offen offset:2048 sc1\n"
      "buffer_load_dwordx4 a[220:223], v30, s[4:7], s77 offen offset:3072 sc1\n"
      "v_exp_f32 v128, v128\n"
      "v_exp_f32 v129, v129\n"
      "v_exp_f32 v130, v130\n"
      "v_exp_f32 v131, v131\n"
      "v_mfma_f32_32x32x16_bf16 v[144:159], v[244:247], a[244:247], v[144:159]\n"
      "v_exp_f32 v132, v132\n"
      "v_exp_f32 v133, v133\n"
      "v_exp_f32 v134, v134\n"
      "v_exp_f32 v135, v135\n"
      "v_smfmac_f32_32x32x32_bf16 v[48:63], v[248:251], v[112:119], v15\n"
      "v_exp_f32 v136, v136\n"
      "v_exp_f32 v137, v137\n"
      "v_exp_f32 v138, v138\n"
      "v_exp_f32 v139, v139\n"
      "v_smfmac_f32_32x32x32_bf16 v[48:63], v[252:255], v[120:127], v15\n"
      "v_exp_f32 v140, v140\n"
      "v_exp_f32 v141, v141\n"
      "v_exp_f32 v142, v142\n"
      "v_exp_f32 v143, v143\n"
      "v_mfma_f32_32x32x16_bf16 v[160:175], v[240:243], a[248:251], 0\n"
      "v_exp_f32 v144, v144\n"
      "v_exp_f32 v145, v145\n"
      "v_exp_f32 v146, v146\n"
      "v_exp_f32 v147, v147\n"
      "v_mfma_f32_32x32x16_bf16 v[160:175], v[244:247], a[252:255], v[160:175]\n"
      "v_exp_f32 v148, v148\n"
      "v_exp_f32 v149, v149\n"
      "v_exp_f32 v150, v150\n"
      "v_exp_f32 v151, v151\n"
      "v_smfmac_f32_32x32x32_bf16 v[64:79], v[248:251], v[128:135], v15\n"
      "v_exp_f32 v152, v152\n"
      "v_exp_f32 v153, v153\n"
      "v_exp_f32 v154, v154\n"
      "v_exp_f32 v155, v155\n"
      "v_smfmac_f32_32x32x32_bf16 v[64:79], v[252:255], v[136:143], v15\n"
      "v_exp_f32 v156, v156\n"
      "v_exp_f32 v157, v157\n"
      "v_exp_f32 v158, v158\n"
      "v_exp_f32 v159, v159\n"
      "s_waitcnt vmcnt(12)\n"
      "v_mfma_f32_32x32x16_bf16 v[112:127], v[224:227], a[224:227], 0\n"
      "s_cbranch_scc1 L_po_39\n"
      "buffer_store_dwordx4 v[200:203], v0, s[4:7], s53 offen offset:1024 sc1\n"
      "buffer_store_dwordx4 v[200:203], v0, s[4:7], s53 offen offset:2048 sc1\n"
      "buffer_store_dwordx4 v[200:203], v0, s[4:7], s53 offen offset:3072 sc1\n"
      "L_po_39:\n"
      "v_exp_f32 v160, v160\n"
      "v_exp_f32 v161, v161\n"
      "v_exp_f32 v162, v162\n"
      "v_exp_f32 v163, v163\n"
      "v_mfma_f32_32x32x16_bf16 v[112:127], v[228:231], a[228:231], v[112:127]\n"
      "v_exp_f32 v164, v164\n"
      "v_exp_f32 v165, v165\n"
      "v_exp_f32 v166, v166\n"
      "v_exp_f32 v167, v167\n"
      "v_smfmac_f32_32x32x32_bf16 v[80:95], v[248:251], v[144:151], v15\n"
      "v_exp_f32 v168, v168\n"
      "v_exp_f32 v169, v169\n"
      "v_exp_f32 v170, v170\n"
      "v_exp_f32 v171, v171\n"
      "v_smfmac_f32_32x32x32_bf16 v[80:95], v[252:255], v[152:159], v15\n"
      "v_exp_f32 v172, v172\n"
      "v_exp_f32 v173, v173\n"
      "v_exp_f32 v174, v174\n"
      "v_exp_f32 v175, v175\n"
      "v_mfma_f32_32x32x16_bf16 v[128:143], v[224:227], a[232:235], 0\n"
      "v_exp_f32 v112, v112\n"
      "v_exp_f32 v113, v113\n"
      "v_exp_f32 v114, v114\n"
      "v_exp_f32 v115, v115\n"
      "v_mfma_f32_32x32x16_bf16 v[128:143], v[228:231], a[236:239], v[128:143]\n"
      "v_exp_f32 v116, v116\n"
      "v_exp_f32 v117, v117\n"
      "v_exp_f32 v118, v118\n"
      "v_exp_f32 v119, v119\n"
      "v_smfmac_f32_32x32x32_bf16 v[96:111], v[248:251], v[160:167], v15\n"
      "v_exp_f32 v120, v120\n"
      "v_exp_f32 v121, v121\n"
      "v_exp_f32 v122, v122\n"
      "v_exp_f32 v123, v123\n"
      "v_smfmac_f32_32x32x32_bf16 v[96:111], v[252:255], v[168:175], v15\n"
      "v_exp_f32 v124, v124\n"
      "v_exp_f32 v125, v125\n"
      "v_exp_f32 v126, v126\n"
      "v_exp_f32 v127, v127\n"
      "v_mfma_f32_32x32x16_bf16 v[144:159], v[224:227], a[240:243], 0\n"
      "buffer_load_dwordx4 v[240:243], v30, s[4:7], s78 offen sc1\n"
      "buffer_load_dwordx4 v[244:247], v30, s[4:7], s78 offen offset:1024 sc1\n"
      "buffer_load_dwordx4 v[248:251], v30, s[4:7], s78 offen offset:2048 sc1\n"
      "buffer_load_dwordx4 v[252:255], v30, s[4:7], s78 offen offset:3072 sc1\n"
      "v_exp_f32 v128, v128\n"
      "v_exp_f32 v129, v129\n"
      "v_exp_f32 v130, v130\n"
      "v_exp_f32 v131, v131\n"
      "v_mfma_f32_32x32x16_bf16 v[144:159], v[228:231], a[244:247], v[144:159]\n"
      "v_exp_f32 v132, v132\n"
      "v_exp_f32 v133, v133\n"
      "v_exp_f32 v134, v134\n"
      "v_exp_f32 v135, v135\n"
      "v_smfmac_f32_32x32x32_bf16 v[48:63], v[232:235], v[112:119], v15\n"
      "v_exp_f32 v136, v136\n"
      "v_exp_f32 v137, v137\n"
      "v_exp_f32 v138, v138\n"
      "v_exp_f32 v139, v139\n"
      "v_smfmac_f32_32x32x32_bf16 v[48:63], v[236:239], v[120:127], v15\n"
      "v_exp_f32 v140, v140\n"
      "v_exp_f32 v141, v141\n"
      "v_exp_f32 v142, v142\n"
      "v_exp_f32 v143, v143\n"
      "v_mfma_f32_32x32x16_bf16 v[160:175], v[224:227], a[248:251], 0\n"
      "v_exp_f32 v144, v144\n"
      "v_exp_f32 v145, v145\n"
      "v_exp_f32 v146, v146\n"
      "v_exp_f32 v147, v147\n"
      "v_mfma_f32_32x32x16_bf16 v[160:175], v[228:231], a[252:255], v[160:175]\n"
      "v_exp_f32 v148, v148\n"
      "v_exp_f32 v149, v149\n"
      "v_exp_f32 v150, v150\n"
      "v_exp_f32 v151, v151\n"
      "v_smfmac_f32_32x32x32_bf16 v[64:79], v[232:235], v[128:135], v15\n"
      "v_exp_f32 v152, v152\n"
      "v_exp_f32 v153, v153\n"
      "v_exp_f32 v154, v154\n"
      "v_exp_f32 v155, v155\n"
      "v_smfmac_f32_32x32x32_bf16 v[64:79], v[236:239], v[136:143], v15\n"
      "v_exp_f32 v156, v156\n"
      "v_exp_f32 v157, v157\n"
      "v_exp_f32 v158, v158\n"
      "v_exp_f32 v159, v159\n"
      "s_waitcnt vmcnt(12)\n"
      "v_mfma_f32_32x32x16_bf16 v[112:127], a[176:179], a[224:227], 0\n"
      "v_exp_f32 v160, v160\n"
      "v_exp_f32 v161, v161\n"
      "v_exp_f32 v162, v162\n"
      "v_exp_f32 v163, v163\n"
      "v_mfma_f32_32x32x16_bf16 v[112:127], a[180:183], a[228:231], v[112:127]\n"
      "v_exp_f32 v164, v164\n"
      "v_exp_f32 v165, v165\n"
      "v_exp_f32 v166, v166\n"
      "v_exp_f32 v167, v167\n"
      "v_smfmac_f32_32x32x32_bf16 v[80:95], v[232:235], v[144:151], v15\n"
      "v_exp_f32 v168, v168\n"
      "v_exp_f32 v169, v169\n"
      "v_exp_f32 v170, v170\n"
      "v_exp_f32 v171, v171\n"
      "v_smfmac_f32_32x32x32_bf16 v[80:95], v[236:239], v[152:159], v15\n"
      "v_exp_f32 v172, v172\n"
      "v_exp_f32 v173, v173\n"
      "v_exp_f32 v174, v174\n"
      "v_exp_f32 v175, v175\n"
      "v_mfma_f32_32x32x16_bf16 v[128:143], a[176:179], a[232:235], 0\n"
      "v_exp_f32 v112, v112\n"
      "v_exp_f32 v113, v113\n"
      "v_exp_f32 v114, v114\n"
      "v_exp_f32 v115, v115\n"
      "v_mfma_f32_32x32x16_bf16 v[128:143], a[180:183], a[236:239], v[128:143]\n"
      "v_exp_f32 v116, v116\n"
      "v_exp_f32 v117, v117\n"
      "v_exp_f32 v118, v118\n"
      "v_exp_f32 v119, v119\n"
      "v_smfmac_f32_32x32x32_bf16 v[96:111], v[232:235], v[160:167], v15\n"
      "v_exp_f32 v120, v120\n"
      "v_exp_f32 v121, v121\n"
      "v_exp_f32 v122, v122\n"
      "v_exp_f32 v123, v123\n"
      "v_smfmac_f32_32x32x32_bf16 v[96:111], v[236:239], v[168:175], v15\n"
      "v_exp_f32 v124, v124\n"
      "v_exp_f32 v125, v125\n"
      "v_exp_f32 v126, v126\n"
      "v_exp_f32 v127, v127\n"
      "v_mfma_f32_32x32x16_bf16 v[144:159], a[176:179], a[240:243], 0\n"
      "v_exp_f32 v128, v128\n"
      "v_exp_f32 v129, v129\n"
      "v_exp_f32 v130, v130\n"
      "v_exp_f32 v131, v131\n"
      "v_mfma_f32_32x32x16_bf16 v[144:159], a[180:183], a[244:247], v[144:159]\n"
      "v_exp_f32 v132, v132\n"
      "v_exp_f32 v133, v133\n"
      "v_exp_f32 v134, v134\n"
      "v_exp_f32 v135, v135\n"
      "v_smfmac_f32_32x32x32_bf16 v[48:63], a[184:187], v[112:119], v15\n"
      "v_exp_f32 v136, v136\n"
      "v_exp_f32 v137, v137\n"
      "v_exp_f32 v138, v138\n"
      "v_exp_f32 v139, v139\n"
      "v_smfmac_f32_32x32x32_bf16 v[48:63], a[188:191], v[120:127], v15\n"
      "v_exp_f32 v140, v140\n"
      "v_exp_f32 v141, v141\n"
      "v_exp_f32 v142, v142\n"
      "v_exp_f32 v143, v143\n"
      "v_mfma_f32_32x32x16_bf16 v[160:175], a[176:179], a[248:251], 0\n"
      "v_exp_f32 v144, v144\n"
      "v_exp_f32 v145, v145\n"
      "v_exp_f32 v146, v146\n"
      "v_exp_f32 v147, v147\n"
      "v_mfma_f32_32x32x16_bf16 v[160:175], a[180:183], a[252:255], v[160:175]\n"
      "v_exp_f32 v148, v148\n"
      "v_exp_f32 v149, v149\n"
      "v_exp_f32 v150, v150\n"
      "v_exp_f32 v151, v151\n"
      "v_smfmac_f32_32x32x32_bf16 v[64:79], a[184:187], v[128:135], v15\n"
      "v_exp_f32 v152, v152\n"
      "v_exp_f32 v153, v153\n"
      "v_exp_f32 v154, v154\n"
      "v_exp_f32 v155, v155\n"
      "v_smfmac_f32_32x32x32_bf16 v[64:79], a[188:191], v[136:143], v15\n"
      "v_exp_f32 v156, v156\n"
      "v_exp_f32 v157, v157\n"
      "v_exp_f32 v158, v158\n"
      "v_exp_f32 v159, v159\n"
      "s_waitcnt vmcnt(8)\n"
      "v_mfma_f32_32x32x16_bf16 v[112:127], a[192:195], a[224:227], 0\n"
      "v_exp_f32 v160, v160\n"
      "v_exp_f32 v161, v161\n"
      "v_exp_f32 v162, v162\n"
      "v_exp_f32 v163, v163\n"
      "v_mfma_f32_32x32x16_bf16 v[112:127], a[196:199], a[228:231], v[112:127]\n"
      "v_exp_f32 v164, v164\n"
      "v_exp_f32 v165, v165\n"
      "v_exp_f32 v166, v166\n"
      "v_exp_f32 v167, v167\n"
      "v_smfmac_f32_32x32x32_bf16 v[80:95], a[184:187], v[144:151], v15\n"
      "v_exp_f32 v168, v168\n"
      "v_exp_f32 v169, v169\n"
      "v_exp_f32 v170, v170\n"
      "v_exp_f32 v171, v171\n"
      "v_smfmac_f32_32x32x32_bf16 v[80:95], a[188:191], v[152:159], v15\n"
      "v_exp_f32 v172, v172\n"
      "v_exp_f32 v173, v173\n"
      "v_exp_f32 v174, v174\n"
      "v_exp_f32 v175, v175\n"
      "s_waitcnt vmcnt(4)\n"
      "v_mfma_f32_32x32x16_bf16 v[128:143], a[208:211], a[224:227], 0\n"
      "v_exp_f32 v112, v112\n"
      "v_exp_f32 v113, v113\n"
      "v_exp_f32 v114, v114\n"
      "v_exp_f32 v115, v115\n"
      "v_mfma_f32_32x32x16_bf16 v[128:143], a[212:215], a[228:231], v[128:143]\n"
      "v_exp_f32 v116, v116\n"
      "v_exp_f32 v117, v117\n"
      "v_exp_f32 v118, v118\n"
      "v_exp_f32 v119, v119\n"
      "v_smfmac_f32_32x32x32_bf16 v[96:111], a[184:187], v[160:167], v15\n"
      "v_exp_f32 v120, v120\n"
      "v_exp_f32 v121, v121\n"
      "v_exp_f32 v122, v122\n"
      "v_exp_f32 v123, v123\n"
      "v_smfmac_f32_32x32x32_bf16 v[96:111], a[188:191], v[168:175], v15\n"
      "v_exp_f32 v124, v124\n"
      "v_exp_f32 v125, v125\n"
      "v_exp_f32 v126, v126\n"
      "v_exp_f32 v127, v127\n"
      "s_waitcnt vmcnt(0)\n"
      "v_mfma_f32_32x32x16_bf16 v[144:159], v[240:243], a[224:227], 0\n"
      "v_exp_f32 v128, v128\n"
      "v_exp_f32 v129, v129\n"
      "v_exp_f32 v130, v130\n"
      "v_exp_f32 v131, v131\n"
      "v_mfma_f32_32x32x16_bf16 v[144:159], v[244:247], a[228:231], v[144:159]\n"
      "v_exp_f32 v132, v132\n"
      "v_exp_f32 v133, v133\n"
      "v_exp_f32 v134, v134\n"
      "v_exp_f32 v135, v135\n"
      "v_smfmac_f32_32x32x32_bf16 v[48:63], a[200:203], v[112:119], v15\n"
      "v_exp_f32 v136, v136\n"
      "v_exp_f32 v137, v137\n"
      "v_exp_f32 v138, v138\n"
      "v_exp_f32 v139, v139\n"
      "v_smfmac_f32_32x32x32_bf16 v[48:63], a[204:207], v[120:127], v15\n"
      "v_exp_f32 v140, v140\n"
      "v_exp_f32 v141, v141\n"
      "v_exp_f32 v142, v142\n"
      "v_exp_f32 v143, v143\n"
      "v_mfma_f32_32x32x16_bf16 v[160:175], a[192:195], a[232:235], 0\n"
      "v_exp_f32 v144, v144\n"
      "v_exp_f32 v145, v145\n"
      "v_exp_f32 v146, v146\n"
      "v_exp_f32 v147, v147\n"
      "v_mfma_f32_32x32x16_bf16 v[160:175], a[196:199], a[236:239], v[160:175]\n"
      "v_exp_f32 v148, v148\n"
      "v_exp_f32 v149, v149\n"
      "v_exp_f32 v150, v150\n"
      "v_exp_f32 v151, v151\n"
      "v_smfmac_f32_32x32x32_bf16 v[48:63], a[216:219], v[128:135], v15\n"
      "v_exp_f32 v152, v152\n"
      "v_exp_f32 v153, v153\n"
      "v_exp_f32 v154, v154\n"
      "v_exp_f32 v155, v155\n"
      "v_smfmac_f32_32x32x32_bf16 v[48:63], a[220:223], v[136:143], v15\n"
      "v_exp_f32 v156, v156\n"
      "v_exp_f32 v157, v157\n"
      "v_exp_f32 v158, v158\n"
      "v_exp_f32 v159, v159\n"
      "v_mfma_f32_32x32x16_bf16 v[112:127], a[208:211], a[232:235], 0\n"
      "v_exp_f32 v160, v160\n"
      "v_exp_f32 v161, v161\n"
      "v_exp_f32 v162, v162\n"
      "v_exp_f32 v163, v163\n"
      "v_mfma_f32_32x32x16_bf16 v[112:127], a[212:215], a[236:239], v[112:127]\n"
      "v_exp_f32 v164, v164\n"
      "v_exp_f32 v165, v165\n"
      "v_exp_f32 v166, v166\n"
      "v_exp_f32 v167, v167\n"
      "v_smfmac_f32_32x32x32_bf16 v[48:63], v[248:251], v[144:151], v15\n"
      "v_exp_f32 v168, v168\n"
      "v_exp_f32 v169, v169\n"
      "v_exp_f32 v170, v170\n"
      "v_exp_f32 v171, v171\n"
      "v_smfmac_f32_32x32x32_bf16 v[48:63], v[252:255], v[152:159], v15\n"
      "v_exp_f32 v172, v172\n"
      "v_exp_f32 v173, v173\n"
      "v_exp_f32 v174, v174\n"
      "v_exp_f32 v175, v175\n"
      "v_mfma_f32_32x32x16_bf16 v[128:143], v[240:243], a[232:235], 0\n"
      "v_exp_f32 v112, v112\n"
      "v_exp_f32 v113, v113\n"
      "v_exp_f32 v114, v114\n"
      "v_exp_f32 v115, v115\n"
      "v_mfma_f32_32x32x16_bf16 v[128:143], v[244:247], a[236:239], v[128:143]\n"
      "ds_write_b128 v16, v[48:51] offset:2048\n"
      "v_exp_f32 v116, v116\n"
      "v_exp_f32 v117, v117\n"
      "v_exp_f32 v118, v118\n"
      "v_exp_f32 v119, v119\n"
      "v_smfmac_f32_32x32x32_bf16 v[64:79], a[200:203], v[160:167], v15\n"
      "v_exp_f32 v120, v120\n"
      "v_exp_f32 v121, v121\n"
      "v_exp_f32 v122, v122\n"
      "v_exp_f32 v123, v123\n"
      "v_smfmac_f32_32x32x32_bf16 v[64:79], a[204:207], v[168:175], v15\n"
      "ds_write_b128 v16, v[52:55] offset:3072\n"
      "v_exp_f32 v124, v124\n"
      "v_exp_f32 v125, v125\n"
      "v_exp_f32 v126, v126\n"
      "v_exp_f32 v127, v127\n"
      "v_mfma_f32_32x32x16_bf16 v[144:159], a[192:195], a[240:243], 0\n"
      "v_exp_f32 v128, v128\n"
      "v_exp_f32 v129, v129\n"
      "v_exp_f32 v130, v130\n"
      "v_exp_f32 v131, v131\n"
      "v_mfma_f32_32x32x16_bf16 v[144:159], a[196:199], a[244:247], v[144:159]\n"
      "ds_write_b128 v16, v[56:59] offset:4096\n"
      "v_exp_f32 v132, v132\n"
      "v_exp_f32 v133, v133\n"
      "v_exp_f32 v134, v134\n"
      "v_exp_f32 v135, v135\n"
      "v_smfmac_f32_32x32x32_bf16 v[64:79], a[216:219], v[112:119], v15\n"
      "v_exp_f32 v136, v136\n"
      "v_exp_f32 v137, v137\n"
      "v_exp_f32 v138, v138\n"
      "v_exp_f32 v139, v139\n"
      "v_smfmac_f32_32x32x32_bf16 v[64:79], a[220:223], v[120:127], v15\n"
      "ds_write_b128 v16, v[60:63] offset:5120\n"
      "v_exp_f32 v140, v140\n"
      "v_exp_f32 v141, v141\n"
      "v_exp_f32 v142, v142\n"
      "v_exp_f32 v143, v143\n"
      "v_mfma_f32_32x32x16_bf16 v[160:175], a[208:211], a[240:243], 0\n"
      "v_exp_f32 v144, v144\n"
      "v_exp_f32 v145, v145\n"
      "v_exp_f32 v146, v146\n"
      "v_exp_f32 v147, v147\n"
      "v_mfma_f32_32x32x16_bf16 v[160:175], a[212:215], a[244:247], v[160:175]\n"
      "v_exp_f32 v148, v148\n"
      "v_exp_f32 v149, v149\n"
      "v_exp_f32 v150, v150\n"
      "v_exp_f32 v151, v151\n"
      "v_smfmac_f32_32x32x32_bf16 v[64:79], v[248:251], v[128:135], v15\n"
      "v_exp_f32 v152, v152\n"
      "v_exp_f32 v153, v153\n"
      "v_exp_f32 v154, v154\n"
      "v_exp_f32 v155, v155\n"
      "v_smfmac_f32_32x32x32_bf16 v[64:79], v[252:255], v[136:143], v15\n"
      "v_exp_f32 v156, v156\n"
      "v_exp_f32 v157, v157\n"
      "v_exp_f32 v158, v158\n"
      "v_exp_f32 v159, v159\n"
      "v_mfma_f32_32x32x16_bf16 v[112:127], v[240:243], a[240:243], 0\n"
      "v_exp_f32 v160, v160\n"
      "v_exp_f32 v161, v161\n"
      "v_exp_f32 v162, v162\n"
      "v_exp_f32 v163, v163\n"
      "v_mfma_f32_32x32x16_bf16 v[112:127], v[244:247], a[244:247], v[112:127]\n"
      "ds_write_b128 v17, v[64:67] offset:6144\n"
      "v_exp_f32 v164, v164\n"
      "v_exp_f32 v165, v165\n"
      "v_exp_f32 v166, v166\n"
      "v_exp_f32 v167, v167\n"
      "v_smfmac_f32_32x32x32_bf16 v[80:95], a[200:203], v[144:151], v15\n"
      "v_exp_f32 v168, v168\n"
      "v_exp_f32 v169, v169\n"
      "v_exp_f32 v170, v170\n"
      "v_exp_f32 v171, v171\n"
      "v_smfmac_f32_32x32x32_bf16 v[80:95], a[204:207], v[152:159], v15\n"
      "ds_write_b128 v17, v[68:71] offset:7168\n"
      "v_exp_f32 v172, v172\n"
      "v_exp_f32 v173, v173\n"
      "v_exp_f32 v174, v174\n"
      "v_exp_f32 v175, v175\n"
      "v_mfma_f32_32x32x16_bf16 v[128:143], a[192:195], a[248:251], 0\n"
      "v_exp_f32 v112, v112\n"
      "v_exp_f32 v113, v113\n"
      "v_exp_f32 v114, v114\n"
      "v_exp_f32 v115, v115\n"
      "v_mfma_f32_32x32x16_bf16 v[128:143], a[196:199], a[252:255], v[128:143]\n"
      "ds_write_b128 v17, v[72:75] offset:8192\n"
      "v_exp_f32 v116, v116\n"
      "v_exp_f32 v117, v117\n"
      "v_exp_f32 v118, v118\n"
      "v_exp_f32 v119, v119\n"
      "v_smfmac_f32_32x32x32_bf16 v[80:95], a[216:219], v[160:167], v15\n"
      "v_exp_f32 v120, v120\n"
      "v_exp_f32 v121, v121\n"
      "v_exp_f32 v122, v122\n"
      "v_exp_f32 v123, v123\n"
      "v_smfmac_f32_32x32x32_bf16 v[80:95], a[220:223], v[168:175], v15\n"
      "ds_write_b128 v17, v[76:79] offset:9216\n"
      "v_exp_f32 v124, v124\n"
      "v_exp_f32 v125, v125\n"
      "v_exp_f32 v126, v126\n"
      "v_exp_f32 v127, v127\n"
      "v_mfma_f32_32x32x16_bf16 v[144:159], a[208:211], a[248:251], 0\n"
      "v_exp_f32 v128, v128\n"
      "v_exp_f32 v129, v129\n"
      "v_exp_f32 v130, v130\n"
      "v_exp_f32 v131, v131\n"
      "v_mfma_f32_32x32x16_bf16 v[144:159], a[212:215], a[252:255], v[144:159]\n"
      "v_exp_f32 v132, v132\n"
      "v_exp_f32 v133, v133\n"
      "v_exp_f32 v134, v134\n"
      "v_exp_f32 v135, v135\n"
      "v_smfmac_f32_32x32x32_bf16 v[80:95], v[248:251], v[112:119], v15\n"
      "v_exp_f32 v136, v136\n"
      "v_exp_f32 v137, v137\n"
      "v_exp_f32 v138, v138\n"
      "v_exp_f32 v139, v139\n"
      "v_smfmac_f32_32x32x32_bf16 v[80:95], v[252:255], v[120:127], v15\n"
      "v_exp_f32 v140, v140\n"
      "v_exp_f32 v141, v141\n"
      "v_exp_f32 v142, v142\n"
      "v_exp_f32 v143, v143\n"
      "v_mfma_f32_32x32x16_bf16 v[160:175], v[240:243], a[248:251], 0\n"
      "v_exp_f32 v144, v144\n"
      "v_exp_f32 v145, v145\n"
      "v_exp_f32 v146, v146\n"
      "v_exp_f32 v147, v147\n"
      "v_mfma_f32_32x32x16_bf16 v[160:175], v[244:247], a[252:255], v[160:175]\n"
      "ds_write_b128 v18, v[80:83] offset:10240\n"
      "v_exp_f32 v148, v148\n"
      "v_exp_f32 v149, v149\n"
      "v_exp_f32 v150, v150\n"
      "v_exp_f32 v151, v151\n"
      "v_smfmac_f32_32x32x32_bf16 v[96:111], a[200:203], v[128:135], v15\n"
      "ds_write_b128 v18, v[84:87] offset:11264\n"
      "v_exp_f32 v152, v152\n"
      "v_exp_f32 v153, v153\n"
      "v_exp_f32 v154, v154\n"
      "v_exp_f32 v155, v155\n"
      "v_smfmac_f32_32x32x32_bf16 v[96:111], a[204:207], v[136:143], v15\n"
      "ds_write_b128 v18, v[88:91] offset:12288\n"
      "v_exp_f32 v156, v156\n"
      "v_exp_f32 v157, v157\n"
      "v_exp_f32 v158, v158\n"
      "v_exp_f32 v159, v159\n"
      "ds_write_b128 v18, v[92:95] offset:13312\n"
      "v_exp_f32 v160, v160\n"
      "v_exp_f32 v161, v161\n"
      "v_exp_f32 v162, v162\n"
      "v_exp_f32 v163, v163\n"
      "v_exp_f32 v164, v164\n"
      "v_exp_f32 v165, v165\n"
      "v_exp_f32 v166, v166\n"
      "v_exp_f32 v167, v167\n"
      "v_smfmac_f32_32x32x32_bf16 v[96:111], a[216:219], v[144:151], v15\n"
      "s_waitcnt lgkmcnt(0)\n"
      "s_barrier\n"
      "ds_read_b128 v[176:179], v19 offset:2048\n"
      "ds_read_b128 v[180:183], v19 offset:3072\n"
      "ds_read_b128 v[184:187], v19 offset:4096\n"
      "ds_read_b128 v[188:191], v19 offset:5120\n"
      "ds_read_b128 v[64:67], v19 offset:6144\n"
      "ds_read_b128 v[68:71], v19 offset:7168\n"
      "v_exp_f32 v168, v168\n"
      "v_exp_f32 v169, v169\n"
      "v_exp_f32 v170, v170\n"
      "v_exp_f32 v171, v171\n"
      "v_smfmac_f32_32x32x32_bf16 v[96:111], a[220:223], v[152:159], v15\n"
      "ds_read_b128 v[72:75], v19 offset:8192\n"
      "ds_read_b128 v[76:79], v19 offset:9216\n"
      "ds_read_b128 v[48:51], v19 offset:10240\n"
      "ds_read_b128 v[52:55], v19 offset:11264\n"
      "ds_read_b128 v[56:59], v19 offset:12288\n"
      "ds_read_b128 v[60:63], v19 offset:13312\n"
      "v_exp_f32 v172, v172\n"
      "v_exp_f32 v173, v173\n"
      "v_exp_f32 v174, v174\n"
      "v_exp_f32 v175, v175\n"
      "v_smfmac_f32_32x32x32_bf16 v[96:111], v[248:251], v[160:167], v15\n"
      "s_nop 0\n"
      "v_smfmac_f32_32x32x32_bf16 v[96:111], v[252:255], v[168:175], v15\n"
      "s_waitcnt lgkmcnt(0)\n"
      "v_pk_add_f32 v[176:177], v[176:177], v[64:65]\n"
      "v_pk_add_f32 v[178:179], v[178:179], v[66:67]\n"
      "v_pk_add_f32 v[180:181], v[180:181], v[68:69]\n"
      "v_pk_add_f32 v[182:183], v[182:183], v[70:71]\n"
      "v_pk_add_f32 v[184:185], v[184:185], v[72:73]\n"
      "v_pk_add_f32 v[186:187], v[186:187], v[74:75]\n"
      "v_pk_add_f32 v[188:189], v[188:189], v[76:77]\n"
      "v_pk_add_f32 v[190:191], v[190:191], v[78:79]\n"
      "v_pk_add_f32 v[176:177], v[176:177], v[48:49]\n"
      "v_pk_add_f32 v[178:179], v[178:179], v[50:51]\n"
      "v_pk_add_f32 v[180:181], v[180:181], v[52:53]\n"
      "v_pk_add_f32 v[182:183], v[182:183], v[54:55]\n"
      "v_pk_add_f32 v[184:185], v[184:185], v[56:57]\n"
      "v_pk_add_f32 v[186:187], v[186:187], v[58:59]\n"
      "v_pk_add_f32 v[188:189], v[188:189], v[60:61]\n"
      "v_pk_add_f32 v[190:191], v[190:191], v[62:63]\n"
      "v_pk_add_f32 v[108:109], v[108:109], v[188:189]\n"
      "v_cmp_nlt_f32 vcc, v108, v222\n"
      "v_pk_add_f32 v[110:111], v[110:111], v[190:191]\n"
      "v_rcp_f32 v29, v108\n"
      "v_pk_add_f32 v[96:97], v[96:97], v[176:177]\n"
      "v_cndmask_b32 v29, v29, -1, vcc\n"
      "v_pk_add_f32 v[98:99], v[98:99], v[178:179]\n"
      "v_mov_b32 v28, v29\n"
      "v_pk_add_f32 v[100:101], v[100:101], v[180:181]\n"
      "v_pk_add_f32 v[102:103], v[102:103], v[182:183]\n"
      "v_permlane32_swap_b32 v29, v28\n"
      "v_pk_add_f32 v[104:105], v[104:105], v[184:185]\n"
      "v_pk_add_f32 v[106:107], v[106:107], v[186:187]\n"
      "v_mul_f32 v29, v28, v23\n"
      "v_pk_mul_f32 v[112:113], v[96:97], v[28:29] op_sel_hi:[1,0]\n"
      "v_pk_mul_f32 v[114:115], v[98:99], v[28:29] op_sel_hi:[1,0]\n"
      "v_pk_mul_f32 v[116:117], v[100:101], v[28:29] op_sel_hi:[1,0]\n"
      "v_pk_mul_f32 v[118:119], v[102:103], v[28:29] op_sel_hi:[1,0]\n"
      "v_cvt_pk_bf16_f32 v128, v112, v113\n"
      "v_cvt_pk_bf16_f32 v129, v114, v115\n"
      "v_cvt_pk_bf16_f32 v130, v116, v117\n"
      "v_cvt_pk_bf16_f32 v131, v118, v119\n"
      "buffer_store_dwordx4 v[128:131], v0, s[4:7], s45 offen sc1\n"
      "v_pk_mul_f32 v[120:121], v[104:105], v[28:29] op_sel_hi:[1,0]\n"
      "v_pk_mul_f32 v[122:123], v[106:107], v[28:29] op_sel_hi:[1,0]\n"
      "v_pk_mul_f32 v[124:125], v[108:109], v[28:29] op_sel:[0,1] op_sel_hi:[1,1]\n"
      "v_pk_mul_f32 v[126:127], v[110:111], v[28:29] op_sel:[0,1] op_sel_hi:[1,1]\n"
      "v_cvt_pk_bf16_f32 v132, v120, v121\n"
      "v_cvt_pk_bf16_f32 v133, v122, v123\n"
      "v_cvt_pk_bf16_f32 v134, v124, v125\n"
      "v_cvt_pk_bf16_f32 v135, v126, v127\n"
      "buffer_store_dwordx4 v[132:135], v0, s[4:7], s45 offen offset:1024 sc1\n"
      "v_mov_b32 v140, v28\n"
      "; EARLY flag: every wave has issued its stores; wave 0 pushes the tag into the 16 consumers' lines (consumers validate what they read)\n"
      "s_add_u32 s34, s16, 6\n"
      "v_mov_b32 v28, s34\n"
      "v_mov_b32 v29, s17\n"
      "s_cmp_lg_u32 s14, 0\n"
      "s_barrier\n"
      "s_cbranch_scc1 L_sig_40\n"
      "buffer_atomic_swap_x2 v[28:29], v6, s[4:7], s80 offen\n"
      "L_sig_40:\n"
      "; was the sampled reference safe? (row sum below 1e30, every accumulator finite)\n"
      "v_pk_mul_f32 v[136:137], v[96:97], 0 op_sel_hi:[1,0]\n"
      "v_pk_fma_f32 v[136:137], v[98:99], 0, v[136:137] op_sel_hi:[1,0,1]\n"
      "v_pk_fma_f32 v[136:137], v[100:101], 0, v[136:137] op_sel_hi:[1,0,1]\n"
      "v_pk_fma_f32 v[136:137], v[102:103], 0, v[136:137] op_sel_hi:[1,0,1]\n"
      "v_pk_fma_f32 v[136:137], v[104:105], 0, v[136:137] op_sel_hi:[1,0,1]\n"
      "v_pk_fma_f32 v[136:137], v[106:107], 0, v[136:137] op_sel_hi:[1,0,1]\n"
      "v_pk_fma_f32 v[136:137], v[108:109], 0, v[136:137] op_sel_hi:[1,0,1]\n"
      "v_pk_fma_f32 v[136:137], v[110:111], 0, v[136:137] op_sel_hi:[1,0,1]\n"
      "v_add_f32 v136, v136, v137\n"
      "v_add_f32 v136, v136, v140\n"
      "v_mov_b32 v138, 159744\n"
      "v_mov_b32 v139, 1\n"
      "v_cmp_nlt_f32 vcc, 0xda24260, v136\n"
      "s_and_saveexec_b64 s[40:41], vcc\n"
      "ds_write_b32 v138, v139\n"
      "s_mov_b64 exec, -1\n"
      "s_waitcnt lgkmcnt(0)\n"
      "s_barrier\n"
      "ds_read_b32 v139, v138\n"
      "s_waitcnt lgkmcnt(0)\n"
      "v_readfirstlane_b32 s34, v139\n"
      "s_andn2_b32 s34, s34, s59\n"
      "s_cbranch_scc1 L_FB\n"
      "s_add_u32 s52, s44, 0\n"
      "s_add_u32 s53, s44, 4096\n"
      "s_add_u32 s54, s44, 8192\n"
      "s_add_u32 s55, s44, 12288\n"
      "s_add_u32 s44, s44, 2097152\n"
      "s_add_u32 s45, s45, 2097152\n"
      "v_add_u32 v223, s85, v0\n"
      "ds_write_b128 v223, a[112:115] offset:0\n"
      "ds_write_b128 v223, a[116:119] offset:1024\n"
      "ds_write_b128 v223, a[120:123] offset:2048\n"
      "ds_write_b128 v223, a[124:127] offset:3072\n"
      "ds_write_b128 v223, a[128:131] offset:4096\n"
      "s_cmp_lg_u32 s14, 3\n"
      "s_cbranch_scc1 L_ep_41\n"
      "s_mov_b64 exec, 0xffff\n"
      "v_mov_b64 v[28:29], 0\n"
      "buffer_atomic_or_x2 v[28:29], v7, s[4:7], s82 offen sc0\n"
      "s_mov_b64 exec, -1\n"
      "L_ep_41:\n"
      "ds_write_b128 v223, a[132:135] offset:5120\n"
      "ds_write_b128 v223, a[136:139] offset:6144\n"
      "ds_write_b128 v223, a[140:143] offset:7168\n"
      "ds_write_b128 v223, a[144:147] offset:8192\n"
      "ds_write_b128 v223, a[148:151] offset:9216\n"
      "ds_write_b128 v223, a[152:155] offset:10240\n"
      "ds_write_b128 v223, a[156:159] offset:11264\n"
      "ds_write_b128 v223, a[160:163] offset:12288\n"
      "ds_write_b128 v223, a[164:167] offset:13312\n"
      "v_and_b32 v240, 0x1f0, v0\n"
      "v_mul_u32_u24 v240, 56, v240\n"
      "v_lshrrev_b32 v241, 9, v0\n"
      "v_lshl_add_u32 v240, v241, 4, v240\n"
      "v_and_b32 v241, 0x80000000, v3\n"
      "v_or_b32 v240, v240, v241\n"
      "s_mov_b32 s8, s30\n"
      "s_and_b32 s9, s31, 0xffff\n"
      "s_mov_b32 s10, 1835008\n"
      "s_mov_b32 s1, 0\n"
      "v_mov_b32 v222, 0x207\n"
      "; hand-off: wave 3 polls its flags, barrier\n"
      "s_add_u32 s34, s16, 6\n"
      "s_cmp_lg_u32 s14, 3\n"
      "s_cbranch_scc1 L_pd_43\n"
      "s_mov_b32 s51, 0x40000\n"
      "s_mov_b64 exec, 0xffff\n"
      "s_branch L_pc_44\n"
      "L_poll_42:\n"
      "s_mov_b64 exec, 0xffff\n"
      "v_mov_b64 v[28:29], 0\n"
      "buffer_atomic_or_x2 v[28:29], v7, s[4:7], s82 offen sc0\n"
      "s_sub_u32 s51, s51, 1\n"
      "s_cbranch_scc1 L_pd_43\n"
      "L_pc_44:\n"
      "s_waitcnt vmcnt(0)\n"
      "v_subrev_u32 v28, s34, v28\n"
      "v_cmp_eq_u32 s[40:41], s17, v29\n"
      "v_cmp_gt_u32 vcc, 64, v28\n"
      "s_and_b64 vcc, vcc, s[40:41]\n"
      "s_cmp_eq_u64 vcc, exec\n"
      "s_cbranch_scc0 L_poll_42\n"
      "L_pd_43:\n"
      "s_mov_b64 exec, -1\n"
      "s_waitcnt vmcnt(0) lgkmcnt(0)\n"
      "s_barrier\n"
      "L_OB_RE:\n"
      "buffer_load_dwordx4 v[48:51], v3, s[4:7], s52 offen sc1\n"
      "buffer_load_dwordx4 v[52:55], v3, s[4:7], s52 offen offset:1024 sc1\n"
      "ds_read_b64_tr_b16 a[176:177], v5 offset:0\n"
      "ds_read_b64_tr_b16 a[178:179], v5 offset:1024\n"
      "ds_read_b64_tr_b16 a[180:181], v5 offset:2048\n"
      "ds_read_b64_tr_b16 a[182:183], v5 offset:3072\n"
      "ds_read_b64_tr_b16 a[184:185], v5 offset:3584\n"
      "ds_read_b64_tr_b16 a[186:187], v5 offset:4608\n"
      "buffer_load_dwordx4 v[56:59], v3, s[4:7], s52 offen offset:2048 sc1\n"
      "buffer_load_dwordx4 v[60:63], v3, s[4:7], s52 offen offset:3072 sc1\n"
      "ds_read_b64_tr_b16 a[188:189], v5 offset:5632\n"
      "ds_read_b64_tr_b16 a[190:191], v5 offset:6656\n"
      "ds_read_b64_tr_b16 a[192:193], v5 offset:7168\n"
      "ds_read_b64_tr_b16 a[194:195], v5 offset:8192\n"
      "ds_read_b64_tr_b16 a[196:197], v4 offset:0\n"
      "ds_read_b64_tr_b16 a[198:199], v4 offset:1024\n"
      "ds_read_b64_tr_b16 a[200:201], v5 offset:9216\n"
      "ds_read_b64_tr_b16 a[202:203], v5 offset:10240\n"
      "buffer_load_dwordx4 v[64:67], v3, s[4:7], s53 offen sc1\n"
      "buffer_load_dwordx4 v[68:71], v3, s[4:7], s53 offen offset:1024 sc1\n"
      "ds_read_b64_tr_b16 a[204:205], v4 offset:2048\n"
      "ds_read_b64_tr_b16 a[206:207], v4 offset:3072\n"
      "ds_read_b64_tr_b16 a[208:209], v5 offset:10752\n"
      "ds_read_b64_tr_b16 a[210:211], v5 offset:11776\n"
      "ds_read_b64_tr_b16 a[212:213], v4 offset:3584\n"
      "ds_read_b64_tr_b16 a[214:215], v4 offset:4608\n"
      "buffer_load_dwordx4 v[72:75], v3, s[4:7], s53 offen offset:2048 sc1\n"
      "buffer_load_dwordx4 v[76:79], v3, s[4:7], s53 offen offset:3072 sc1\n"
      "ds_read_b64_tr_b16 a[216:217], v5 offset:12800\n"
      "ds_read_b64_tr_b16 a[218:219], v5 offset:13824\n"
      "ds_read_b64_tr_b16 a[220:221], v4 offset:5632\n"
      "ds_read_b64_tr_b16 a[222:223], v4 offset:6656\n"
      "ds_read_b64_tr_b16 a[224:225], v5 offset:14336\n"
      "ds_read_b64_tr_b16 a[226:227], v5 offset:15360\n"
      "ds_read_b64_tr_b16 a[228:229], v4 offset:7168\n"
      "ds_read_b64_tr_b16 a[230:231], v4 offset:8192\n"
      "buffer_load_dwordx4 v[80:83], v3, s[4:7], s54 offen sc1\n"
      "buffer_load_dwordx4 v[84:87], v3, s[4:7], s54 offen offset:1024 sc1\n"
      "ds_read_b64_tr_b16 a[232:233], v5 offset:16384\n"
      "ds_read_b64_tr_b16 a[234:235], v5 offset:17408\n"
      "ds_read_b64_tr_b16 a[236:237], v4 offset:9216\n"
      "ds_read_b64_tr_b16 a[238:239], v4 offset:10240\n"
      "ds_read_b64_tr_b16 a[240:241], v5 offset:17920\n"
      "ds_read_b64_tr_b16 a[242:243], v5 offset:18944\n"
      "ds_read_b64_tr_b16 a[244:245], v4 offset:10752\n"
      "ds_read_b64_tr_b16 a[246:247], v4 offset:11776\n"
      "v_mov_b64 v[192:193], v[32:33]\n"
      "v_mov_b64 v[194:195], v[34:35]\n"
      "v_mov_b64 v[196:197], v[36:37]\n"
      "v_mov_b64 v[198:199], v[38:39]\n"
      "v_mov_b64 v[200:201], v[40:41]\n"
      "v_mov_b64 v[202:203], v[42:43]\n"
      "v_mov_b64 v[204:205], v[44:45]\n"
      "v_mov_b64 v[206:207], v[46:47]\n"
      "s_waitcnt vmcnt(9) lgkmcnt(15)\n"
      "v_mfma_f32_32x32x16_bf16 v[160:175], a[176:179], v[48:51], 0\n"
      "ds_read_b64_tr_b16 a[248:249], v5 offset:19968\n"
      "ds_read_b64_tr_b16 a[250:251], v5 offset:20992\n"
      "buffer_load_dwordx4 v[88:91], v3, s[4:7], s54 offen offset:2048 sc1\n"
      "s_waitcnt vmcnt(9)\n"
      "v_mfma_f32_32x32x16_bf16 v[160:175], a[180:183], v[52:55], v[160:175]\n"
      "ds_read_b64_tr_b16 a[252:253], v4 offset:12800\n"
      "ds_read_b64_tr_b16 a[254:255], v4 offset:13824\n"
      "buffer_load_dwordx4 v[92:95], v3, s[4:7], s54 offen offset:3072 sc1\n"
      "s_waitcnt vmcnt(9)\n"
      "v_mfma_f32_32x32x16_bf16 v[160:175], a[184:187], v[56:59], v[160:175]\n"
      "ds_read_b64_tr_b16 a[176:177], v5 offset:21504\n"
      "ds_read_b64_tr_b16 a[178:179], v5 offset:22528\n"
      "buffer_load_dwordx4 v[96:99], v3, s[4:7], s55 offen sc1\n"
      "s_waitcnt vmcnt(9)\n"
      "v_mfma_f32_32x32x16_bf16 v[160:175], a[188:191], v[60:63], v[160:175]\n"
      "ds_read_b64_tr_b16 a[180:181], v4 offset:14336\n"
      "ds_read_b64_tr_b16 a[182:183], v4 offset:15360\n"
      "buffer_load_dwordx4 v[100:103], v3, s[4:7], s55 offen offset:1024 sc1\n"
      "s_waitcnt vmcnt(9)\n"
      "v_mfma_f32_32x32x16_bf16 v[160:175], a[192:195], v[64:67], v[160:175]\n"
      "ds_read_b64_tr_b16 a[184:185], v5 offset:23552\n"
      "ds_read_b64_tr_b16 a[186:187], v5 offset:24576\n"
      "buffer_load_dwordx4 v[104:107], v3, s[4:7], s55 offen offset:2048 sc1\n"
      "v_mfma_f32_32x32x16_bf16 v[32:47], a[196:199], v[48:51], v[32:47]\n"
      "ds_read_b64_tr_b16 a[188:189], v4 offset:16384\n"
      "ds_read_b64_tr_b16 a[190:191], v4 offset:17408\n"
      "s_waitcnt vmcnt(9)\n"
      "v_mfma_f32_32x32x16_bf16 v[160:175], a[200:203], v[68:71], v[160:175]\n"
      "ds_read_b64_tr_b16 a[192:193], v5 offset:25088\n"
      "ds_read_b64_tr_b16 a[194:195], v5 offset:26112\n"
      "buffer_load_dwordx4 v[108:111], v3, s[4:7], s55 offen offset:3072 sc1\n"
      "v_mfma_f32_32x32x16_bf16 v[32:47], a[204:207], v[52:55], v[32:47]\n"
      "ds_read_b64_tr_b16 a[196:197], v4 offset:17920\n"
      "ds_read_b64_tr_b16 a[198:199], v4 offset:18944\n"
      "s_waitcnt vmcnt(9)\n"
      "v_mfma_f32_32x32x16_bf16 v[160:175], a[208:211], v[72:75], v[160:175]\n"
      "ds_read_b64_tr_b16 a[200:201], v5 offset:27136\n"
      "ds_read_b64_tr_b16 a[202:203], v5 offset:28160\n"
      "v_mfma_f32_32x32x16_bf16 v[32:47], a[212:215], v[56:59], v[32:47]\n"
      "ds_read_b64_tr_b16 a[204:205], v4 offset:19968\n"
      "ds_read_b64_tr_b16 a[206:207], v4 offset:20992\n"
      "s_waitcnt vmcnt(8) lgkmcnt(15)\n"
      "v_mfma_f32_32x32x16_bf16 v[160:175], a[216:219], v[76:79], v[160:175]\n"
      "ds_read_b64_tr_b16 a[208:209], v4 offset:21504\n"
      "ds_read_b64_tr_b16 a[210:211], v4 offset:22528\n"
      "v_mfma_f32_32x32x16_bf16 v[32:47], a[220:223], v[60:63], v[32:47]\n"
      "ds_read_b64_tr_b16 a[212:213], v4 offset:23552\n"
      "ds_read_b64_tr_b16 a[214:215], v4 offset:24576\n"
      "s_waitcnt vmcnt(7)\n"
      "v_mfma_f32_32x32x16_bf16 v[160:175], a[224:227], v[80:83], v[160:175]\n"
      "ds_read_b64_tr_b16 a[216:217], v4 offset:25088\n"
      "ds_read_b64_tr_b16 a[218:219], v4 offset:26112\n"
      "v_mfma_f32_32x32x16_bf16 v[32:47], a[228:231], v[64:67], v[32:47]\n"
      "ds_read_b64_tr_b16 a[220:221], v4 offset:27136\n"
      "ds_read_b64_tr_b16 a[222:223], v4 offset:28160\n"
      "s_waitcnt vmcnt(6)\n"
      "v_mfma_f32_32x32x16_bf16 v[160:175], a[232:235], v[84:87], v[160:175]\n"
      "v_mfma_f32_32x32x16_bf16 v[32:47], a[236:239], v[68:71], v[32:47]\n"
      "s_waitcnt vmcnt(5)\n"
      "v_mfma_f32_32x32x16_bf16 v[160:175], a[240:243], v[88:91], v[160:175]\n"
      "v_mfma_f32_32x32x16_bf16 v[32:47], a[244:247], v[72:75], v[32:47]\n"
      "s_waitcnt vmcnt(4)\n"
      "v_mfma_f32_32x32x16_bf16 v[160:175], a[248:251], v[92:95], v[160:175]\n"
      "v_mfma_f32_32x32x16_bf16 v[32:47], a[252:255], v[76:79], v[32:47]\n"
      "s_waitcnt vmcnt(3) lgkmcnt(15)\n"
      "v_mfma_f32_32x32x16_bf16 v[160:175], a[176:179], v[96:99], v[160:175]\n"
      "v_mfma_f32_32x32x16_bf16 v[32:47], a[180:183], v[80:83], v[32:47]\n"
      "s_waitcnt vmcnt(2)\n"
      "v_mfma_f32_32x32x16_bf16 v[160:175], a[184:187], v[100:103], v[160:175]\n"
      "v_mfma_f32_32x32x16_bf16 v[32:47], a[188:191], v[84:87], v[32:47]\n"
      "s_waitcnt vmcnt(1) lgkmcnt(12)\n"
      "v_mfma_f32_32x32x16_bf16 v[160:175], a[192:195], v[104:107], v[160:175]\n"
      "v_mfma_f32_32x32x16_bf16 v[32:47], a[196:199], v[88:91], v[32:47]\n"
      "s_waitcnt vmcnt(0) lgkmcnt(8)\n"
      "v_mfma_f32_32x32x16_bf16 v[160:175], a[200:203], v[108:111], v[160:175]\n"
      "v_mfma_f32_32x32x16_bf16 v[32:47], a[204:207], v[92:95], v[32:47]\n"
      "; LATE flag: every wave drained its stores in front of the barrier just passed\n"
      "s_cmp_lg_u32 s14, 1\n"
      "s_cbranch_scc1 L_late_45\n"
      "s_add_u32 s34, s16, 6\n"
      "v_mov_b32 v28, s34\n"
      "v_mov_b32 v29, s17\n"
      "buffer_atomic_swap_x2 v[28:29], v6, s[4:7], s80 offen offset:128\n"
      "L_late_45:\n"
      "s_waitcnt lgkmcnt(4)\n"
      "v_mfma_f32_32x32x16_bf16 v[32:47], a[208:211], v[96:99], v[32:47]\n"
      "v_cmp_class_f32 vcc, v160, v222\n"
      "s_cbranch_vccnz L_OB_BAD\n"
      "L_OB_OK:\n"
      "s_waitcnt lgkmcnt(0)\n"
      "s_barrier\n"
      "v_mfma_f32_32x32x16_bf16 v[32:47], a[212:215], v[100:103], v[32:47]\n"
      "ds_write_b128 v217, v[160:163] offset:0\n"
      "ds_write_b128 v217, v[164:167] offset:1024\n"
      "ds_write_b128 v217, v[168:171] offset:2048\n"
      "ds_write_b128 v217, v[172:175] offset:3072\n"
      "v_mfma_f32_32x32x16_bf16 v[32:47], a[216:219], v[104:107], v[32:47]\n"
      "v_mfma_f32_32x32x16_bf16 v[32:47], a[220:223], v[108:111], v[32:47]\n"
      "s_waitcnt lgkmcnt(0)\n"
      "s_barrier\n"
      "ds_read_b128 v[176:179], v27 offset:0\n"
      "ds_read_b128 v[180:183], v27 offset:1024\n"
      "ds_read_b128 v[184:187], v27 offset:2048\n"
      "ds_read_b128 v[188:191], v27 offset:3072\n"
      "s_cmp_lg_u32 s15, 0\n"
      "s_nop 4\n"
      "s_waitcnt lgkmcnt(2)\n"
      "v_pk_add_f32 v[32:33], v[32:33], v[176:177]\n"
      "v_pk_add_f32 v[34:35], v[34:35], v[178:179]\n"
      "v_pk_add_f32 v[36:37], v[36:37], v[180:181]\n"
      "v_pk_add_f32 v[38:39], v[38:39], v[182:183]\n"
      "v_cvt_pk_bf16_f32 v248, v32, v33\n"
      "v_cvt_pk_bf16_f32 v249, v34, v35\n"
      "v_cvt_pk_bf16_f32 v250, v36, v37\n"
      "v_cvt_pk_bf16_f32 v251, v38, v39\n"
      "s_waitcnt lgkmcnt(0)\n"
      "v_pk_add_f32 v[40:41], v[40:41], v[184:185]\n"
      "v_pk_add_f32 v[42:43], v[42:43], v[186:187]\n"
      "v_pk_add_f32 v[44:45], v[44:45], v[188:189]\n"
      "v_pk_add_f32 v[46:47], v[46:47], v[190:191]\n"
      "v_cvt_pk_bf16_f32 v252, v40, v41\n"
      "v_cvt_pk_bf16_f32 v253, v42, v43\n"
      "v_cvt_pk_bf16_f32 v254, v44, v45\n"
      "v_cvt_pk_bf16_f32 v255, v46, v47\n"
      "s_cbranch_scc1 L_FINAL\n"
      "buffer_store_dwordx4 v[248:251], v3, s[4:7], s43 offen sc1\n"
      "buffer_store_dwordx4 v[252:255], v3, s[4:7], s43 offen offset:1024 sc1\n"
      "; EARLY flag: every wave has issued its stores; wave 0 pushes the tag into the 16 consumers' lines (consumers validate what they read)\n"
      "s_add_u32 s34, s16, 7\n"
      "v_mov_b32 v28, s34\n"
      "v_mov_b32 v29, s17\n"
      "s_cmp_lg_u32 s14, 0\n"
      "s_barrier\n"
      "s_cbranch_scc1 L_sig_46\n"
      "buffer_atomic_swap_x2 v[28:29], v6, s[4:7], s80 offen\n"
      "L_sig_46:\n"
      "; partial row sums of the new x slice\n"
      "v_pk_add_f32 v[160:161], v[32:33], v[40:41]\n"
      "v_pk_add_f32 v[162:163], v[34:35], v[42:43]\n"
      "v_pk_add_f32 v[164:165], v[36:37], v[44:45]\n"
      "v_pk_add_f32 v[166:167], v[38:39], v[46:47]\n"
      "v_pk_mul_f32 v[168:169], v[32:33], v[32:33]\n"
      "v_pk_mul_f32 v[170:171], v[34:35], v[34:35]\n"
      "v_pk_add_f32 v[160:161], v[160:161], v[164:165]\n"
      "v_pk_add_f32 v[162:163], v[162:163], v[166:167]\n"
      "v_pk_fma_f32 v[168:169], v[36:37], v[36:37], v[168:169]\n"
      "v_pk_fma_f32 v[170:171], v[38:39], v[38:39], v[170:171]\n"
      "v_pk_fma_f32 v[168:169], v[40:41], v[40:41], v[168:169]\n"
      "v_pk_fma_f32 v[170:171], v[42:43], v[42:43], v[170:171]\n"
      "v_pk_fma_f32 v[168:169], v[44:45], v[44:45], v[168:169]\n"
      "v_pk_fma_f32 v[170:171], v[46:47], v[46:47], v[170:171]\n"
      "v_pk_add_f32 v[160:161], v[160:161], v[162:163]\n"
      "v_pk_add_f32 v[168:169], v[168:169], v[170:171]\n"
      "v_add_f32 v160, v160, v161\n"
      "v_add_f32 v168, v168, v169\n"
      "s_add_u32 s43, s43, 1835008\n"
      "s_nop 0\n"
      "v_permlane32_swap_b32 v160, v168\n"
      "v_add_f32 v160, v160, v168\n"
      "buffer_store_dword v160, v9, s[4:7], s47 offen sc1\n"
      "s_add_u32 s47, s47, 262144\n"
      "s_mov_b32 s15, 1\n"
      "s_add_u32 s16, s16, 6\n"
      "s_branch L_LOOP\n"
      "L_FINAL:\n"
      "buffer_store_dwordx4 v[248:251], v240, s[8:11], s3 offen\n"
      "buffer_store_dwordx4 v[252:255], v240, s[8:11], s3 offen offset:32\n"
      "s_or_b32 s34, s2, s14\n"
      "s_cmp_lg_u32 s34, 0\n"
      "s_cbranch_scc1 L_end_47\n"
      "s_add_u32 s18, s18, 1\n"
      "s_addc_u32 s19, s19, 0\n"
      "v_mov_b32 v28, s18\n"
      "v_mov_b32 v29, s19\n"
      "v_mov_b32 v30, 131072\n"
      "s_mov_b64 exec, 1\n"
      "buffer_store_dwordx2 v[28:29], v30, s[4:7], 0 offen sc1\n"
      "L_end_47:\n"
      "s_endpgm\n"
      "; ---- rare path: take the true row maximum and run the attention again ----\n"
      "L_FB:\n"
      "s_waitcnt vmcnt(0) lgkmcnt(0)\n"
      "s_add_u32 s34, s16, 5\n"
      "s_mov_b32 s51, 0x40000\n"
      "s_mov_b64 exec, 0xffff\n"
      "L_wl_48:\n"
      "v_mov_b64 v[28:29], 0\n"
      "buffer_atomic_or_x2 v[28:29], v7, s[4:7], s83 offen offset:128 sc0\n"
      "s_waitcnt vmcnt(0)\n"
      "s_sub_u32 s51, s51, 1\n"
      "s_cbranch_scc1 L_wd_49\n"
      "v_subrev_u32 v28, s34, v28\n"
      "v_cmp_eq_u32 s[40:41], s17, v29\n"
      "v_cmp_gt_u32 vcc, 64, v28\n"
      "s_and_b64 vcc, vcc, s[40:41]\n"
      "s_cmp_eq_u64 vcc, exec\n"
      "s_cbranch_scc0 L_wl_48\n"
      "L_wd_49:\n"
      "s_mov_b64 exec, -1\n"
      "s_mov_b32 exec_lo, 0\n"
      "v_accvgpr_write_b32 a230, 0\n"
      "v_accvgpr_write_b32 a231, 0\n"
      "v_accvgpr_write_b32 a238, 0\n"
      "v_accvgpr_write_b32 a239, 0\n"
      "v_accvgpr_write_b32 a246, 0\n"
      "v_accvgpr_write_b32 a247, 0\n"
      "v_accvgpr_write_b32 a254, 0\n"
      "v_accvgpr_write_b32 a255, 0\n"
      "s_mov_b64 exec, -1\n"
      "v_mov_b32 v144, 0xff800000\n"
      "v_mov_b32 v145, 0xff800000\n"
      "v_mov_b32 v146, 0xff800000\n"
      "v_mov_b32 v147, 0xff800000\n"
      "s_mov_b32 s36, 0\n"
      "L_FB1:\n"
      "buffer_load_dwordx4 v[152:155], v30, s[4:7], s36 offen sc1\n"
      "buffer_load_dwordx4 v[156:159], v30, s[4:7], s36 offen offset:1024 sc1\n"
      "s_waitcnt vmcnt(0)\n"
      "v_mfma_f32_32x32x16_bf16 v[112:127], v[152:155], a[224:227], 0\n"
      "v_mfma_f32_32x32x16_bf16 v[112:127], v[156:159], a[228:231], v[112:127]\n"
      "s_nop 11\n"
      "v_max3_f32 v144, v144, v112, v113\n"
      "v_max3_f32 v144, v144, v114, v115\n"
      "v_max3_f32 v144, v144, v116, v117\n"
      "v_max3_f32 v144, v144, v118, v119\n"
      "v_max3_f32 v144, v144, v120, v121\n"
      "v_max3_f32 v144, v144, v122, v123\n"
      "v_max3_f32 v144, v144, v124, v125\n"
      "v_max3_f32 v144, v144, v126, v127\n"
      "v_mfma_f32_32x32x16_bf16 v[112:127], v[152:155], a[232:235], 0\n"
      "v_mfma_f32_32x32x16_bf16 v[112:127], v[156:159], a[236:239], v[112:127]\n"
      "s_nop 11\n"
      "v_max3_f32 v145, v145, v112, v113\n"
      "v_max3_f32 v145, v145, v114, v115\n"
      "v_max3_f32 v145, v145, v116, v117\n"
      "v_max3_f32 v145, v145, v118, v119\n"
      "v_max3_f32 v145, v145, v120, v121\n"
      "v_max3_f32 v145, v145, v122, v123\n"
      "v_max3_f32 v145, v145, v124, v125\n"
      "v_max3_f32 v145, v145, v126, v127\n"
      "v_mfma_f32_32x32x16_bf16 v[112:127], v[152:155], a[240:243], 0\n"
      "v_mfma_f32_32x32x16_bf16 v[112:127], v[156:159], a[244:247], v[112:127]\n"
      "s_nop 11\n"
      "v_max3_f32 v146, v146, v112, v113\n"
      "v_max3_f32 v146, v146, v114, v115\n"
      "v_max3_f32 v146, v146, v116, v117\n"
      "v_max3_f32 v146, v146, v118, v119\n"
      "v_max3_f32 v146, v146, v120, v121\n"
      "v_max3_f32 v146, v146, v122, v123\n"
      "v_max3_f32 v146, v146, v124, v125\n"
      "v_max3_f32 v146, v146, v126, v127\n"
      "v_mfma_f32_32x32x16_bf16 v[112:127], v[152:155], a[248:251], 0\n"
      "v_mfma_f32_32x32x16_bf16 v[112:127], v[156:159], a[252:255], v[112:127]\n"
      "s_nop 11\n"
      "v_max3_f32 v147, v147, v112, v113\n"
      "v_max3_f32 v147, v147, v114, v115\n"
      "v_max3_f32 v147, v147, v116, v117\n"
      "v_max3_f32 v147, v147, v118, v119\n"
      "v_max3_f32 v147, v147, v120, v121\n"
      "v_max3_f32 v147, v147, v122, v123\n"
      "v_max3_f32 v147, v147, v124, v125\n"
      "v_max3_f32 v147, v147, v126, v127\n"
      "s_add_u32 s36, s36, 16384\n"
      "s_cmp_lt_u32 s36, 262144\n"
      "s_cbranch_scc1 L_FB1\n"
      "ds_write_b32 v16, v144 offset:2048\n"
      "ds_write_b32 v17, v145 offset:6144\n"
      "ds_write_b32 v18, v146 offset:10240\n"
      "buffer_load_dwordx4 v[240:243], v30, s[4:7], s79 offen offset:0 sc1\n"
      "buffer_load_dwordx4 v[244:247], v30, s[4:7], s79 offen offset:1024 sc1\n"
      "buffer_load_dwordx4 v[248:251], v30, s[4:7], s79 offen offset:2048 sc1\n"
      "buffer_load_dwordx4 v[252:255], v30, s[4:7], s79 offen offset:3072 sc1\n"
      "buffer_load_dwordx4 v[224:227], v30, s[4:7], s64 offen sc1\n"
      "buffer_load_dwordx4 v[228:231], v30, s[4:7], s64 offen offset:1024 sc1\n"
      "buffer_load_dwordx4 v[232:235], v30, s[4:7], s64 offen offset:2048 sc1\n"
      "buffer_load_dwordx4 v[236:239], v30, s[4:7], s64 offen offset:3072 sc1\n"
      "v_mov_b64 v[48:49], 0\n"
      "v_mov_b64 v[50:51], 0\n"
      "v_mov_b64 v[52:53], 0\n"
      "v_mov_b64 v[54:55], 0\n"
      "v_mov_b64 v[56:57], 0\n"
      "v_mov_b64 v[58:59], 0\n"
      "v_mov_b64 v[60:61], 0\n"
      "v_mov_b64 v[62:63], 0\n"
      "v_mov_b64 v[64:65], 0\n"
      "v_mov_b64 v[66:67], 0\n"
      "v_mov_b64 v[68:69], 0\n"
      "v_mov_b64 v[70:71], 0\n"
      "v_mov_b64 v[72:73], 0\n"
      "v_mov_b64 v[74:75], 0\n"
      "v_mov_b64 v[76:77], 0\n"
      "v_mov_b64 v[78:79], 0\n"
      "v_mov_b64 v[80:81], 0\n"
      "v_mov_b64 v[82:83], 0\n"
      "v_mov_b64 v[84:85], 0\n"
      "v_mov_b64 v[86:87], 0\n"
      "v_mov_b64 v[88:89], 0\n"
      "v_mov_b64 v[90:91], 0\n"
      "v_mov_b64 v[92:93], 0\n"
      "v_mov_b64 v[94:95], 0\n"
      "v_mov_b64 v[96:97], 0\n"
      "v_mov_b64 v[98:99], 0\n"
      "v_mov_b64 v[100:101], 0\n"
      "v_mov_b64 v[102:103], 0\n"
      "v_mov_b64 v[104:105], 0\n"
      "v_mov_b64 v[106:107], 0\n"
      "v_mov_b64 v[108:109], 0\n"
      "v_mov_b64 v[110:111], 0\n"
      "s_waitcnt lgkmcnt(0)\n"
      "s_barrier\n"
      "ds_read_b32 v148, v19 offset:2048\n"
      "ds_read_b32 v149, v19 offset:6144\n"
      "ds_read_b32 v150, v19 offset:10240\n"
      "s_waitcnt vmcnt(0) lgkmcnt(0)\n"
      "s_barrier\n"
      "v_max3_f32 v128, v147, v148, v149\n"
      "v_max_f32 v128, v128, v150\n"
      "s_mov_b32 s59, -1\n"
      "s_branch L_PATCH\n"
      "; ---- rare paths: a consumer met poison ----\n"
      "L_XA_BAD:\n"
      "s_cmp_lg_u32 s1, 0\n"
      "s_cbranch_scc1 L_XA_OK\n"
      "s_mov_b32 s1, 1\n"
      "s_waitcnt vmcnt(0) lgkmcnt(0)\n"
      "s_add_u32 s34, s16, 1\n"
      "s_mov_b32 s51, 0x40000\n"
      "s_mov_b64 exec, 0xffff\n"
      "L_wl_50:\n"
      "v_mov_b64 v[28:29], 0\n"
      "buffer_atomic_or_x2 v[28:29], v7, s[4:7], s82 offen offset:128 sc0\n"
      "s_waitcnt vmcnt(0)\n"
      "s_sub_u32 s51, s51, 1\n"
      "s_cbranch_scc1 L_wd_51\n"
      "v_subrev_u32 v28, s34, v28\n"
      "v_cmp_eq_u32 s[40:41], s17, v29\n"
      "v_cmp_gt_u32 vcc, 64, v28\n"
      "s_and_b64 vcc, vcc, s[40:41]\n"
      "s_cmp_eq_u64 vcc, exec\n"
      "s_cbranch_scc0 L_wl_50\n"
      "L_wd_51:\n"
      "s_mov_b64 exec, -1\n"
      "s_sub_u32 s46, s46, 262144\n"
      "s_branch L_XA_RE\n"
      "L_P_BAD:\n"
      "s_cmp_lg_u32 s1, 0\n"
      "s_cbranch_scc1 L_P_OK\n"
      "s_mov_b32 s1, 1\n"
      "s_waitcnt vmcnt(0) lgkmcnt(0)\n"
      "s_add_u32 s34, s16, 2\n"
      "s_mov_b32 s51, 0x40000\n"
      "s_mov_b64 exec, 0xffff\n"
      "L_wl_52:\n"
      "v_mov_b64 v[28:29], 0\n"
      "buffer_atomic_or_x2 v[28:29], v7, s[4:7], s83 offen offset:128 sc0\n"
      "s_waitcnt vmcnt(0)\n"
      "s_sub_u32 s51, s51, 1\n"
      "s_cbranch_scc1 L_wd_53\n"
      "v_subrev_u32 v28, s34, v28\n"
      "v_cmp_eq_u32 s[40:41], s17, v29\n"
      "v_cmp_gt_u32 vcc, 64, v28\n"
      "s_and_b64 vcc, vcc, s[40:41]\n"
      "s_cmp_eq_u64 vcc, exec\n"
      "s_cbranch_scc0 L_wl_52\n"
      "L_wd_53:\n"
      "s_mov_b64 exec, -1\n"
      "v_mov_b64 v[192:193], 0\n"
      "v_mov_b64 v[194:195], 0\n"
      "v_mov_b64 v[196:197], 0\n"
      "v_mov_b64 v[198:199], 0\n"
      "buffer_load_dwordx4 v[48:51], v219, s[4:7], s52 offen sc1\n"
      "buffer_load_dwordx4 v[52:55], v30, s[4:7], s52 offen sc1\n"
      "buffer_load_dwordx4 v[56:59], v219, s[4:7], s53 offen sc1\n"
      "buffer_load_dwordx4 v[60:63], v30, s[4:7], s53 offen sc1\n"
      "buffer_load_dwordx4 v[64:67], v219, s[4:7], s54 offen sc1\n"
      "buffer_load_dwordx4 v[68:71], v30, s[4:7], s54 offen sc1\n"
      "buffer_load_dwordx4 v[72:75], v219, s[4:7], s55 offen sc1\n"
      "buffer_load_dwordx4 v[76:79], v30, s[4:7], s55 offen sc1\n"
      "s_waitcnt vmcnt(7)\n"
      "v_dot2c_f32_bf16 v192, v48, v200\n"
      "v_dot2c_f32_bf16 v193, v48, v201\n"
      "v_dot2c_f32_bf16 v194, v49, v200\n"
      "v_dot2c_f32_bf16 v195, v49, v201\n"
      "v_dot2c_f32_bf16 v196, v50, v200\n"
      "v_dot2c_f32_bf16 v197, v50, v201\n"
      "v_dot2c_f32_bf16 v198, v51, v200\n"
      "v_dot2c_f32_bf16 v199, v51, v201\n"
      "s_waitcnt vmcnt(6)\n"
      "v_dot2c_f32_bf16 v192, v52, v200\n"
      "v_dot2c_f32_bf16 v193, v52, v201\n"
      "v_dot2c_f32_bf16 v194, v53, v200\n"
      "v_dot2c_f32_bf16 v195, v53, v201\n"
      "v_dot2c_f32_bf16 v196, v54, v200\n"
      "v_dot2c_f32_bf16 v197, v54, v201\n"
      "v_dot2c_f32_bf16 v198, v55, v200\n"
      "v_dot2c_f32_bf16 v199, v55, v201\n"
      "s_waitcnt vmcnt(5)\n"
      "v_dot2c_f32_bf16 v192, v56, v200\n"
      "v_dot2c_f32_bf16 v193, v56, v201\n"
      "v_dot2c_f32_bf16 v194, v57, v200\n"
      "v_dot2c_f32_bf16 v195, v57, v201\n"
      "v_dot2c_f32_bf16 v196, v58, v200\n"
      "v_dot2c_f32_bf16 v197, v58, v201\n"
      "v_dot2c_f32_bf16 v198, v59, v200\n"
      "v_dot2c_f32_bf16 v199, v59, v201\n"
      "s_waitcnt vmcnt(4)\n"
      "v_dot2c_f32_bf16 v192, v60, v200\n"
      "v_dot2c_f32_bf16 v193, v60, v201\n"
      "v_dot2c_f32_bf16 v194, v61, v200\n"
      "v_dot2c_f32_bf16 v195, v61, v201\n"
      "v_dot2c_f32_bf16 v196, v62, v200\n"
      "v_dot2c_f32_bf16 v197, v62, v201\n"
      "v_dot2c_f32_bf16 v198, v63, v200\n"
      "v_dot2c_f32_bf16 v199, v63, v201\n"
      "s_waitcnt vmcnt(3)\n"
      "v_dot2c_f32_bf16 v192, v64, v200\n"
      "v_dot2c_f32_bf16 v193, v64, v201\n"
      "v_dot2c_f32_bf16 v194, v65, v200\n"
      "v_dot2c_f32_bf16 v195, v65, v201\n"
      "v_dot2c_f32_bf16 v196, v66, v200\n"
      "v_dot2c_f32_bf16 v197, v66, v201\n"
      "v_dot2c_f32_bf16 v198, v67, v200\n"
      "v_dot2c_f32_bf16 v199, v67, v201\n"
      "s_waitcnt vmcnt(2)\n"
      "v_dot2c_f32_bf16 v192, v68, v200\n"
      "v_dot2c_f32_bf16 v193, v68, v201\n"
      "v_dot2c_f32_bf16 v194, v69, v200\n"
      "v_dot2c_f32_bf16 v195, v69, v201\n"
      "v_dot2c_f32_bf16 v196, v70, v200\n"
      "v_dot2c_f32_bf16 v197, v70, v201\n"
      "v_dot2c_f32_bf16 v198, v71, v200\n"
      "v_dot2c_f32_bf16 v199, v71, v201\n"
      "s_waitcnt vmcnt(1)\n"
      "v_dot2c_f32_bf16 v192, v72, v200\n"
      "v_dot2c_f32_bf16 v193, v72, v201\n"
      "v_dot2c_f32_bf16 v194, v73, v200\n"
      "v_dot2c_f32_bf16 v195, v73, v201\n"
      "v_dot2c_f32_bf16 v196, v74, v200\n"
      "v_dot2c_f32_bf16 v197, v74, v201\n"
      "v_dot2c_f32_bf16 v198, v75, v200\n"
      "v_dot2c_f32_bf16 v199, v75, v201\n"
      "s_waitcnt vmcnt(0)\n"
      "v_dot2c_f32_bf16 v192, v76, v200\n"
      "v_dot2c_f32_bf16 v193, v76, v201\n"
      "v_dot2c_f32_bf16 v194, v77, v200\n"
      "v_dot2c_f32_bf16 v195, v77, v201\n"
      "v_dot2c_f32_bf16 v196, v78, v200\n"
      "v_dot2c_f32_bf16 v197, v78, v201\n"
      "v_dot2c_f32_bf16 v198, v79, v200\n"
      "v_dot2c_f32_bf16 v199, v79, v201\n"
      "s_branch L_P_RE\n"
      "L_OA_BAD:\n"
      "s_cmp_lg_u32 s1, 0\n"
      "s_cbranch_scc1 L_OA_OK\n"
      "s_mov_b32 s1, 1\n"
      "s_waitcnt vmcnt(0) lgkmcnt(0)\n"
      "s_add_u32 s34, s16, 3\n"
      "s_mov_b32 s51, 0x40000\n"
      "s_mov_b64 exec, 0xffff\n"
      "L_wl_54:\n"
      "v_mov_b64 v[28:29], 0\n"
      "buffer_atomic_or_x2 v[28:29], v7, s[4:7], s82 offen offset:128 sc0\n"
      "s_waitcnt vmcnt(0)\n"
      "s_sub_u32 s51, s51, 1\n"
      "s_cbranch_scc1 L_wd_55\n"
      "v_subrev_u32 v28, s34, v28\n"
      "v_cmp_eq_u32 s[40:41], s17, v29\n"
      "v_cmp_gt_u32 vcc, 64, v28\n"
      "s_and_b64 vcc, vcc, s[40:41]\n"
      "s_cmp_eq_u64 vcc, exec\n"
      "s_cbranch_scc0 L_wl_54\n"
      "L_wd_55:\n"
      "s_mov_b64 exec, -1\n"
      "v_mov_b64 v[32:33], v[192:193]\n"
      "v_mov_b64 v[34:35], v[194:195]\n"
      "v_mov_b64 v[36:37], v[196:197]\n"
      "v_mov_b64 v[38:39], v[198:199]\n"
      "v_mov_b64 v[40:41], v[200:201]\n"
      "v_mov_b64 v[42:43], v[202:203]\n"
      "v_mov_b64 v[44:45], v[204:205]\n"
      "v_mov_b64 v[46:47], v[206:207]\n"
      "s_branch L_OA_RE\n"
      "L_XB_BAD:\n"
      "s_cmp_lg_u32 s1, 0\n"
      "s_cbranch_scc1 L_XB_OK\n"
      "s_mov_b32 s1, 1\n"
      "s_waitcnt vmcnt(0) lgkmcnt(0)\n"
      "s_add_u32 s34, s16, 4\n"
      "s_mov_b32 s51, 0x40000\n"
      "s_mov_b64 exec, 0xffff\n"
      "L_wl_56:\n"
      "v_mov_b64 v[28:29], 0\n"
      "buffer_atomic_or_x2 v[28:29], v7, s[4:7], s82 offen offset:128 sc0\n"
      "s_waitcnt vmcnt(0)\n"
      "s_sub_u32 s51, s51, 1\n"
      "s_cbranch_scc1 L_wd_57\n"
      "v_subrev_u32 v28, s34, v28\n"
      "v_cmp_eq_u32 s[40:41], s17, v29\n"
      "v_cmp_gt_u32 vcc, 64, v28\n"
      "s_and_b64 vcc, vcc, s[40:41]\n"
      "s_cmp_eq_u64 vcc, exec\n"
      "s_cbranch_scc0 L_wl_56\n"
      "L_wd_57:\n"
      "s_mov_b64 exec, -1\n"
      "s_sub_u32 s46, s46, 262144\n"
      "s_branch L_XB_RE\n"
      "L_OB_BAD:\n"
      "s_cmp_lg_u32 s1, 0\n"
      "s_cbranch_scc1 L_OB_OK\n"
      "s_mov_b32 s1, 1\n"
      "s_waitcnt vmcnt(0) lgkmcnt(0)\n"
      "s_add_u32 s34, s16, 6\n"
      "s_mov_b32 s51, 0x40000\n"
      "s_mov_b64 exec, 0xffff\n"
      "L_wl_58:\n"
      "v_mov_b64 v[28:29], 0\n"
      "buffer_atomic_or_x2 v[28:29], v7, s[4:7], s82 offen offset:128 sc0\n"
      "s_waitcnt vmcnt(0)\n"
      "s_sub_u32 s51, s51, 1\n"
      "s_cbranch_scc1 L_wd_59\n"
      "v_subrev_u32 v28, s34, v28\n"
      "v_cmp_eq_u32 s[40:41], s17, v29\n"
      "v_cmp_gt_u32 vcc, 64, v28\n"
      "s_and_b64 vcc, vcc, s[40:41]\n"
      "s_cmp_eq_u64 vcc, exec\n"
      "s_cbranch_scc0 L_wl_58\n"
      "L_wd_59:\n"
      "s_mov_b64 exec, -1\n"
      "v_mov_b64 v[32:33], v[192:193]\n"
      "v_mov_b64 v[34:35], v[194:195]\n"
      "v_mov_b64 v[36:37], v[196:197]\n"
      "v_mov_b64 v[38:39], v[198:199]\n"
      "v_mov_b64 v[40:41], v[200:201]\n"
      "v_mov_b64 v[42:43], v[202:203]\n"
      "v_mov_b64 v[44:45], v[204:205]\n"
      "v_mov_b64 v[46:47], v[206:207]\n"
      "s_branch L_OB_RE\n"
      :
      : "{s[0:1]}"(__builtin_amdgcn_kernarg_segment_ptr()), "{s2}"(blockIdx.x), "{v0}"(threadIdx.x)
      : "v1", "v2", "v3", "v4", "v5", "v6", "v7", "v8", "v9", "v10", "v11", "v12", "v13", "v14", "v15", "v16", "v17", "v18", "v19", "v20", "v21", "v22", "v23", "v24", "v25", "v26", "v27", "v28", "v29", "v30", "v31", "v32", "v33", "v34", "v35", "v36", "v37", "v38", "v39", "v40", "v41", "v42", "v43", "v44", "v45", "v46", "v47", "v48", "v49", "v50", "v51", "v52", "v53", "v54", "v55", "v56", "v57", "v58", "v59", "v60", "v61", "v62", "v63", "v64", "v65", "v66", "v67", "v68", "v69", "v70", "v71", "v72", "v73", "v74", "v75", "v76", "v77", "v78", "v79", "v80", "v81", "v82", "v83", "v84", "v85", "v86", "v87", "v88", "v89", "v90", "v91", "v92", "v93", "v94", "v95", "v96", "v97", "v98", "v99", "v100", "v101", "v102", "v103", "v104", "v105", "v106", "v107", "v108", "v109", "v110", "v111", "v112", "v113", "v114", "v115", "v116", "v117", "v118", "v119", "v120", "v121", "v122", "v123", "v124", "v125", "v126", "v127", "v128", "v129", "v130", "v131", "v132", "v133", "v134", "v135", "v136", "v137", "v138", "v139", "v140", "v141", "v142", "v143", "v144", "v145", "v146", "v147", "v148", "v149", "v150", "v151", "v152", "v153", "v154", "v155", "v156", "v157", "v158", "v159", "v160", "v161", "v162", "v163", "v164", "v165", "v166", "v167", "v168", "v169", "v170", "v171", "v172", "v173", "v174", "v175", "v176", "v177", "v178", "v179", "v180", "v181", "v182", "v183", "v184", "v185", "v186", "v187", "v188", "v189", "v190", "v191", "v192", "v193", "v194", "v195", "v196", "v197", "v198", "v199", "v200", "v201", "v202", "v203", "v204", "v205", "v206", "v207", "v208", "v209", "v210", "v211", "v212", "v213", "v214", "v215", "v216", "v217", "v218", "v219", "v220", "v221", "v222", "v223", "v224", "v225", "v226", "v227", "v228", "v229", "v230", "v231", "v232", "v233", "v234", "v235", "v236", "v237", "v238", "v239", "v240", "v241", "v242", "v243", "v244", "v245", "v246", "v247", "v248", "v249", "v250", "v251", "v252", "v253", "v254", "v255", "a0", "a1", "a2", "a3", "a4", "a5", "a6", "a7", "a8", "a9", "a10", "a11", "a12", "a13", "a14", "a15", "a16", "a17", "a18", "a19", "a20", "a21", "a22", "a23", "a24", "a25", "a26", "a27", "a28", "a29", "a30", "a31", "a32", "a33", "a34", "a35", "a36", "a37", "a38", "a39", "a40", "a41", "a42", "a43", "a44", "a45", "a46", "a47", "a48", "a49", "a50", "a51", "a52", "a53", "a54", "a55", "a56", "a57", "a58", "a59", "a60", "a61", "a62", "a63", "a64", "a65", "a66", "a67", "a68", "a69", "a70", "a71", "a72", "a73", "a74", "a75", "a76", "a77", "a78", "a79", "a80", "a81", "a82", "a83", "a84", "a85", "a86", "a87", "a88", "a89", "a90", "a91", "a92", "a93", "a94", "a95", "a96", "a97", "a98", "a99", "a100", "a101", "a102", "a103", "a104", "a105", "a106", "a107", "a108", "a109", "a110", "a111", "a112", "a113", "a114", "a115", "a116", "a117", "a118", "a119", "a120", "a121", "a122", "a123", "a124", "a125", "a126", "a127", "a128", "a129", "a130", "a131", "a132", "a133", "a134", "a135", "a136", "a137", "a138", "a139", "a140", "a141", "a142", "a143", "a144", "a145", "a146", "a147", "a148", "a149", "a150", "a151", "a152", "a153", "a154", "a155", "a156", "a157", "a158", "a159", "a160", "a161", "a162", "a163", "a164", "a165", "a166", "a167", "a168", "a169", "a170", "a171", "a172", "a173", "a174", "a175", "a176", "a177", "a178", "a179", "a180", "a181", "a182", "a183", "a184", "a185", "a186", "a187", "a188", "a189", "a190", "a191", "a192", "a193", "a194", "a195", "a196", "a197", "a198", "a199", "a200", "a201", "a202", "a203", "a204", "a205", "a206", "a207", "a208", "a209", "a210", "a211", "a212", "a213", "a214", "a215", "a216", "a217", "a218", "a219", "a220", "a221", "a222", "a223", "a224", "a225", "a226", "a227", "a228", "a229", "a230", "a231", "a232", "a233", "a234", "a235", "a236", "a237", "a238", "a239", "a240", "a241", "a242", "a243", "a244", "a245", "a246", "a247", "a248", "a249", "a250", "a251", "a252", "a253", "a254", "a255", "s3", "s4", "s5", "s6", "s7", "s8", "s9", "s10", "s11", "s12", "s13", "s14", "s15", "s16", "s17", "s18", "s19", "s20", "s21", "s22", "s23", "s24", "s25", "s26", "s27", "s28", "s29", "s30", "s31", "s33", "s34", "s35", "s36", "s37", "s38", "s39", "s40", "s41", "s42", "s43", "s44", "s45", "s46", "s47", "s48", "s49", "s50", "s51", "s52", "s53", "s54", "s55", "s56", "s57", "s58", "s59", "s60", "s61", "s62", "s63", "s64", "s65", "s66", "s67", "s68", "s69", "s70", "s71", "s72", "s73", "s74", "s75", "s76", "s77", "s78", "s79", "s80", "s81", "s82", "s83", "s84", "s85", "s86", "s87", "s88", "s89", "s90", "s91", "s92", "s93", "s94", "s95", "s96", "s97", "s98", "s99", "vcc", "memory");
}

extern "C" void kernel_launch(void* const* d_in, const int* in_sizes, int n_in, void* d_out, int out_size, void* d_ws, size_t ws_size,
                              hipStream_t stream) {
  Params p;
  p.x0 = d_in[0]; p.wa = d_in[1]; p.woa = d_in[2]; p.wb = d_in[4]; p.wob = d_in[5]; p.out = d_out; p.ws = d_ws;
  (void)hipFuncSetAttribute((const void*)fused_kernel, hipFuncAttributeMaxDynamicSharedMemorySize, 161920);
  hipLaunchKernelGGL(fused_kernel, dim3(256), dim3(256), 161920, stream, p);
}
```
